```python
import torch

def _softmax(x, axis=-1):
    return torch.softmax(x, dim=axis)

def _gelu(x):
    return torch.nn.functional.gelu(x, approximate='tanh')

def _silu(x):
    return torch.nn.functional.silu(x)

def _sigmoid(x):
    return torch.sigmoid(x)

def _layernorm(x, axis=-1, eps=1e-05):
    m = x.mean(dim=axis, keepdim=True)
    v = x.var(dim=axis, keepdim=True, unbiased=False)
    return (x - m) / torch.sqrt(v + eps)

def _scatter_add(tgt, idx, src, axis=0):
    return tgt.clone().index_add_(axis, idx, src)

def _segment_sum(src, seg, n):
    out = torch.zeros((n,) + src.shape[1:], dtype=src.dtype)
    return out.index_add_(0, seg, src)

def _pad(x, pw):
    flat = tuple((v for pair in reversed(pw) for v in pair))
    return torch.nn.functional.pad(x, flat)
kernel_name = 'sequential_preLN__memory_aug_recurrent_mem_tr__glu_gated_bilinear__prompt'

def setup_inputs():

    _w = lambda a: (lambda t: t.to(torch.bfloat16) if t.is_floating_point() else t)(torch.as_tensor(a))

    def _A_setup():
        B, S, Dm = (1, 2048, 448)
        H, Dh = (16, 256)
        M, seg = (8, 32)
        seg = min(seg, S)
        S = S // seg * seg
        return {'x': _w((lambda *s: torch.randn(*s, dtype=torch.bfloat16) * 0.05)(B, S, Dm)), 'mem0': _w((lambda *s: torch.randn(*s, dtype=torch.bfloat16) * 0.2)(B, M, Dm)), 'Wq': _w((lambda *s: torch.randn(*s, dtype=torch.bfloat16) * 1.6)(Dm, H, Dh) * Dm ** (-0.5)), 'Wk': _w((lambda *s: torch.randn(*s, dtype=torch.bfloat16) * 1.6)(Dm, H, Dh) * Dm ** (-0.5)), 'Wv': _w((lambda *s: torch.randn(*s, dtype=torch.bfloat16) * 0.2)(Dm, H, Dh) * Dm ** (-0.5)), 'Wo': _w((lambda *s: torch.randn(*s, dtype=torch.bfloat16) * 0.2)(H, Dh, Dm) * (H * Dh) ** (-0.5)), 'seg_len': seg}
    _A_in = _A_setup()

    def _B_setup():
        B, S, D, F = (1, 2048, 448, 1792)
        return {'x': _w((lambda *s: torch.randn(*s, dtype=torch.bfloat16) * 0.05)(B, S, D)), 'w_gate': _w((lambda *s: torch.randn(*s, dtype=torch.bfloat16) * 0.2)(D, F) * D ** (-0.5)), 'w_up': _w((lambda *s: torch.randn(*s, dtype=torch.bfloat16) * 0.2)(D, F) * D ** (-0.5)), 'w_down': _w((lambda *s: torch.randn(*s, dtype=torch.bfloat16) * 0.2)(F, D) * F ** (-0.5))}
    _B_in = _B_setup()
    return {'A_x': _A_in['x'], 'A_mem0': _A_in['mem0'], 'A_Wq': _A_in['Wq'], 'A_Wk': _A_in['Wk'], 'A_Wv': _A_in['Wv'], 'A_Wo': _A_in['Wo'], 'A_seg_len': _A_in['seg_len'], 'B_x': _B_in['x'], 'B_w_gate': _B_in['w_gate'], 'B_w_up': _B_in['w_up'], 'B_w_down': _B_in['w_down']}

def reference(A_x, A_mem0, A_Wq, A_Wk, A_Wv, A_Wo, A_seg_len, B_x, B_w_gate, B_w_up, B_w_down):

    def _A_ref(x, mem0, Wq, Wk, Wv, Wo, seg_len):
        B, S, Dm = x.shape
        H, Dh = (Wq.shape[1], Wq.shape[2])
        M = mem0.shape[1]
        scale = Dh ** (-0.5)
        n_seg = S // seg_len
        L = M + seg_len
        idx = torch.arange(L)
        mask = (idx[:, None] < M) | (idx[:, None] >= idx[None, :])
        mem = mem0
        outs = []
        for s in range(n_seg):
            xs = x[:, s * seg_len:(s + 1) * seg_len, :].clone()
            h = torch.concatenate([mem, xs], axis=1)
            q = torch.einsum('btd,dhe->bhte', h, Wq)
            k = torch.einsum('btd,dhe->bhte', h, Wk)
            v = torch.einsum('btd,dhe->bhte', h, Wv)
            scores = torch.einsum('bhqe,bhke->bhqk', q, k) * scale
            scores = torch.where(mask[None, None], scores, -1000000000.0)
            p = _softmax(scores, axis=-1)
            o = torch.einsum('bhqk,bhke->bhqe', p, v)
            o = torch.einsum('bhte,hed->btd', o, Wo)
            mem = o[:, :M, :].clone()
            outs.append(o[:, M:, :])
        return torch.concatenate(outs, axis=1)

    def _B_ref(x, w_gate, w_up, w_down):
        g = torch.einsum('bsd,df->bsf', x, w_gate)
        u = torch.einsum('bsd,df->bsf', x, w_up)
        h = g * u
        return torch.einsum('bsf,fd->bsd', h, w_down)
    x = A_x
    h = x + _A_ref(_layernorm(x, axis=-1), A_mem0, A_Wq, A_Wk, A_Wv, A_Wo, A_seg_len)
    h2 = h + _B_ref(_layernorm(h, axis=-1), B_w_gate, B_w_up, B_w_down)
    return h2
```

```cpp
#include <hip/hip_runtime.h>
#include <hip/hip_bf16.h>
#include <stdint.h>

typedef __bf16 bf16x8 __attribute__((ext_vector_type(8)));
typedef __bf16 bf16x16 __attribute__((ext_vector_type(16)));
typedef __bf16 bf16x2 __attribute__((ext_vector_type(2)));
typedef float f32x4 __attribute__((ext_vector_type(4)));
typedef unsigned u32x4 __attribute__((ext_vector_type(4)));
typedef unsigned u32x2 __attribute__((ext_vector_type(2)));
typedef unsigned u32x8 __attribute__((ext_vector_type(8)));
typedef short s16x4 __attribute__((ext_vector_type(4)));
#define LAS __attribute__((address_space(3)))
#define DI __device__ __forceinline__

constexpr int DM = 448, NH = 16, HD = 256, NSEG = 64, SEG = 32, SEQ = 2048, NTOT = 14336, NKT = 7168, FF = 1792;
constexpr int MEMIMG_B = 7168;
constexpr int XM_OFF = 0, XS_OFF = 8192, XA_OFF = 9216, XCH_B = 9216 + 28 * 8192;
constexpr int KVM_B = NH * 8 * DM * 2;

constexpr int KR = 5, PRE0 = 4, XW = 1;
constexpr int D2A = 11, D2B = 6, D2N = 16, DM0 = 10, DM7 = 16, DH = 30;

constexpr int L_M = 0, L_T = 7168, T_STR = 640, L_P = L_T + 8 * T_STR, P_STR = 1056, L_WB = L_P + 8 * P_STR + 64;
constexpr int L_WB_AL = (L_WB + 255) & ~255;
constexpr int CHAIN_LDS = L_WB_AL + 7 * (14 - KR) * 2048;

constexpr size_t WS_MEMIMG = 0;
constexpr size_t WS_XCH = WS_MEMIMG + (size_t)(NSEG + 1) * MEMIMG_B;
constexpr size_t WS_SENT_END = WS_XCH + (size_t)NSEG * XCH_B;
constexpr size_t WS_XF = (WS_SENT_END + 4095) & ~(size_t)4095;
constexpr size_t WS_WT = WS_XF + (size_t)SEQ * DM * 2;
constexpr size_t WS_KT = WS_WT + (size_t)NTOT * DM * 2;
constexpr size_t WS_UT = WS_KT + (size_t)SEQ * NKT * 2;
constexpr size_t WS_KTM = WS_UT + (size_t)SEQ * NKT * 2;
constexpr size_t WS_VTM = WS_KTM + (size_t)NSEG * KVM_B;
constexpr size_t WS_PO = WS_VTM + (size_t)NSEG * KVM_B;
constexpr size_t WS_H = WS_PO + (size_t)4 * SEQ * DM * 4;
constexpr size_t WS_HN = WS_H + (size_t)SEQ * DM * 4;
constexpr size_t WS_WG = WS_HN + (size_t)SEQ * DM * 2;
constexpr size_t WS_WD = WS_WG + (size_t)2 * FF * DM * 2;
constexpr size_t WS_HG = WS_WD + (size_t)FF * DM * 2;
constexpr size_t WS_END = WS_HG + (size_t)SEQ * FF * 2;

struct Params {
  const uint16_t* x; const uint16_t* mem0; const uint16_t* Wq; const uint16_t* Wk; const uint16_t* Wv; const uint16_t* Wo;
  const uint16_t* wg; const uint16_t* wu; const uint16_t* wd; uint16_t* out; char* ws;
};

DI unsigned cvt_pk(float a, float b) { bf16x2 r = {(__bf16)a, (__bf16)b}; return __builtin_bit_cast(unsigned, r); }
DI void wg_barrier() { asm volatile("s_waitcnt lgkmcnt(0)\n\ts_barrier" ::: "memory"); }
DI unsigned umax3(unsigned a, unsigned b, unsigned c) { return max(max(a, b), c); }
DI float fmax3(float a, float b, float c) { return fmaxf(fmaxf(a, b), c); }
DI float u2f(unsigned u) { return __builtin_bit_cast(float, u); }
DI unsigned f2u(float f) { return __builtin_bit_cast(unsigned, f); }
DI float blo(unsigned u) { return u2f(u << 16); }
DI float bhi(unsigned u) { return u2f(u & 0xffff0000u); }
template<int CTRL> DI float dpp_f(float x) { return u2f((unsigned)__builtin_amdgcn_update_dpp(0, (int)f2u(x), CTRL, 0xf, 0xf, true)); }
template<int CTRL> DI unsigned dpp_u(unsigned x) { return (unsigned)__builtin_amdgcn_update_dpp(0, (int)x, CTRL, 0xf, 0xf, true); }
DI float x32_max(float x) { auto r = __builtin_amdgcn_permlane32_swap(f2u(x), f2u(x), false, false); return fmaxf(u2f(r[0]), u2f(r[1])); }
DI float x32_sum(float x) { auto r = __builtin_amdgcn_permlane32_swap(f2u(x), f2u(x), false, false); return u2f(r[0]) + u2f(r[1]); }
DI float x16_max(float x) { auto r = __builtin_amdgcn_permlane16_swap(f2u(x), f2u(x), false, false); return fmaxf(u2f(r[0]), u2f(r[1])); }
DI float x16_sum(float x) { auto r = __builtin_amdgcn_permlane16_swap(f2u(x), f2u(x), false, false); return u2f(r[0]) + u2f(r[1]); }
DI float wave_sum(float v) {
  v += dpp_f<0xB1>(v); v += dpp_f<0x4E>(v); v += dpp_f<0x124>(v); v += dpp_f<0x128>(v);
  v = x16_sum(v); v = x32_sum(v); return v;
}
#define SMFMAC(a, b, c, idx) __builtin_amdgcn_smfmac_f32_16x16x64_bf16((a), (b), (c), (idx), 0, 0)
#define MFMA16(a, b, c) __builtin_amdgcn_mfma_f32_16x16x32_bf16((a), (b), (c), 0, 0, 0)
constexpr unsigned OOB = 0x7fffffffu;
#define LD16(r, off) __builtin_amdgcn_raw_buffer_load_b128((r), (off), 0, 16)
#define LD8(r, off) __builtin_amdgcn_raw_buffer_load_b64((r), (off), 0, 16)
#define CBAR() asm volatile("" ::: "memory")
#define SBAR() __builtin_amdgcn_sched_barrier(0)

DI u32x4 ln_row(float (&v)[8], bool act) {
  float s = 0.f;
#pragma unroll
  for (int j = 0; j < 8; ++j) s += v[j];
  s = wave_sum(act ? s : 0.f);
  const float mean = s * (1.0f / DM);
  float q = 0.f;
#pragma unroll
  for (int j = 0; j < 8; ++j) { float d = v[j] - mean; q += d * d; }
  q = wave_sum(act ? q : 0.f);
  const float rstd = 1.0f / sqrtf(q * (1.0f / DM) + 1e-5f);
  u32x4 r;
#pragma unroll
  for (int j = 0; j < 4; ++j) r[j] = cvt_pk((v[2 * j] - mean) * rstd, (v[2 * j + 1] - mean) * rstd);
  return r;
}
DI unsigned tiled_off(int row, int c8) { return (unsigned)((((row >> 4) * 14 + (c8 >> 2)) * 64 + (c8 & 3) * 16 + (row & 15)) * 16); }

__global__ void __launch_bounds__(256) k_prep(Params p) {
  const int tid = threadIdx.x, l = tid & 63, w = tid >> 6;
  const int nb = gridDim.x, b = blockIdx.x;
  for (int row = b * 4 + w; row < SEQ; row += nb * 4) {
    const bool act = l < 56;
    float v[8];
    u32x4 t = act ? *(const u32x4*)(p.x + (size_t)row * DM + l * 8) : (u32x4){0, 0, 0, 0};
#pragma unroll
    for (int j = 0; j < 4; ++j) { v[2 * j] = blo(t[j]); v[2 * j + 1] = bhi(t[j]); }
    u32x4 r = ln_row(v, act);
    if (act) *(u32x4*)(p.ws + WS_XF + tiled_off(row, l)) = r;
  }
  {
    u32x4* d = (u32x4*)(p.ws + MEMIMG_B);
    const size_t n = (WS_SENT_END - MEMIMG_B) / 16;
    for (size_t i = (size_t)b * 256 + tid; i < n; i += (size_t)nb * 256) d[i] = (u32x4){~0u, ~0u, ~0u, ~0u};
  }
  if (b == 0) {
    for (int c = tid; c < 448; c += 256) {
      const int row = c & 7, kg = c >> 3;
      u32x4 t = *(const u32x4*)(p.mem0 + row * DM + kg * 8);
      t.x = min(t.x, 0xfffffffeu); t.y = min(t.y, 0xfffffffeu); t.z = min(t.z, 0xfffffffeu); t.w = min(t.w, 0xfffffffeu);
      *(u32x4*)(p.ws + WS_MEMIMG + c * 16) = t;
    }
  }
  {
    const int nG = 112 * 2 * 14 * 64, nD = 28 * 56 * 64;
    for (int i = b * 256 + tid; i < nG + nD; i += nb * 256) {
      const uint16_t* src; int stride; u32x4* dst;
      if (i < nG) {
        const int ln = i & 63, ks = (i >> 6) % 14, gu = (i / (64 * 14)) & 1, ft = i / (64 * 14 * 2);
        src = (gu ? p.wu : p.wg) + (size_t)(32 * ks + 8 * (ln >> 4)) * FF + ft * 16 + (ln & 15); stride = FF;
        dst = (u32x4*)(p.ws + WS_WG) + i;
      } else {
        const int j = i - nG, ln = j & 63, kf = (j >> 6) % 56, o = j / (64 * 56);
        src = p.wd + (size_t)(32 * kf + 8 * (ln >> 4)) * DM + o * 16 + (ln & 15); stride = DM;
        dst = (u32x4*)(p.ws + WS_WD) + j;
      }
      u32x4 r;
#pragma unroll
      for (int q = 0; q < 4; ++q) r[q] = (unsigned)src[(2 * q) * stride] | ((unsigned)src[(2 * q + 1) * stride] << 16);
      *dst = r;
    }
  }
}

__global__ void __launch_bounds__(256) k_wt(Params p) {
  __shared__ __attribute__((aligned(16))) char sm[4 * 32 * 144];
  const int l = threadIdx.x & 63, w = threadIdx.x >> 6, n16 = l & 15, g = l >> 4;
  const int task = blockIdx.x * 4 + w;
  if (task >= 32 * 49) return;
  const int mat = task / 49, t = task % 49, te = t / 7, tn = t % 7;
  const int isB = mat >> 4, h = mat & 15;
  const uint16_t* Q = (isB ? p.Wv : p.Wk) + (size_t)(te * 64 + n16) * (NH * HD) + h * HD + 8 * g;
  const uint16_t* P = p.Wq + (size_t)(tn * 64 + n16) * (NH * HD) + h * HD + 8 * g;
  const uint16_t* Wo = p.Wo + (size_t)h * HD * DM + tn * 64;
  LAS char* my = (LAS char*)sm + w * 32 * 144;
  f32x4 acc[4][4];
#pragma unroll
  for (int a = 0; a < 4; ++a)
#pragma unroll
    for (int c = 0; c < 4; ++c) acc[a][c] = (f32x4){0, 0, 0, 0};
  for (int ks = 0; ks < 8; ++ks) {
    bf16x8 qa[4], pb[4];
#pragma unroll
    for (int a = 0; a < 4; ++a) qa[a] = *(const bf16x8*)(Q + (size_t)a * 16 * (NH * HD) + ks * 32);
    if (!isB) {
#pragma unroll
      for (int c = 0; c < 4; ++c) pb[c] = *(const bf16x8*)(P + (size_t)c * 16 * (NH * HD) + ks * 32);
    } else {
#pragma unroll
      for (int i = 0; i < 4; ++i) {
        const int f = i * 8 + (l >> 3);
        u32x4 v = *(const u32x4*)(Wo + (size_t)(ks * 32 + f) * DM + (l & 7) * 8);
        *(LAS u32x4*)(my + f * 144 + (l & 7) * 16) = v;
      }
#pragma unroll
      for (int c = 0; c < 4; ++c) {
        LAS char* bb = my + (8 * g + (n16 >> 2)) * 144 + c * 32 + (n16 & 3) * 8;
        s16x4 lo = __builtin_amdgcn_ds_read_tr16_b64_v4i16((LAS s16x4*)bb);
        s16x4 hi = __builtin_amdgcn_ds_read_tr16_b64_v4i16((LAS s16x4*)(bb + 4 * 144));
        u32x2 l2 = __builtin_bit_cast(u32x2, lo), h2 = __builtin_bit_cast(u32x2, hi);
        pb[c] = __builtin_bit_cast(bf16x8, (u32x4){l2.x, l2.y, h2.x, h2.y});
      }
    }
#pragma unroll
    for (int a = 0; a < 4; ++a)
#pragma unroll
      for (int c = 0; c < 4; ++c) acc[a][c] = MFMA16(qa[a], pb[c], acc[a][c]);
  }
  const float sc = isB ? 1.0f : 0.0625f * 1.4426950408889634f;
  uint16_t* WT = (uint16_t*)(p.ws + WS_WT) + (size_t)(isB * NKT + h * DM + tn * 64 + n16) * DM + te * 64 + 4 * g;
#pragma unroll
  for (int c = 0; c < 4; ++c)
#pragma unroll
    for (int a = 0; a < 4; ++a) {
      u32x2 v = {cvt_pk(acc[a][c][0] * sc, acc[a][c][1] * sc), cvt_pk(acc[a][c][2] * sc, acc[a][c][3] * sc)};
      *(u32x2*)(WT + (size_t)c * 16 * DM + a * 16) = v;
    }
}

DI void xgemm_wg(const Params& p, LAS char* lds, int nb, int seg0, int step, int w, int l) {
  const int n16 = l & 15, g = l >> 4;
  const int n0 = nb * 256 + w * 32;
  const bool isU = n0 >= NKT;
  const uint16_t* WT = (const uint16_t*)(p.ws + WS_WT);
  const int r0 = isU ? n0 + n16 : n0 + 8 * (n16 >> 2) + (n16 & 3);
  const int r1 = isU ? r0 + 16 : r0 + 4;
  bf16x8 w0[14], w1[14];
#pragma unroll
  for (int ks = 0; ks < 14; ++ks) {
    w0[ks] = *(const bf16x8*)(WT + (size_t)r0 * DM + ks * 32 + 8 * g);
    w1[ks] = *(const bf16x8*)(WT + (size_t)r1 * DM + ks * 32 + 8 * g);
  }
  const int nn = isU ? n0 - NKT : n0;
  const int hh = nn / DM, cc = nn % DM;
  const int tid = w * 64 + l;
  const u32x4* XF = (const u32x4*)(p.ws + WS_XF);
  u32x4 pre[4];
  auto gload = [&](int s) {
#pragma unroll
    for (int i = 0; i < 4; ++i) { const int c = i * 512 + tid; if (c < 1792) pre[i] = XF[(size_t)s * 1792 + c]; }
  };
  gload(seg0);
  int buf = 0;
  for (int s = seg0; s < NSEG; s += step) {
    LAS char* L = lds + buf * 28672;
#pragma unroll
    for (int i = 0; i < 4; ++i) { const int c = i * 512 + tid; if (c < 1792) *(LAS u32x4*)(L + c * 16) = pre[i]; }
    if (s + step < NSEG) gload(s + step);
    wg_barrier();
    f32x4 a00 = {0, 0, 0, 0}, a01 = a00, a10 = a00, a11 = a00;
#pragma unroll
    for (int ks = 0; ks < 14; ++ks) {
      bf16x8 x0 = *(LAS bf16x8*)(L + ks * 1024 + l * 16), x1 = *(LAS bf16x8*)(L + (14 + ks) * 1024 + l * 16);
      if (isU) { a00 = MFMA16(x0, w0[ks], a00); a01 = MFMA16(x1, w0[ks], a01); a10 = MFMA16(x0, w1[ks], a10); a11 = MFMA16(x1, w1[ks], a11); }
      else     { a00 = MFMA16(w0[ks], x0, a00); a01 = MFMA16(w1[ks], x0, a01); a10 = MFMA16(w0[ks], x1, a10); a11 = MFMA16(w1[ks], x1, a11); }
    }
    u32x4 o0 = {cvt_pk(a00[0], a00[1]), cvt_pk(a00[2], a00[3]), cvt_pk(a01[0], a01[1]), cvt_pk(a01[2], a01[3])};
    u32x4 o1 = {cvt_pk(a10[0], a10[1]), cvt_pk(a10[2], a10[3]), cvt_pk(a11[0], a11[1]), cvt_pk(a11[2], a11[3])};
    if (isU) {
      u32x4* d = (u32x4*)(p.ws + WS_UT) + ((size_t)(s * NH + hh) * 28 + (cc >> 4)) * 64 + l;
      d[0] = o0; d[64] = o1;
    } else {
      u32x4* d = (u32x4*)(p.ws + WS_KT) + ((size_t)(s * NH + hh) * 28 + (cc >> 5)) * 64 + l;
      d[0] = o0; d[14 * 64] = o1;
    }
    buf ^= 1;
  }
}
__global__ void __launch_bounds__(512, 2) k_xgemm(Params p) {
  extern __shared__ __attribute__((aligned(16))) char smem[];
  const int w = __builtin_amdgcn_readfirstlane(threadIdx.x >> 6), l = threadIdx.x & 63;
  xgemm_wg(p, (LAS char*)smem, blockIdx.x % 56, blockIdx.x / 56, 4, w, l);
}

DI bf16x16 load_wfrag(const uint16_t* ra, const uint16_t* rb, int ks, int g) {
  u32x2 alo = *(const u32x2*)(ra + 32 * ks + 4 * g), ahi = *(const u32x2*)(ra + 32 * ks + 16 + 4 * g);
  u32x2 blo_ = *(const u32x2*)(rb + 32 * ks + 4 * g), bhi_ = *(const u32x2*)(rb + 32 * ks + 16 + 4 * g);
  u32x8 r = {alo.x, blo_.x, alo.y, blo_.y, ahi.x, bhi_.x, ahi.y, bhi_.y};
  return __builtin_bit_cast(bf16x16, r);
}
DI f32x4 comb32(f32x4 a, f32x4 b) {
  f32x4 r;
#pragma unroll
  for (int i = 0; i < 4; ++i) { float fa = a[i], fb = b[i]; auto t = __builtin_amdgcn_permlane32_swap(f2u(fa), f2u(fb), false, false); r[i] = u2f(t[0]) + u2f(t[1]); }
  return r;
}
DI bf16x16 lds_wfrag(LAS char* d) { u32x4 t0 = *(LAS u32x4*)d, t1 = *(LAS u32x4*)(d + 1024);
  u32x8 t = {t0.x, t0.y, t0.z, t0.w, t1.x, t1.y, t1.z, t1.w}; return __builtin_bit_cast(bf16x16, t); }
DI unsigned mx4(u32x4 v) { return max(umax3(v.x, v.y, v.z), v.w); }
DI bool isbad(unsigned m) { return __builtin_amdgcn_ballot_w64(m == 0xffffffffu) != 0; }

template<bool W7, bool OUT>
DI void chain_wave(const Params& p, const int cw, LAS char* lds, const int w, const int l) {
  constexpr int KRW = W7 ? 14 : KR;
  constexpr int NL = 14 - KRW;
  constexpr int PRE = W7 ? 0 : PRE0;
  const int n16 = l & 15, g = l >> 4, tid = w * 64 + l;
  const int h = cw >> 1, c = cw & 1, o = cw;
  const int idx = n16 < 8 ? 0x4444 : 0xEEEE;
  const uint16_t* WT = (const uint16_t*)(p.ws + WS_WT);
  const __amdgpu_buffer_rsrc_t rmem = __builtin_amdgcn_make_buffer_rsrc(p.ws + WS_MEMIMG, 0, (NSEG + 1) * MEMIMG_B, 0x00020000);
  const __amdgpu_buffer_rsrc_t rx = __builtin_amdgcn_make_buffer_rsrc(p.ws + WS_XCH, 0, NSEG * XCH_B, 0x00020000);
  const __amdgpu_buffer_rsrc_t rktm = __builtin_amdgcn_make_buffer_rsrc(p.ws + WS_KTM, 0, NSEG * KVM_B, 0x00020000);
  const __amdgpu_buffer_rsrc_t rvtm = __builtin_amdgcn_make_buffer_rsrc(p.ws + WS_VTM, 0, NSEG * KVM_B, 0x00020000);
  bf16x16 wa[W7 ? 1 : 14];
  bf16x16 wb[OUT ? KRW : 1];
  if (!W7) {
    const uint16_t* ra = WT + (size_t)(h * DM + (14 * c + 2 * w) * 16 + n16) * DM;
#pragma unroll
    for (int u = 0; u < 14; ++u) { const uint16_t* r = ra + (u & 1) * 16 * DM; wa[u] = load_wfrag(r, r + 224, u >> 1, g); }
  }
  LAS char* wl = lds + L_WB_AL + w * (14 - KR) * 2048 + l * 16;
  if (OUT) {
    const uint16_t* ra = WT + (size_t)(NKT + (2 * w) * DM + 16 * o + n16) * DM;
#pragma unroll
    for (int u = 0; u < 14; ++u) {
      const uint16_t* r = ra + (u & 1) * DM * DM;
      bf16x16 f = load_wfrag(r, r + 224, u >> 1, g);
      if (u < KRW) wb[u] = f;
      else { u32x8 t = __builtin_bit_cast(u32x8, f); LAS char* d = wl + (u - KRW) * 2048;
        *(LAS u32x4*)d = (u32x4){t[0], t[1], t[2], t[3]}; *(LAS u32x4*)(d + 1024) = (u32x4){t[4], t[5], t[6], t[7]}; }
    }
  }
  if (W7) { if (l < T_STR / 16) *(LAS u32x4*)(lds + L_T + 7 * T_STR + l * 16) = (u32x4){0, 0, 0, 0}; }
  unsigned off_mem = tid * 16;
  const int hb = n16 >> 3, i8 = n16 & 7;
  unsigned off_mm = XM_OFF + w * 1024 + (hb * 16 + (g & 1) * 8 + i8) * 16;
  unsigned off_ms = XS_OFF + w * 128 + n16 * 8;
  unsigned off_ax = XA_OFF + (o * 8 + w) * 1024 + l * 16;
  const bool own_m = hb == (g >> 1);
  const bool st_m = n16 < 8 && g < 2;
  unsigned st_mm = st_m ? XM_OFF + (h >> 1) * 1024 + c * 512 + ((h & 1) * 16 + g * 8 + i8) * 16 : OOB;
  const unsigned inc_mm = st_m ? XCH_B : 0;
  const int gq = (l >> 1) & 3;
  unsigned st_mem = ((l & 3) == 0) ? MEMIMG_B + o * 256 + (gq >> 1) * 128 + (l >> 3) * 16 : OOB;
  const unsigned inc_mem = ((l & 3) == 0) ? MEMIMG_B : 0;
  unsigned st_vtm = ((((w >> 1) * 28 + o) * 64 + l) * 8 + (w & 1) * 4) * 2;
  unsigned st_ktm = ((((h >> 1) * 14 + 7 * c + (g >> 1)) * 4 + 2 * hb + (g & 1)) * 16 + (h & 1) * 8 + i8) * 16;
  LAS char* a_rd = lds + L_M + hb * 7 * 512 + g * 128 + i8 * 16;
  int budget = 1 << 20;
  wg_barrier();
  for (int s = 0; s < NSEG; ++s) {
    bf16x16 tw[NL > 0 ? NL : 1];
    if (OUT) {
#pragma unroll
      for (int j = 0; j < PRE; ++j) tw[j] = lds_wfrag(wl + j * 2048);
      SBAR();
    }
    if (!W7) {
      if (!OUT) { __builtin_amdgcn_s_sleep(D2N); } else if (w != 0) { __builtin_amdgcn_s_sleep(D2A); } else { __builtin_amdgcn_s_sleep(D2B); }
      CBAR();
      u32x4 v = LD16(rmem, off_mem);
      while (__builtin_expect(isbad(mx4(v)), 0)) { if (--budget < 0) break; if (!OUT) __builtin_amdgcn_s_sleep(1); CBAR(); v = LD16(rmem, off_mem); }
      *(LAS u32x4*)(lds + L_M + tid * 16) = v;
      off_mem += MEMIMG_B;
    }
    wg_barrier();
    bf16x8 af[7];
#pragma unroll
    for (int k = 0; k < 7; ++k) af[k] = *(LAS bf16x8*)(a_rd + k * 512);
    f32x4 vt1 = {0, 0, 0, 0}, vt2 = {0, 0, 0, 0};
    if (!W7) {
      f32x4 acc1 = {0, 0, 0, 0}, acc2 = {0, 0, 0, 0};
#pragma unroll
      for (int k = 0; k < 7; ++k) { acc1 = SMFMAC(af[k], wa[2 * k], acc1, idx); acc2 = SMFMAC(af[k], wa[2 * k + 1], acc2, idx); }
      f32x4 acc = comb32(acc1, acc2);
      u32x2 pk = {cvt_pk(acc[0], acc[1]), cvt_pk(acc[2], acc[3])};
      *(LAS u32x2*)(lds + L_T + w * T_STR + n16 * 40 + g * 8) = pk;
      wg_barrier();
    } else {
      bf16x8 mb[4];
#pragma unroll
      for (int q = 0; q < 4; ++q) { int ks = min(7 * c + 2 * q + (g >> 1), 13); mb[q] = *(LAS bf16x8*)(lds + L_M + ks * 512 + (2 * hb + (g & 1)) * 128 + i8 * 16); }
      if (OUT) {
#pragma unroll
        for (int k = 0; k < 7; ++k) { vt1 = SMFMAC(af[k], wb[2 * k], vt1, idx); vt2 = SMFMAC(af[k], wb[2 * k + 1], vt2, idx); }
      }
      asm volatile("" : "+v"(vt1), "+v"(vt2));
      SBAR();
      __builtin_amdgcn_s_setprio(3);
      wg_barrier();
      f32x4 d2 = {0, 0, 0, 0}, d3 = {0, 0, 0, 0};
      bf16x8 kt[4];
#pragma unroll
      for (int q = 0; q < 4; ++q) {
        LAS char* b = lds + L_T + (2 * q + (g >> 1)) * T_STR + (8 * (g & 1) + (n16 >> 2)) * 40 + (n16 & 3) * 8;
        s16x4 lo = __builtin_amdgcn_ds_read_tr16_b64_v4i16((LAS s16x4*)b);
        s16x4 hi = __builtin_amdgcn_ds_read_tr16_b64_v4i16((LAS s16x4*)(b + 160));
        u32x2 l2 = __builtin_bit_cast(u32x2, lo), h2 = __builtin_bit_cast(u32x2, hi);
        kt[q] = __builtin_bit_cast(bf16x8, (u32x4){l2.x, l2.y, h2.x, h2.y});
      }
      d2 = MFMA16(kt[0], mb[0], d2); d3 = MFMA16(kt[1], mb[1], d3); d2 = MFMA16(kt[2], mb[2], d2); d3 = MFMA16(kt[3], mb[3], d3);
      u32x4 s2;
#pragma unroll
      for (int r = 0; r < 4; ++r) { float f = d2[r] + d3[r]; auto t = __builtin_amdgcn_permlane32_swap(f2u(f), f2u(f), false, false);
        s2[r] = min(f2u(f + dpp_f<0x128>(u2f(t[1]))), 0xfffffffeu); }
      __builtin_amdgcn_raw_buffer_store_b128(s2, rx, st_mm, 0, 16);
      __builtin_amdgcn_s_setprio(0);
      CBAR();
#pragma unroll
      for (int q = 0; q < 4; ++q) __builtin_amdgcn_raw_buffer_store_b128(__builtin_bit_cast(u32x4, kt[q]), rktm, (q == 3 && g >= 2) ? OOB : st_ktm + q * 2048, 0, 16);
      st_mm += inc_mm; st_ktm += KVM_B;
    }
    if (OUT) {
      if (!W7) {
#pragma unroll
        for (int u = 0; u < 14; ++u) {
          if (u < KRW) { if (u & 1) vt2 = SMFMAC(af[u >> 1], wb[u], vt2, idx); else vt1 = SMFMAC(af[u >> 1], wb[u], vt1, idx);
            if (u >= KRW - XW && PRE + (u - (KRW - XW)) < NL) tw[PRE + (u - (KRW - XW))] = lds_wfrag(wl + (PRE + (u - (KRW - XW))) * 2048); }
          else { if (u & 1) vt2 = SMFMAC(af[u >> 1], tw[u - KRW], vt2, idx); else vt1 = SMFMAC(af[u >> 1], tw[u - KRW], vt1, idx);
            if (u - KRW + PRE + XW < NL) tw[u - KRW + PRE + XW] = lds_wfrag(wl + (u - KRW + PRE + XW) * 2048); }
          SBAR();
        }
      }
      f32x4 vt = comb32(vt1, vt2);
      u32x2 vpk = {cvt_pk(vt[0], vt[1]), cvt_pk(vt[2], vt[3])};
      if (__builtin_expect(s + 1 < NSEG, 1)) {
        bf16x8 va = __builtin_bit_cast(bf16x8, (u32x4){vpk.x, vpk.y, 0u, 0u});
        if (W7) { __builtin_amdgcn_s_sleep(DM7); } else { __builtin_amdgcn_s_sleep(DM0); }
        CBAR();
        u32x4 m0 = LD16(rx, off_mm), m1 = LD16(rx, off_mm + 512), axu = LD16(rx, off_ax);
        u32x2 ms = LD8(rx, off_ms);
        while (__builtin_expect(isbad(max(mx4(m0), mx4(m1))), 0)) { if (--budget < 0) break; CBAR(); m0 = LD16(rx, off_mm); m1 = LD16(rx, off_mm + 512); }
        while (__builtin_expect(isbad(umax3(mx4(axu), ms.x, ms.y)), 0)) { if (--budget < 0) break; CBAR(); axu = LD16(rx, off_ax); ms = LD8(rx, off_ms); }
        off_mm += XCH_B; off_ms += XCH_B; off_ax += XCH_B;
        const float mx = u2f(ms.x), sumx = u2f(ms.y);
        f32x4 mv = __builtin_bit_cast(f32x4, m0) + __builtin_bit_cast(f32x4, m1);
        float mm = fmaxf(fmax3(mv[0], mv[1], mv[2]), fmaxf(mv[3], mx));
        mm = x16_max(mm);
        float em[4];
#pragma unroll
        for (int r = 0; r < 4; ++r) em[r] = __builtin_amdgcn_exp2f(mv[r] - mm);
        float fx = __builtin_amdgcn_exp2f(mx - mm);
        unsigned pm0 = cvt_pk(em[0], em[1]), pm1 = cvt_pk(em[2], em[3]);
        bf16x8 pm = __builtin_bit_cast(bf16x8, (u32x4){own_m ? pm0 : 0u, own_m ? pm1 : 0u, 0u, 0u});
        f32x4 axs = __builtin_bit_cast(f32x4, axu) * fx;
        f32x4 am = MFMA16(va, pm, axs);
        float summ = x16_sum((em[0] + em[1]) + (em[2] + em[3]));
        float rinv = __builtin_amdgcn_rcpf(fx * sumx + summ);
        f32x4 r4;
#pragma unroll
        for (int r = 0; r < 4; ++r) { float t = am[r] * rinv; r4[r] = t + dpp_f<0x128>(t); }
        *(LAS f32x4*)(lds + L_P + w * P_STR + n16 * 64 + g * 16) = r4;
        __builtin_amdgcn_raw_buffer_store_b64(vpk, rvtm, st_vtm, 0, 16);
        wg_barrier();
        if (w == 0) {
          LAS char* b = lds + L_P + (l & 1) * 4 * P_STR + (l >> 3) * 64 + gq * 16;
          f32x4 t0 = *(LAS f32x4*)b, t1 = *(LAS f32x4*)(b + P_STR), t2 = *(LAS f32x4*)(b + 2 * P_STR), t3 = *(LAS f32x4*)(b + 3 * P_STR);
          f32x4 t = (t0 + t1) + (t2 + t3);
#pragma unroll
          for (int r = 0; r < 4; ++r) t[r] += dpp_f<0xB1>(t[r]);
          unsigned p0 = min(cvt_pk(t[0], t[1]), 0xfffffffeu), p1 = min(cvt_pk(t[2], t[3]), 0xfffffffeu);
          unsigned q0 = dpp_u<0x4E>(p0), q1 = dpp_u<0x4E>(p1);
          __builtin_amdgcn_raw_buffer_store_b128((u32x4){p0, p1, q0, q1}, rmem, st_mem, 0, 16);
          st_mem += inc_mem;
        }
      } else {
        __builtin_amdgcn_raw_buffer_store_b64(vpk, rvtm, st_vtm, 0, 16);
      }
      st_vtm += KVM_B;
    } else {
      wg_barrier();
    }
  }
}

constexpr int H_P = 0, HP_STR = 1056, H_E = 7 * HP_STR + 32;
DI void helper_wave(const Params& p, const int h, LAS char* lds, const int w, const int l) {
  const int n16 = l & 15, g = l >> 4, hb = n16 >> 3, i8 = n16 & 7;
  const __amdgpu_buffer_rsrc_t rmem = __builtin_amdgcn_make_buffer_rsrc(p.ws + WS_MEMIMG, 0, (NSEG + 1) * MEMIMG_B, 0x00020000);
  const __amdgpu_buffer_rsrc_t rx = __builtin_amdgcn_make_buffer_rsrc(p.ws + WS_XCH, 0, NSEG * XCH_B, 0x00020000);
  const int wk = w < 7 ? w : 0;
  unsigned off_mem = (wk + 7 * hb) * 512 + g * 128 + i8 * 16;
  const uint16_t* kp = (const uint16_t*)(p.ws + WS_KT) + ((size_t)h * 28 * 64 + l) * 8;
  const uint16_t* up = (const uint16_t*)(p.ws + WS_UT) + ((size_t)(h * 28 + w) * 64 + l) * 8;
  unsigned st_ax = n16 < 8 ? XA_OFF + (w * 8 + (h >> 1)) * 1024 + (g * 16 + (h & 1) * 8 + n16) * 16 : OOB;
  const unsigned inc_ax = n16 < 8 ? XCH_B : 0;
  unsigned st_ms = l < 8 ? XS_OFF + (h >> 1) * 128 + ((h & 1) * 8 + l) * 8 : OOB;
  const unsigned inc_ms = l < 8 ? XCH_B : 0;
  bf16x8 k00, k01, k10, k11, u[4];
  auto prefetch = [&](int s) {
    const uint16_t* k = kp + (size_t)s * (SEG * NKT);
    k00 = *(const bf16x8*)(k + wk * 512); k01 = *(const bf16x8*)(k + (wk + 7) * 512);
    k10 = *(const bf16x8*)(k + (14 + wk) * 512); k11 = *(const bf16x8*)(k + (21 + wk) * 512);
    const uint16_t* q = up + (size_t)s * (SEG * NKT);
    u[0] = *(const bf16x8*)q; u[1] = *(const bf16x8*)(q + 8 * 512); u[2] = *(const bf16x8*)(q + 16 * 512); u[3] = *(const bf16x8*)(q + (w < 4 ? 24 : 16) * 512);
  };
  prefetch(0);
  int budget = 1 << 20;
  for (int s = 0; s + 1 < NSEG; ++s) {
    if (w < 7) {
      if (s) __builtin_amdgcn_s_sleep(DH);
      CBAR();
      u32x4 v = LD16(rmem, off_mem);
      while (__builtin_expect(isbad(mx4(v)), 0)) { if (--budget < 0) break; __builtin_amdgcn_s_sleep(1); CBAR(); v = LD16(rmem, off_mem); }
      off_mem += MEMIMG_B;
      bf16x8 f = __builtin_bit_cast(bf16x8, v);
      f32x4 z = {0, 0, 0, 0};
      f32x4 a0 = MFMA16(k00, f, z), a1 = MFMA16(k01, f, z), b0 = MFMA16(k10, f, z), b1 = MFMA16(k11, f, z);
      f32x4 r;
#pragma unroll
      for (int q = 0; q < 4; ++q) { float own = hb ? b1[q] : a0[q]; float oth = hb ? a1[q] : b0[q]; r[q] = own + dpp_f<0x128>(oth); }
      *(LAS f32x4*)(lds + H_P + w * HP_STR + l * 16) = r;
    }
    wg_barrier();
    if (w == 0) {
      f32x4 t[7];
#pragma unroll
      for (int k = 0; k < 7; ++k) t[k] = *(LAS f32x4*)(lds + H_P + k * HP_STR + l * 16);
      f32x4 sc = ((t[0] + t[1]) + (t[2] + t[3])) + ((t[4] + t[5]) + t[6]);
      float mx = fmaxf(fmax3(sc[0], sc[1], sc[2]), sc[3]);
      mx = fmaxf(mx, dpp_f<0x128>(mx)); mx = x32_max(mx); mx = x16_max(mx);
      float e0 = __builtin_amdgcn_exp2f(sc[0] - mx), e1 = __builtin_amdgcn_exp2f(sc[1] - mx), e2 = __builtin_amdgcn_exp2f(sc[2] - mx), e3 = __builtin_amdgcn_exp2f(sc[3] - mx);
      *(LAS u32x2*)(lds + H_E + (g * 16 + i8) * 16 + hb * 8) = (u32x2){cvt_pk(e0, e1), cvt_pk(e2, e3)};
      wg_barrier();
      float sm = (e0 + e1) + (e2 + e3);
      sm += dpp_f<0x128>(sm); sm = x32_sum(sm); sm = x16_sum(sm);
      __builtin_amdgcn_raw_buffer_store_b64((u32x2){min(f2u(mx), 0xfffffffeu), min(f2u(sm), 0xfffffffeu)}, rx, st_ms, 0, 16);
      st_ms += inc_ms;
    } else {
      wg_barrier();
    }
    bf16x8 pe = *(LAS bf16x8*)(lds + H_E + (g * 16 + i8) * 16);
    f32x4 z = {0, 0, 0, 0};
#pragma unroll
    for (int j = 0; j < 4; ++j) {
      f32x4 d = MFMA16(u[j], pe, z);
      u32x4 sv = {min(f2u(d[0]), 0xfffffffeu), min(f2u(d[1]), 0xfffffffeu), min(f2u(d[2]), 0xfffffffeu), min(f2u(d[3]), 0xfffffffeu)};
      if (j < 3 || w < 4) __builtin_amdgcn_raw_buffer_store_b128(sv, rx, j == 0 ? st_ax : (n16 < 8 ? st_ax + j * 65536 : OOB), 0, 16);
    }
    st_ax += inc_ax;
    CBAR();
    prefetch(s + 1);
  }
}

__global__ void __launch_bounds__(512, 2) k_chain(Params p) {
  extern __shared__ __attribute__((aligned(16))) char smem[];
  const int xg = blockIdx.x & 7, r = blockIdx.x >> 3;
  const int w = __builtin_amdgcn_readfirstlane(threadIdx.x >> 6), l = threadIdx.x & 63;
  if (xg == 0) {
    const int cw = r;
    if (cw < 28) { if (w == 7) chain_wave<true, true>(p, cw, (LAS char*)smem, w, l); else chain_wave<false, true>(p, cw, (LAS char*)smem, w, l); }
    else { if (w == 7) chain_wave<true, false>(p, cw, (LAS char*)smem, w, l); else chain_wave<false, false>(p, cw, (LAS char*)smem, w, l); }
  } else if (xg == 1 && r < 16) {
    helper_wave(p, r, (LAS char*)smem, w, l);
  }
}

DI void att_wg(const Params& p, LAS char* lds, int s, int hg, int w, int l) {
  const int n16 = l & 15, g = l >> 4;
  const int hl = w >> 1, qt = w & 1, h = hg * 4 + hl;
  const bf16x8* XF = (const bf16x8*)(p.ws + WS_XF) + ((size_t)(s * 2 + qt) * 14) * 64 + l;
  const bf16x8* KT = (const bf16x8*)(p.ws + WS_KT) + ((size_t)(s * NH + h) * 28) * 64 + l;
  const bf16x8* KM = (const bf16x8*)(p.ws + WS_KTM + (size_t)s * KVM_B) + ((size_t)(h >> 1) * 14) * 64 + l;
  f32x4 s0 = {0, 0, 0, 0}, s1 = s0, sm = s0;
#pragma unroll 7
  for (int ks = 0; ks < 14; ++ks) {
    bf16x8 q = XF[ks * 64];
    s0 = MFMA16(KT[ks * 64], q, s0);
    if (qt) s1 = MFMA16(KT[(14 + ks) * 64], q, s1);
    sm = MFMA16(KM[ks * 64], q, sm);
  }
  const float NEG = -3.0e38f;
  const bool ownm = (g >> 1) == (h & 1);
  float v[12];
#pragma unroll
  for (int r = 0; r < 4; ++r) {
    v[r] = (qt || (4 * g + r) <= n16) ? s0[r] : NEG;
    v[4 + r] = (qt && (4 * g + r) <= n16) ? s1[r] : NEG;
    v[8 + r] = ownm ? sm[r] : NEG;
  }
  float mx = NEG;
#pragma unroll
  for (int r = 0; r < 12; ++r) mx = fmaxf(mx, v[r]);
  mx = x16_max(mx); mx = x32_max(mx);
  float sum = 0.f;
#pragma unroll
  for (int r = 0; r < 12; ++r) { v[r] = v[r] > -1.0e38f ? __builtin_amdgcn_exp2f(v[r] - mx) : 0.f; sum += v[r]; }
  sum = x16_sum(sum); sum = x32_sum(sum);
  const float rinv = 1.0f / sum;
  u32x4 px = {cvt_pk(v[0] * rinv, v[1] * rinv), cvt_pk(v[2] * rinv, v[3] * rinv), cvt_pk(v[4] * rinv, v[5] * rinv), cvt_pk(v[6] * rinv, v[7] * rinv)};
  *(LAS u32x4*)(lds + (hl * 2 + qt) * 1024 + l * 16) = px;
  if (ownm) *(LAS u32x2*)(lds + 8192 + qt * 1024 + l * 16 + (hl >> 1) * 8) = (u32x2){cvt_pk(v[8] * rinv, v[9] * rinv), cvt_pk(v[10] * rinv, v[11] * rinv)};
  wg_barrier();
  bf16x8 P[2][5];
#pragma unroll
  for (int t = 0; t < 2; ++t) {
#pragma unroll
    for (int k = 0; k < 4; ++k) P[t][k] = *(LAS bf16x8*)(lds + (k * 2 + t) * 1024 + l * 16);
    P[t][4] = *(LAS bf16x8*)(lds + 8192 + t * 1024 + l * 16);
  }
  const bf16x8* UT = (const bf16x8*)(p.ws + WS_UT) + ((size_t)(s * NH + hg * 4) * 28) * 64 + l;
  const bf16x8* VM = (const bf16x8*)(p.ws + WS_VTM + (size_t)s * KVM_B) + ((size_t)hg * 28) * 64 + l;
  float* PO = (float*)(p.ws + WS_PO) + ((size_t)hg * SEQ + s * SEG + n16) * DM + 4 * g;
  for (int o = w; o < 28; o += 8) {
    bf16x8 a[5];
#pragma unroll
    for (int k = 0; k < 4; ++k) a[k] = UT[(k * 28 + o) * 64];
    a[4] = VM[o * 64];
    f32x4 d0 = {0, 0, 0, 0}, d1 = d0;
#pragma unroll
    for (int k = 0; k < 5; ++k) { d0 = MFMA16(a[k], P[0][k], d0); d1 = MFMA16(a[k], P[1][k], d1); }
    *(f32x4*)(PO + o * 16) = d0;
    *(f32x4*)(PO + 16 * DM + o * 16) = d1;
  }
}
__global__ void __launch_bounds__(512, 2) k_att(Params p) {
  extern __shared__ __attribute__((aligned(16))) char smem[];
  const int w = __builtin_amdgcn_readfirstlane(threadIdx.x >> 6), l = threadIdx.x & 63;
  att_wg(p, (LAS char*)smem, blockIdx.x >> 2, blockIdx.x & 3, w, l);
}

DI void comb_row(const Params& p, int row, int l) {
  const bool act = l < 56;
  float v[8];
  if (act) {
    u32x4 t = *(const u32x4*)(p.x + (size_t)row * DM + l * 8);
#pragma unroll
    for (int j = 0; j < 4; ++j) { v[2 * j] = blo(t[j]); v[2 * j + 1] = bhi(t[j]); }
    const float* po = (const float*)(p.ws + WS_PO) + (size_t)row * DM + l * 8;
    f32x4 a = {0, 0, 0, 0}, b = a;
#pragma unroll
    for (int k = 0; k < 4; ++k) { a += *(const f32x4*)(po + (size_t)k * SEQ * DM); b += *(const f32x4*)(po + (size_t)k * SEQ * DM + 4); }
#pragma unroll
    for (int j = 0; j < 4; ++j) { v[j] += a[j]; v[4 + j] += b[j]; }
    float* hp = (float*)(p.ws + WS_H) + (size_t)row * DM + l * 8;
    *(f32x4*)hp = (f32x4){v[0], v[1], v[2], v[3]}; *(f32x4*)(hp + 4) = (f32x4){v[4], v[5], v[6], v[7]};
  } else {
#pragma unroll
    for (int j = 0; j < 8; ++j) v[j] = 0.f;
  }
  u32x4 r = ln_row(v, act);
  if (act) *(u32x4*)(p.ws + WS_HN + tiled_off(row, l)) = r;
}
__global__ void __launch_bounds__(256) k_comb(Params p) {
  comb_row(p, blockIdx.x * 4 + (threadIdx.x >> 6), threadIdx.x & 63);
}

DI void glu1_wg(const Params& p, int fb, int seg0, int step, int w, int l) {
  const int ftl = w & 1, rt = (w >> 1) & 1, sp = w >> 2;
  const int ft = fb * 2 + ftl;
  const bf16x8* WG = (const bf16x8*)(p.ws + WS_WG) + (size_t)ft * 28 * 64 + l;
  bf16x8 wgt[14], wup[14];
#pragma unroll
  for (int ks = 0; ks < 14; ++ks) { wgt[ks] = WG[ks * 64]; wup[ks] = WG[(14 + ks) * 64]; }
  const int n16 = l & 15, g = l >> 4;
  for (int s = seg0 + sp; s < NSEG; s += step) {
    const bf16x8* HN = (const bf16x8*)(p.ws + WS_HN) + ((size_t)(s * 2 + rt) * 14) * 64 + l;
    f32x4 ag = {0, 0, 0, 0}, au = ag;
#pragma unroll
    for (int ks = 0; ks < 14; ++ks) { bf16x8 x = HN[ks * 64]; ag = MFMA16(wgt[ks], x, ag); au = MFMA16(wup[ks], x, au); }
    u32x2 o = {cvt_pk(ag[0] * au[0], ag[1] * au[1]), cvt_pk(ag[2] * au[2], ag[3] * au[3])};
    char* d = p.ws + WS_HG + ((((size_t)(s * 2 + rt) * 56 + fb) * 64 + (2 * ftl + (g >> 1)) * 16 + n16) * 8 + 4 * (g & 1)) * 2;
    *(u32x2*)d = o;
  }
}
__global__ void __launch_bounds__(512, 2) k_glu1(Params p) {
  const int w = __builtin_amdgcn_readfirstlane(threadIdx.x >> 6), l = threadIdx.x & 63;
  glu1_wg(p, blockIdx.x % 56, (blockIdx.x / 56) * 2, 8, w, l);
}

DI void glu2_wg(const Params& p, LAS char* lds, int o, int seg0, int step, int w, int l) {
  const bf16x8* WD = (const bf16x8*)(p.ws + WS_WD) + ((size_t)o * 56 + 7 * w) * 64 + l;
  bf16x8 wd[7];
#pragma unroll
  for (int k = 0; k < 7; ++k) wd[k] = WD[k * 64];
  const int tid = w * 64 + l;
  int buf = 0;
  for (int s = seg0; s < NSEG; s += step) {
    const bf16x8* HG = (const bf16x8*)(p.ws + WS_HG) + ((size_t)(s * 2) * 56 + 7 * w) * 64 + l;
    f32x4 d0 = {0, 0, 0, 0}, d1 = d0;
#pragma unroll
    for (int k = 0; k < 7; ++k) { d0 = MFMA16(wd[k], HG[k * 64], d0); d1 = MFMA16(wd[k], HG[(56 + k) * 64], d1); }
    LAS char* L = lds + buf * 16384;
    *(LAS f32x4*)(L + w * 2048 + l * 16) = d0;
    *(LAS f32x4*)(L + w * 2048 + 1024 + l * 16) = d1;
    wg_barrier();
    if (tid < 128) {
      f32x4 a = {0, 0, 0, 0};
#pragma unroll
      for (int k = 0; k < 8; ++k) a += *(LAS f32x4*)(L + k * 2048 + tid * 16);
      const int row = s * SEG + (tid >> 6) * 16 + (tid & 15), col = o * 16 + 4 * ((tid >> 4) & 3);
      f32x4 hv = *(const f32x4*)((const float*)(p.ws + WS_H) + (size_t)row * DM + col);
      a += hv;
      *(u32x2*)(p.out + (size_t)row * DM + col) = (u32x2){cvt_pk(a[0], a[1]), cvt_pk(a[2], a[3])};
    }
    buf ^= 1;
  }
}
__global__ void __launch_bounds__(512, 2) k_glu2(Params p) {
  extern __shared__ __attribute__((aligned(16))) char smem[];
  const int w = __builtin_amdgcn_readfirstlane(threadIdx.x >> 6), l = threadIdx.x & 63;
  glu2_wg(p, (LAS char*)smem, blockIdx.x % 28, blockIdx.x / 28, 8, w, l);
}

extern "C" void kernel_launch(void* const* d_in, const int* in_sizes, int n_in,
                              void* d_out, int out_size, void* d_ws, size_t ws_size,
                              hipStream_t stream) {
  Params p;
  p.x = (const uint16_t*)d_in[0]; p.mem0 = (const uint16_t*)d_in[1];
  p.Wq = (const uint16_t*)d_in[2]; p.Wk = (const uint16_t*)d_in[3]; p.Wv = (const uint16_t*)d_in[4]; p.Wo = (const uint16_t*)d_in[5];
  p.wg = (const uint16_t*)d_in[8]; p.wu = (const uint16_t*)d_in[9]; p.wd = (const uint16_t*)d_in[10];
  p.out = (uint16_t*)d_out; p.ws = (char*)d_ws;
  (void)hipFuncSetAttribute((const void*)k_chain, hipFuncAttributeMaxDynamicSharedMemorySize, CHAIN_LDS);
  k_prep<<<512, 256, 0, stream>>>(p);
  k_wt<<<392, 256, 0, stream>>>(p);
  k_xgemm<<<224, 512, 57344, stream>>>(p);
  k_chain<<<256, 512, CHAIN_LDS, stream>>>(p);
  k_att<<<256, 512, 10240, stream>>>(p);
  k_comb<<<512, 256, 0, stream>>>(p);
  k_glu1<<<224, 512, 0, stream>>>(p);
  k_glu2<<<224, 512, 32768, stream>>>(p);
}
```

```cpp
#include <hip/hip_runtime.h>
#include <hip/hip_bf16.h>
#include <stdint.h>

typedef __bf16 bf16x8 __attribute__((ext_vector_type(8)));
typedef __bf16 bf16x16 __attribute__((ext_vector_type(16)));
typedef __bf16 bf16x2 __attribute__((ext_vector_type(2)));
typedef float f32x4 __attribute__((ext_vector_type(4)));
typedef unsigned u32x4 __attribute__((ext_vector_type(4)));
typedef unsigned u32x2 __attribute__((ext_vector_type(2)));
typedef unsigned u32x8 __attribute__((ext_vector_type(8)));
typedef short s16x4 __attribute__((ext_vector_type(4)));
#define LAS __attribute__((address_space(3)))
#define DI __device__ __forceinline__

constexpr int DM = 448, NH = 16, HD = 256, NSEG = 64, SEG = 32, SEQ = 2048, NTOT = 14336, NKT = 7168, FF = 1792;
constexpr int MEMIMG_B = 7168;
constexpr int XM_OFF = 0, XS_OFF = 8192, XA_OFF = 9216, XCH_B = 9216 + 28 * 8192;
constexpr int KVM_B = NH * 8 * DM * 2;

constexpr int KR = 5, PRE0 = 4, XW = 1;
constexpr int D2A = 11, D2B = 6, D2N = 16, DM0 = 10, DM7 = 16, DH = 30;

constexpr int L_M = 0, L_T = 7168, T_STR = 640, L_P = L_T + 8 * T_STR, P_STR = 1056, L_WB = L_P + 8 * P_STR + 64;
constexpr int L_WB_AL = (L_WB + 255) & ~255;
constexpr int CHAIN_LDS = L_WB_AL + 7 * (14 - KR) * 2048;

constexpr size_t WS_MEMIMG = 0;
constexpr size_t WS_XCH = WS_MEMIMG + (size_t)(NSEG + 1) * MEMIMG_B;
constexpr size_t WS_SENT_END = WS_XCH + (size_t)NSEG * XCH_B;
constexpr size_t WS_XF = (WS_SENT_END + 4095) & ~(size_t)4095;
constexpr size_t WS_WT = WS_XF + (size_t)SEQ * DM * 2;
constexpr size_t WS_KT = WS_WT + (size_t)NTOT * DM * 2;
constexpr size_t WS_UT = WS_KT + (size_t)SEQ * NKT * 2;
constexpr size_t WS_KTM = WS_UT + (size_t)SEQ * NKT * 2;
constexpr size_t WS_VTM = WS_KTM + (size_t)NSEG * KVM_B;
constexpr size_t WS_PO = WS_VTM + (size_t)NSEG * KVM_B;
constexpr size_t WS_H = WS_PO + (size_t)4 * SEQ * DM * 4;
constexpr size_t WS_HN = WS_H + (size_t)SEQ * DM * 4;
constexpr size_t WS_WG = WS_HN + (size_t)SEQ * DM * 2;
constexpr size_t WS_WD = WS_WG + (size_t)2 * FF * DM * 2;
constexpr size_t WS_HG = WS_WD + (size_t)FF * DM * 2;
constexpr size_t WS_FLG = WS_HG + (size_t)SEQ * FF * 2;
constexpr size_t WS_END = WS_FLG + 5 * NSEG * 128;
constexpr int F_XG = 0, F_CM = 1, F_AT = 2, F_HN = 3, F_G1 = 4;

struct Params {
  const uint16_t* x; const uint16_t* mem0; const uint16_t* Wq; const uint16_t* Wk; const uint16_t* Wv; const uint16_t* Wo;
  const uint16_t* wg; const uint16_t* wu; const uint16_t* wd; uint16_t* out; char* ws;
};

DI unsigned cvt_pk(float a, float b) { bf16x2 r = {(__bf16)a, (__bf16)b}; return __builtin_bit_cast(unsigned, r); }
DI void wg_barrier() { asm volatile("s_waitcnt lgkmcnt(0)\n\ts_barrier" ::: "memory"); }
DI unsigned umax3(unsigned a, unsigned b, unsigned c) { return max(max(a, b), c); }
DI float fmax3(float a, float b, float c) { return fmaxf(fmaxf(a, b), c); }
DI float u2f(unsigned u) { return __builtin_bit_cast(float, u); }
DI unsigned f2u(float f) { return __builtin_bit_cast(unsigned, f); }
DI float blo(unsigned u) { return u2f(u << 16); }
DI float bhi(unsigned u) { return u2f(u & 0xffff0000u); }
template<int CTRL> DI float dpp_f(float x) { return u2f((unsigned)__builtin_amdgcn_update_dpp(0, (int)f2u(x), CTRL, 0xf, 0xf, true)); }
template<int CTRL> DI unsigned dpp_u(unsigned x) { return (unsigned)__builtin_amdgcn_update_dpp(0, (int)x, CTRL, 0xf, 0xf, true); }
DI float x32_max(float x) { auto r = __builtin_amdgcn_permlane32_swap(f2u(x), f2u(x), false, false); return fmaxf(u2f(r[0]), u2f(r[1])); }
DI float x32_sum(float x) { auto r = __builtin_amdgcn_permlane32_swap(f2u(x), f2u(x), false, false); return u2f(r[0]) + u2f(r[1]); }
DI float x16_max(float x) { auto r = __builtin_amdgcn_permlane16_swap(f2u(x), f2u(x), false, false); return fmaxf(u2f(r[0]), u2f(r[1])); }
DI float x16_sum(float x) { auto r = __builtin_amdgcn_permlane16_swap(f2u(x), f2u(x), false, false); return u2f(r[0]) + u2f(r[1]); }
DI float wave_sum(float v) {
  v += dpp_f<0xB1>(v); v += dpp_f<0x4E>(v); v += dpp_f<0x124>(v); v += dpp_f<0x128>(v);
  v = x16_sum(v); v = x32_sum(v); return v;
}
#define SMFMAC(a, b, c, idx) __builtin_amdgcn_smfmac_f32_16x16x64_bf16((a), (b), (c), (idx), 0, 0)
#define MFMA16(a, b, c) __builtin_amdgcn_mfma_f32_16x16x32_bf16((a), (b), (c), 0, 0, 0)
constexpr unsigned OOB = 0x7fffffffu;
#define LD16(r, off) __builtin_amdgcn_raw_buffer_load_b128((r), (off), 0, 16)
#define LD8(r, off) __builtin_amdgcn_raw_buffer_load_b64((r), (off), 0, 16)
#define CBAR() asm volatile("" ::: "memory")
#define SBAR() __builtin_amdgcn_sched_barrier(0)

DI unsigned* flag_ptr(const Params& p, int kind, int s) { return (unsigned*)(p.ws + WS_FLG + (size_t)(kind * NSEG + s) * 128); }
DI void flag_add(unsigned* f) { __hip_atomic_fetch_add(f, 1u, __ATOMIC_RELAXED, __HIP_MEMORY_SCOPE_AGENT); }
DI void flag_wait(const unsigned* f, unsigned want) {
  int budget = 1 << 20;
  while ((unsigned)__builtin_amdgcn_readfirstlane(__hip_atomic_load(f, __ATOMIC_RELAXED, __HIP_MEMORY_SCOPE_AGENT)) < want) {
    if (--budget < 0) break;
    __builtin_amdgcn_s_sleep(8);
  }
  asm volatile("" ::: "memory");
}
DI void drain_barrier() { asm volatile("s_waitcnt vmcnt(0) lgkmcnt(0)\n\ts_barrier" ::: "memory"); }
DI __amdgpu_buffer_rsrc_t ws_rsrc(const Params& p, size_t off, size_t bytes) { return __builtin_amdgcn_make_buffer_rsrc(p.ws + off, 0, (int)bytes, 0x00020000); }
#define LDF(r, off) __builtin_bit_cast(bf16x8, LD16((r), (off)))
#define ST16(v, r, off) __builtin_amdgcn_raw_buffer_store_b128((v), (r), (off), 0, 16)
#define ST8(v, r, off) __builtin_amdgcn_raw_buffer_store_b64((v), (r), (off), 0, 16)

DI u32x4 ln_row(float (&v)[8], bool act) {
  float s = 0.f;
#pragma unroll
  for (int j = 0; j < 8; ++j) s += v[j];
  s = wave_sum(act ? s : 0.f);
  const float mean = s * (1.0f / DM);
  float q = 0.f;
#pragma unroll
  for (int j = 0; j < 8; ++j) { float d = v[j] - mean; q += d * d; }
  q = wave_sum(act ? q : 0.f);
  const float rstd = 1.0f / sqrtf(q * (1.0f / DM) + 1e-5f);
  u32x4 r;
#pragma unroll
  for (int j = 0; j < 4; ++j) r[j] = cvt_pk((v[2 * j] - mean) * rstd, (v[2 * j + 1] - mean) * rstd);
  return r;
}
DI unsigned tiled_off(int row, int c8) { return (unsigned)((((row >> 4) * 14 + (c8 >> 2)) * 64 + (c8 & 3) * 16 + (row & 15)) * 16); }

__global__ void __launch_bounds__(256) k_prep(Params p) {
  const int tid = threadIdx.x, l = tid & 63, w = tid >> 6;
  const int nb = gridDim.x, b = blockIdx.x;
  for (int row = b * 4 + w; row < SEQ; row += nb * 4) {
    const bool act = l < 56;
    float v[8];
    u32x4 t = act ? *(const u32x4*)(p.x + (size_t)row * DM + l * 8) : (u32x4){0, 0, 0, 0};
#pragma unroll
    for (int j = 0; j < 4; ++j) { v[2 * j] = blo(t[j]); v[2 * j + 1] = bhi(t[j]); }
    u32x4 r = ln_row(v, act);
    if (act) *(u32x4*)(p.ws + WS_XF + tiled_off(row, l)) = r;
  }
  {
    u32x4* d = (u32x4*)(p.ws + MEMIMG_B);
    const size_t n = (WS_SENT_END - MEMIMG_B) / 16;
    for (size_t i = (size_t)b * 256 + tid; i < n; i += (size_t)nb * 256) d[i] = (u32x4){~0u, ~0u, ~0u, ~0u};
  }
  if (b == 1) { for (int i = tid; i < 5 * NSEG * 8; i += 256) ((u32x4*)(p.ws + WS_FLG))[i] = (u32x4){0, 0, 0, 0}; }
  if (b == 0) {
    for (int c = tid; c < 448; c += 256) {
      const int row = c & 7, kg = c >> 3;
      u32x4 t = *(const u32x4*)(p.mem0 + row * DM + kg * 8);
      t.x = min(t.x, 0xfffffffeu); t.y = min(t.y, 0xfffffffeu); t.z = min(t.z, 0xfffffffeu); t.w = min(t.w, 0xfffffffeu);
      *(u32x4*)(p.ws + WS_MEMIMG + c * 16) = t;
    }
  }
  {
    const int nG = 112 * 2 * 14 * 64, nD = 28 * 56 * 64;
    for (int i = b * 256 + tid; i < nG + nD; i += nb * 256) {
      const uint16_t* src; int stride; u32x4* dst;
      if (i < nG) {
        const int ln = i & 63, ks = (i >> 6) % 14, gu = (i / (64 * 14)) & 1, ft = i / (64 * 14 * 2);
        src = (gu ? p.wu : p.wg) + (size_t)(32 * ks + 8 * (ln >> 4)) * FF + ft * 16 + (ln & 15); stride = FF;
        dst = (u32x4*)(p.ws + WS_WG) + i;
      } else {
        const int j = i - nG, ln = j & 63, kf = (j >> 6) % 56, o = j / (64 * 56);
        src = p.wd + (size_t)(32 * kf + 8 * (ln >> 4)) * DM + o * 16 + (ln & 15); stride = DM;
        dst = (u32x4*)(p.ws + WS_WD) + j;
      }
      u32x4 r;
#pragma unroll
      for (int q = 0; q < 4; ++q) r[q] = (unsigned)src[(2 * q) * stride] | ((unsigned)src[(2 * q + 1) * stride] << 16);
      *dst = r;
    }
  }
}

__global__ void __launch_bounds__(256) k_wt(Params p) {
  __shared__ __attribute__((aligned(16))) char sm[4 * 32 * 144];
  const int l = threadIdx.x & 63, w = threadIdx.x >> 6, n16 = l & 15, g = l >> 4;
  const int task = blockIdx.x * 4 + w;
  if (task >= 32 * 49) return;
  const int mat = task / 49, t = task % 49, te = t / 7, tn = t % 7;
  const int isB = mat >> 4, h = mat & 15;
  const uint16_t* Q = (isB ? p.Wv : p.Wk) + (size_t)(te * 64 + n16) * (NH * HD) + h * HD + 8 * g;
  const uint16_t* P = p.Wq + (size_t)(tn * 64 + n16) * (NH * HD) + h * HD + 8 * g;
  const uint16_t* Wo = p.Wo + (size_t)h * HD * DM + tn * 64;
  LAS char* my = (LAS char*)sm + w * 32 * 144;
  f32x4 acc[4][4];
#pragma unroll
  for (int a = 0; a < 4; ++a)
#pragma unroll
    for (int c = 0; c < 4; ++c) acc[a][c] = (f32x4){0, 0, 0, 0};
  for (int ks = 0; ks < 8; ++ks) {
    bf16x8 qa[4], pb[4];
#pragma unroll
    for (int a = 0; a < 4; ++a) qa[a] = *(const bf16x8*)(Q + (size_t)a * 16 * (NH * HD) + ks * 32);
    if (!isB) {
#pragma unroll
      for (int c = 0; c < 4; ++c) pb[c] = *(const bf16x8*)(P + (size_t)c * 16 * (NH * HD) + ks * 32);
    } else {
#pragma unroll
      for (int i = 0; i < 4; ++i) {
        const int f = i * 8 + (l >> 3);
        u32x4 v = *(const u32x4*)(Wo + (size_t)(ks * 32 + f) * DM + (l & 7) * 8);
        *(LAS u32x4*)(my + f * 144 + (l & 7) * 16) = v;
      }
#pragma unroll
      for (int c = 0; c < 4; ++c) {
        LAS char* bb = my + (8 * g + (n16 >> 2)) * 144 + c * 32 + (n16 & 3) * 8;
        s16x4 lo = __builtin_amdgcn_ds_read_tr16_b64_v4i16((LAS s16x4*)bb);
        s16x4 hi = __builtin_amdgcn_ds_read_tr16_b64_v4i16((LAS s16x4*)(bb + 4 * 144));
        u32x2 l2 = __builtin_bit_cast(u32x2, lo), h2 = __builtin_bit_cast(u32x2, hi);
        pb[c] = __builtin_bit_cast(bf16x8, (u32x4){l2.x, l2.y, h2.x, h2.y});
      }
    }
#pragma unroll
    for (int a = 0; a < 4; ++a)
#pragma unroll
      for (int c = 0; c < 4; ++c) acc[a][c] = MFMA16(qa[a], pb[c], acc[a][c]);
  }
  const float sc = isB ? 1.0f : 0.0625f * 1.4426950408889634f;
  uint16_t* WT = (uint16_t*)(p.ws + WS_WT) + (size_t)(isB * NKT + h * DM + tn * 64 + n16) * DM + te * 64 + 4 * g;
#pragma unroll
  for (int c = 0; c < 4; ++c)
#pragma unroll
    for (int a = 0; a < 4; ++a) {
      u32x2 v = {cvt_pk(acc[a][c][0] * sc, acc[a][c][1] * sc), cvt_pk(acc[a][c][2] * sc, acc[a][c][3] * sc)};
      *(u32x2*)(WT + (size_t)c * 16 * DM + a * 16) = v;
    }
}

DI void xgemm_wg(const Params& p, LAS char* lds, int nb, int seg0, int step, int w, int l) {
  const int n16 = l & 15, g = l >> 4;
  const int n0 = nb * 256 + w * 32;
  const bool isU = n0 >= NKT;
  const uint16_t* WT = (const uint16_t*)(p.ws + WS_WT);
  const int r0 = isU ? n0 + n16 : n0 + 8 * (n16 >> 2) + (n16 & 3);
  const int r1 = isU ? r0 + 16 : r0 + 4;
  bf16x8 w0[14], w1[14];
#pragma unroll
  for (int ks = 0; ks < 14; ++ks) {
    w0[ks] = *(const bf16x8*)(WT + (size_t)r0 * DM + ks * 32 + 8 * g);
    w1[ks] = *(const bf16x8*)(WT + (size_t)r1 * DM + ks * 32 + 8 * g);
  }
  const int nn = isU ? n0 - NKT : n0;
  const int hh = nn / DM, cc = nn % DM;
  const int tid = w * 64 + l;
  const u32x4* XF = (const u32x4*)(p.ws + WS_XF);
  u32x4 pre[4];
  auto gload = [&](int s) {
#pragma unroll
    for (int i = 0; i < 4; ++i) { const int c = i * 512 + tid; if (c < 1792) pre[i] = XF[(size_t)s * 1792 + c]; }
  };
  gload(seg0);
  int buf = 0, prev = -1;
  const __amdgpu_buffer_rsrc_t rout = ws_rsrc(p, isU ? WS_UT : WS_KT, (size_t)SEQ * NKT * 2);
  for (int s = seg0; s < NSEG; s += step) {
    LAS char* L = lds + buf * 28672;
#pragma unroll
    for (int i = 0; i < 4; ++i) { const int c = i * 512 + tid; if (c < 1792) *(LAS u32x4*)(L + c * 16) = pre[i]; }
    asm volatile("s_waitcnt vmcnt(0)" ::: "memory");
    if (s + step < NSEG) gload(s + step);
    wg_barrier();
    if (tid == 0 && prev >= 0) flag_add(flag_ptr(p, F_XG, prev));
    f32x4 a00 = {0, 0, 0, 0}, a01 = a00, a10 = a00, a11 = a00;
#pragma unroll
    for (int ks = 0; ks < 14; ++ks) {
      bf16x8 x0 = *(LAS bf16x8*)(L + ks * 1024 + l * 16), x1 = *(LAS bf16x8*)(L + (14 + ks) * 1024 + l * 16);
      if (isU) { a00 = MFMA16(x0, w0[ks], a00); a01 = MFMA16(x1, w0[ks], a01); a10 = MFMA16(x0, w1[ks], a10); a11 = MFMA16(x1, w1[ks], a11); }
      else     { a00 = MFMA16(w0[ks], x0, a00); a01 = MFMA16(w1[ks], x0, a01); a10 = MFMA16(w0[ks], x1, a10); a11 = MFMA16(w1[ks], x1, a11); }
    }
    u32x4 o0 = {cvt_pk(a00[0], a00[1]), cvt_pk(a00[2], a00[3]), cvt_pk(a01[0], a01[1]), cvt_pk(a01[2], a01[3])};
    u32x4 o1 = {cvt_pk(a10[0], a10[1]), cvt_pk(a10[2], a10[3]), cvt_pk(a11[0], a11[1]), cvt_pk(a11[2], a11[3])};
    {
      const unsigned off = (unsigned)((((s * NH + hh) * 28 + (isU ? (cc >> 4) : (cc >> 5))) * 64 + l) * 16);
      ST16(o0, rout, off); ST16(o1, rout, off + (isU ? 1024 : 14 * 1024));
    }
    prev = s;
    buf ^= 1;
  }
  drain_barrier();
  if (tid == 0 && prev >= 0) flag_add(flag_ptr(p, F_XG, prev));
}
DI bf16x16 load_wfrag(const uint16_t* ra, const uint16_t* rb, int ks, int g) {
  u32x2 alo = *(const u32x2*)(ra + 32 * ks + 4 * g), ahi = *(const u32x2*)(ra + 32 * ks + 16 + 4 * g);
  u32x2 blo_ = *(const u32x2*)(rb + 32 * ks + 4 * g), bhi_ = *(const u32x2*)(rb + 32 * ks + 16 + 4 * g);
  u32x8 r = {alo.x, blo_.x, alo.y, blo_.y, ahi.x, bhi_.x, ahi.y, bhi_.y};
  return __builtin_bit_cast(bf16x16, r);
}
DI f32x4 comb32(f32x4 a, f32x4 b) {
  f32x4 r;
#pragma unroll
  for (int i = 0; i < 4; ++i) { float fa = a[i], fb = b[i]; auto t = __builtin_amdgcn_permlane32_swap(f2u(fa), f2u(fb), false, false); r[i] = u2f(t[0]) + u2f(t[1]); }
  return r;
}
DI bf16x16 lds_wfrag(LAS char* d) { u32x4 t0 = *(LAS u32x4*)d, t1 = *(LAS u32x4*)(d + 1024);
  u32x8 t = {t0.x, t0.y, t0.z, t0.w, t1.x, t1.y, t1.z, t1.w}; return __builtin_bit_cast(bf16x16, t); }
DI unsigned mx4(u32x4 v) { return max(umax3(v.x, v.y, v.z), v.w); }
DI bool isbad(unsigned m) { return __builtin_amdgcn_ballot_w64(m == 0xffffffffu) != 0; }

template<bool W7, bool OUT>
DI void chain_wave(const Params& p, const int cw, LAS char* lds, const int w, const int l) {
  constexpr int KRW = W7 ? 14 : KR;
  constexpr int NL = 14 - KRW;
  constexpr int PRE = W7 ? 0 : PRE0;
  const int n16 = l & 15, g = l >> 4, tid = w * 64 + l;
  const int h = cw >> 1, c = cw & 1, o = cw;
  const int idx = n16 < 8 ? 0x4444 : 0xEEEE;
  const uint16_t* WT = (const uint16_t*)(p.ws + WS_WT);
  const __amdgpu_buffer_rsrc_t rmem = __builtin_amdgcn_make_buffer_rsrc(p.ws + WS_MEMIMG, 0, (NSEG + 1) * MEMIMG_B, 0x00020000);
  const __amdgpu_buffer_rsrc_t rx = __builtin_amdgcn_make_buffer_rsrc(p.ws + WS_XCH, 0, NSEG * XCH_B, 0x00020000);
  const __amdgpu_buffer_rsrc_t rktm = __builtin_amdgcn_make_buffer_rsrc(p.ws + WS_KTM, 0, NSEG * KVM_B, 0x00020000);
  const __amdgpu_buffer_rsrc_t rvtm = __builtin_amdgcn_make_buffer_rsrc(p.ws + WS_VTM, 0, NSEG * KVM_B, 0x00020000);
  bf16x16 wa[W7 ? 1 : 14];
  bf16x16 wb[OUT ? KRW : 1];
  if (!W7) {
    const uint16_t* ra = WT + (size_t)(h * DM + (14 * c + 2 * w) * 16 + n16) * DM;
#pragma unroll
    for (int u = 0; u < 14; ++u) { const uint16_t* r = ra + (u & 1) * 16 * DM; wa[u] = load_wfrag(r, r + 224, u >> 1, g); }
  }
  LAS char* wl = lds + L_WB_AL + w * (14 - KR) * 2048 + l * 16;
  if (OUT) {
    const uint16_t* ra = WT + (size_t)(NKT + (2 * w) * DM + 16 * o + n16) * DM;
#pragma unroll
    for (int u = 0; u < 14; ++u) {
      const uint16_t* r = ra + (u & 1) * DM * DM;
      bf16x16 f = load_wfrag(r, r + 224, u >> 1, g);
      if (u < KRW) wb[u] = f;
      else { u32x8 t = __builtin_bit_cast(u32x8, f); LAS char* d = wl + (u - KRW) * 2048;
        *(LAS u32x4*)d = (u32x4){t[0], t[1], t[2], t[3]}; *(LAS u32x4*)(d + 1024) = (u32x4){t[4], t[5], t[6], t[7]}; }
    }
  }
  if (W7) { if (l < T_STR / 16) *(LAS u32x4*)(lds + L_T + 7 * T_STR + l * 16) = (u32x4){0, 0, 0, 0}; }
  unsigned off_mem = tid * 16;
  const int hb = n16 >> 3, i8 = n16 & 7;
  unsigned off_mm = XM_OFF + w * 1024 + (hb * 16 + (g & 1) * 8 + i8) * 16;
  unsigned off_ms = XS_OFF + w * 128 + n16 * 8;
  unsigned off_ax = XA_OFF + (o * 8 + w) * 1024 + l * 16;
  const bool own_m = hb == (g >> 1);
  const bool st_m = n16 < 8 && g < 2;
  unsigned st_mm = st_m ? XM_OFF + (h >> 1) * 1024 + c * 512 + ((h & 1) * 16 + g * 8 + i8) * 16 : OOB;
  const unsigned inc_mm = st_m ? XCH_B : 0;
  const int gq = (l >> 1) & 3;
  unsigned st_mem = ((l & 3) == 0) ? MEMIMG_B + o * 256 + (gq >> 1) * 128 + (l >> 3) * 16 : OOB;
  const unsigned inc_mem = ((l & 3) == 0) ? MEMIMG_B : 0;
  unsigned st_vtm = ((((w >> 1) * 28 + o) * 64 + l) * 8 + (w & 1) * 4) * 2;
  unsigned st_ktm = ((((h >> 1) * 14 + 7 * c + (g >> 1)) * 4 + 2 * hb + (g & 1)) * 16 + (h & 1) * 8 + i8) * 16;
  LAS char* a_rd = lds + L_M + hb * 7 * 512 + g * 128 + i8 * 16;
  int budget = 1 << 20;
  wg_barrier();
  for (int s = 0; s < NSEG; ++s) {
    bf16x16 tw[NL > 0 ? NL : 1];
    if (OUT) {
#pragma unroll
      for (int j = 0; j < PRE; ++j) tw[j] = lds_wfrag(wl + j * 2048);
      SBAR();
    }
    if (!W7) {
      if (!OUT) { __builtin_amdgcn_s_sleep(D2N); } else if (w != 0) { __builtin_amdgcn_s_sleep(D2A); } else { __builtin_amdgcn_s_sleep(D2B); }
      CBAR();
      u32x4 v = LD16(rmem, off_mem);
      while (__builtin_expect(isbad(mx4(v)), 0)) { if (--budget < 0) break; if (!OUT) __builtin_amdgcn_s_sleep(1); CBAR(); v = LD16(rmem, off_mem); }
      *(LAS u32x4*)(lds + L_M + tid * 16) = v;
      off_mem += MEMIMG_B;
    }
    if (W7) asm volatile("s_waitcnt vmcnt(0)" ::: "memory");
    wg_barrier();
    if (W7 && l == 0 && s > 0) flag_add(flag_ptr(p, F_CM, s - 1));
    bf16x8 af[7];
#pragma unroll
    for (int k = 0; k < 7; ++k) af[k] = *(LAS bf16x8*)(a_rd + k * 512);
    f32x4 vt1 = {0, 0, 0, 0}, vt2 = {0, 0, 0, 0};
    if (!W7) {
      f32x4 acc1 = {0, 0, 0, 0}, acc2 = {0, 0, 0, 0};
#pragma unroll
      for (int k = 0; k < 7; ++k) { acc1 = SMFMAC(af[k], wa[2 * k], acc1, idx); acc2 = SMFMAC(af[k], wa[2 * k + 1], acc2, idx); }
      f32x4 acc = comb32(acc1, acc2);
      u32x2 pk = {cvt_pk(acc[0], acc[1]), cvt_pk(acc[2], acc[3])};
      *(LAS u32x2*)(lds + L_T + w * T_STR + n16 * 40 + g * 8) = pk;
      wg_barrier();
    } else {
      bf16x8 mb[4];
#pragma unroll
      for (int q = 0; q < 4; ++q) { int ks = min(7 * c + 2 * q + (g >> 1), 13); mb[q] = *(LAS bf16x8*)(lds + L_M + ks * 512 + (2 * hb + (g & 1)) * 128 + i8 * 16); }
      if (OUT) {
#pragma unroll
        for (int k = 0; k < 7; ++k) { vt1 = SMFMAC(af[k], wb[2 * k], vt1, idx); vt2 = SMFMAC(af[k], wb[2 * k + 1], vt2, idx); }
      }
      asm volatile("" : "+v"(vt1), "+v"(vt2));
      SBAR();
      __builtin_amdgcn_s_setprio(3);
      wg_barrier();
      f32x4 d2 = {0, 0, 0, 0}, d3 = {0, 0, 0, 0};
      bf16x8 kt[4];
#pragma unroll
      for (int q = 0; q < 4; ++q) {
        LAS char* b = lds + L_T + (2 * q + (g >> 1)) * T_STR + (8 * (g & 1) + (n16 >> 2)) * 40 + (n16 & 3) * 8;
        s16x4 lo = __builtin_amdgcn_ds_read_tr16_b64_v4i16((LAS s16x4*)b);
        s16x4 hi = __builtin_amdgcn_ds_read_tr16_b64_v4i16((LAS s16x4*)(b + 160));
        u32x2 l2 = __builtin_bit_cast(u32x2, lo), h2 = __builtin_bit_cast(u32x2, hi);
        kt[q] = __builtin_bit_cast(bf16x8, (u32x4){l2.x, l2.y, h2.x, h2.y});
      }
      d2 = MFMA16(kt[0], mb[0], d2); d3 = MFMA16(kt[1], mb[1], d3); d2 = MFMA16(kt[2], mb[2], d2); d3 = MFMA16(kt[3], mb[3], d3);
      u32x4 s2;
#pragma unroll
      for (int r = 0; r < 4; ++r) { float f = d2[r] + d3[r]; auto t = __builtin_amdgcn_permlane32_swap(f2u(f), f2u(f), false, false);
        s2[r] = min(f2u(f + dpp_f<0x128>(u2f(t[1]))), 0xfffffffeu); }
      __builtin_amdgcn_raw_buffer_store_b128(s2, rx, st_mm, 0, 16);
      __builtin_amdgcn_s_setprio(0);
      CBAR();
#pragma unroll
      for (int q = 0; q < 4; ++q) __builtin_amdgcn_raw_buffer_store_b128(__builtin_bit_cast(u32x4, kt[q]), rktm, (q == 3 && g >= 2) ? OOB : st_ktm + q * 2048, 0, 16);
      st_mm += inc_mm; st_ktm += KVM_B;
    }
    if (OUT) {
      if (!W7) {
#pragma unroll
        for (int u = 0; u < 14; ++u) {
          if (u < KRW) { if (u & 1) vt2 = SMFMAC(af[u >> 1], wb[u], vt2, idx); else vt1 = SMFMAC(af[u >> 1], wb[u], vt1, idx);
            if (u >= KRW - XW && PRE + (u - (KRW - XW)) < NL) tw[PRE + (u - (KRW - XW))] = lds_wfrag(wl + (PRE + (u - (KRW - XW))) * 2048); }
          else { if (u & 1) vt2 = SMFMAC(af[u >> 1], tw[u - KRW], vt2, idx); else vt1 = SMFMAC(af[u >> 1], tw[u - KRW], vt1, idx);
            if (u - KRW + PRE + XW < NL) tw[u - KRW + PRE + XW] = lds_wfrag(wl + (u - KRW + PRE + XW) * 2048); }
          SBAR();
        }
      }
      f32x4 vt = comb32(vt1, vt2);
      u32x2 vpk = {cvt_pk(vt[0], vt[1]), cvt_pk(vt[2], vt[3])};
      if (__builtin_expect(s + 1 < NSEG, 1)) {
        bf16x8 va = __builtin_bit_cast(bf16x8, (u32x4){vpk.x, vpk.y, 0u, 0u});
        if (W7) { __builtin_amdgcn_s_sleep(DM7); } else { __builtin_amdgcn_s_sleep(DM0); }
        CBAR();
        u32x4 m0 = LD16(rx, off_mm), m1 = LD16(rx, off_mm + 512), axu = LD16(rx, off_ax);
        u32x2 ms = LD8(rx, off_ms);
        while (__builtin_expect(isbad(max(mx4(m0), mx4(m1))), 0)) { if (--budget < 0) break; CBAR(); m0 = LD16(rx, off_mm); m1 = LD16(rx, off_mm + 512); }
        while (__builtin_expect(isbad(umax3(mx4(axu), ms.x, ms.y)), 0)) { if (--budget < 0) break; CBAR(); axu = LD16(rx, off_ax); ms = LD8(rx, off_ms); }
        off_mm += XCH_B; off_ms += XCH_B; off_ax += XCH_B;
        const float mx = u2f(ms.x), sumx = u2f(ms.y);
        f32x4 mv = __builtin_bit_cast(f32x4, m0) + __builtin_bit_cast(f32x4, m1);
        float mm = fmaxf(fmax3(mv[0], mv[1], mv[2]), fmaxf(mv[3], mx));
        mm = x16_max(mm);
        float em[4];
#pragma unroll
        for (int r = 0; r < 4; ++r) em[r] = __builtin_amdgcn_exp2f(mv[r] - mm);
        float fx = __builtin_amdgcn_exp2f(mx - mm);
        unsigned pm0 = cvt_pk(em[0], em[1]), pm1 = cvt_pk(em[2], em[3]);
        bf16x8 pm = __builtin_bit_cast(bf16x8, (u32x4){own_m ? pm0 : 0u, own_m ? pm1 : 0u, 0u, 0u});
        f32x4 axs = __builtin_bit_cast(f32x4, axu) * fx;
        f32x4 am = MFMA16(va, pm, axs);
        float summ = x16_sum((em[0] + em[1]) + (em[2] + em[3]));
        float rinv = __builtin_amdgcn_rcpf(fx * sumx + summ);
        f32x4 r4;
#pragma unroll
        for (int r = 0; r < 4; ++r) { float t = am[r] * rinv; r4[r] = t + dpp_f<0x128>(t); }
        *(LAS f32x4*)(lds + L_P + w * P_STR + n16 * 64 + g * 16) = r4;
        __builtin_amdgcn_raw_buffer_store_b64(vpk, rvtm, st_vtm, 0, 16);
        wg_barrier();
        if (w == 0) {
          LAS char* b = lds + L_P + (l & 1) * 4 * P_STR + (l >> 3) * 64 + gq * 16;
          f32x4 t0 = *(LAS f32x4*)b, t1 = *(LAS f32x4*)(b + P_STR), t2 = *(LAS f32x4*)(b + 2 * P_STR), t3 = *(LAS f32x4*)(b + 3 * P_STR);
          f32x4 t = (t0 + t1) + (t2 + t3);
#pragma unroll
          for (int r = 0; r < 4; ++r) t[r] += dpp_f<0xB1>(t[r]);
          unsigned p0 = min(cvt_pk(t[0], t[1]), 0xfffffffeu), p1 = min(cvt_pk(t[2], t[3]), 0xfffffffeu);
          unsigned q0 = dpp_u<0x4E>(p0), q1 = dpp_u<0x4E>(p1);
          __builtin_amdgcn_raw_buffer_store_b128((u32x4){p0, p1, q0, q1}, rmem, st_mem, 0, 16);
          st_mem += inc_mem;
        }
      } else {
        __builtin_amdgcn_raw_buffer_store_b64(vpk, rvtm, st_vtm, 0, 16);
      }
      st_vtm += KVM_B;
    } else {
      wg_barrier();
    }
  }
  drain_barrier();
  if (W7 && l == 0) flag_add(flag_ptr(p, F_CM, NSEG - 1));
}

constexpr int H_P = 0, HP_STR = 1056, H_E = 7 * HP_STR + 32;
DI void helper_wave(const Params& p, const int h, LAS char* lds, const int w, const int l) {
  const int n16 = l & 15, g = l >> 4, hb = n16 >> 3, i8 = n16 & 7;
  const __amdgpu_buffer_rsrc_t rmem = __builtin_amdgcn_make_buffer_rsrc(p.ws + WS_MEMIMG, 0, (NSEG + 1) * MEMIMG_B, 0x00020000);
  const __amdgpu_buffer_rsrc_t rx = __builtin_amdgcn_make_buffer_rsrc(p.ws + WS_XCH, 0, NSEG * XCH_B, 0x00020000);
  const int wk = w < 7 ? w : 0;
  unsigned off_mem = (wk + 7 * hb) * 512 + g * 128 + i8 * 16;
  const __amdgpu_buffer_rsrc_t rk = ws_rsrc(p, WS_KT, (size_t)SEQ * NKT * 2), ru = ws_rsrc(p, WS_UT, (size_t)SEQ * NKT * 2);
  const unsigned kp = (unsigned)((h * 28 + wk) * 64 + l) * 16, up = (unsigned)((h * 28 + w) * 64 + l) * 16;
  unsigned st_ax = n16 < 8 ? XA_OFF + (w * 8 + (h >> 1)) * 1024 + (g * 16 + (h & 1) * 8 + n16) * 16 : OOB;
  const unsigned inc_ax = n16 < 8 ? XCH_B : 0;
  unsigned st_ms = l < 8 ? XS_OFF + (h >> 1) * 128 + ((h & 1) * 8 + l) * 8 : OOB;
  const unsigned inc_ms = l < 8 ? XCH_B : 0;
  bf16x8 k00, k01, k10, k11, u[4];
  auto prefetch = [&](int s) {
    flag_wait(flag_ptr(p, F_XG, s), 56);
    const unsigned k = kp + (unsigned)s * (SEG * NKT * 2), q = up + (unsigned)s * (SEG * NKT * 2);
    k00 = LDF(rk, k); k01 = LDF(rk, k + 7 * 1024); k10 = LDF(rk, k + 14 * 1024); k11 = LDF(rk, k + 21 * 1024);
    u[0] = LDF(ru, q); u[1] = LDF(ru, q + 8 * 1024); u[2] = LDF(ru, q + 16 * 1024); u[3] = LDF(ru, q + (w < 4 ? 24 : 16) * 1024);
  };
  prefetch(0);
  int budget = 1 << 20;
  for (int s = 0; s + 1 < NSEG; ++s) {
    if (w < 7) {
      if (s) __builtin_amdgcn_s_sleep(DH);
      CBAR();
      u32x4 v = LD16(rmem, off_mem);
      while (__builtin_expect(isbad(mx4(v)), 0)) { if (--budget < 0) break; __builtin_amdgcn_s_sleep(1); CBAR(); v = LD16(rmem, off_mem); }
      off_mem += MEMIMG_B;
      bf16x8 f = __builtin_bit_cast(bf16x8, v);
      f32x4 z = {0, 0, 0, 0};
      f32x4 a0 = MFMA16(k00, f, z), a1 = MFMA16(k01, f, z), b0 = MFMA16(k10, f, z), b1 = MFMA16(k11, f, z);
      f32x4 r;
#pragma unroll
      for (int q = 0; q < 4; ++q) { float own = hb ? b1[q] : a0[q]; float oth = hb ? a1[q] : b0[q]; r[q] = own + dpp_f<0x128>(oth); }
      *(LAS f32x4*)(lds + H_P + w * HP_STR + l * 16) = r;
    }
    wg_barrier();
    if (w == 0) {
      f32x4 t[7];
#pragma unroll
      for (int k = 0; k < 7; ++k) t[k] = *(LAS f32x4*)(lds + H_P + k * HP_STR + l * 16);
      f32x4 sc = ((t[0] + t[1]) + (t[2] + t[3])) + ((t[4] + t[5]) + t[6]);
      float mx = fmaxf(fmax3(sc[0], sc[1], sc[2]), sc[3]);
      mx = fmaxf(mx, dpp_f<0x128>(mx)); mx = x32_max(mx); mx = x16_max(mx);
      float e0 = __builtin_amdgcn_exp2f(sc[0] - mx), e1 = __builtin_amdgcn_exp2f(sc[1] - mx), e2 = __builtin_amdgcn_exp2f(sc[2] - mx), e3 = __builtin_amdgcn_exp2f(sc[3] - mx);
      *(LAS u32x2*)(lds + H_E + (g * 16 + i8) * 16 + hb * 8) = (u32x2){cvt_pk(e0, e1), cvt_pk(e2, e3)};
      wg_barrier();
      float sm = (e0 + e1) + (e2 + e3);
      sm += dpp_f<0x128>(sm); sm = x32_sum(sm); sm = x16_sum(sm);
      __builtin_amdgcn_raw_buffer_store_b64((u32x2){min(f2u(mx), 0xfffffffeu), min(f2u(sm), 0xfffffffeu)}, rx, st_ms, 0, 16);
      st_ms += inc_ms;
    } else {
      wg_barrier();
    }
    bf16x8 pe = *(LAS bf16x8*)(lds + H_E + (g * 16 + i8) * 16);
    f32x4 z = {0, 0, 0, 0};
#pragma unroll
    for (int j = 0; j < 4; ++j) {
      f32x4 d = MFMA16(u[j], pe, z);
      u32x4 sv = {min(f2u(d[0]), 0xfffffffeu), min(f2u(d[1]), 0xfffffffeu), min(f2u(d[2]), 0xfffffffeu), min(f2u(d[3]), 0xfffffffeu)};
      if (j < 3 || w < 4) __builtin_amdgcn_raw_buffer_store_b128(sv, rx, j == 0 ? st_ax : (n16 < 8 ? st_ax + j * 65536 : OOB), 0, 16);
    }
    st_ax += inc_ax;
    CBAR();
    prefetch(s + 1);
  }
}

DI void comb_row(const Params& p, int row, int l) {
  const bool act = l < 56;
  const __amdgpu_buffer_rsrc_t rpo = ws_rsrc(p, WS_PO, (size_t)4 * SEQ * DM * 4), rh = ws_rsrc(p, WS_H, (size_t)SEQ * DM * 4), rhn = ws_rsrc(p, WS_HN, (size_t)SEQ * DM * 2);
  float v[8];
  const unsigned off = act ? (unsigned)(row * DM + l * 8) * 4 : OOB;
  u32x4 t = act ? *(const u32x4*)(p.x + (size_t)row * DM + l * 8) : (u32x4){0, 0, 0, 0};
  f32x4 a = {0, 0, 0, 0}, b = a;
#pragma unroll
  for (int k = 0; k < 4; ++k) {
    a += __builtin_bit_cast(f32x4, LD16(rpo, act ? off + k * (SEQ * DM * 4) : OOB));
    b += __builtin_bit_cast(f32x4, LD16(rpo, act ? off + k * (SEQ * DM * 4) + 16 : OOB));
  }
#pragma unroll
  for (int j = 0; j < 2; ++j) { v[2 * j] = blo(t[j]) + a[2 * j]; v[2 * j + 1] = bhi(t[j]) + a[2 * j + 1]; v[4 + 2 * j] = blo(t[2 + j]) + b[2 * j]; v[5 + 2 * j] = bhi(t[2 + j]) + b[2 * j + 1]; }
  ST16(((u32x4){f2u(v[0]), f2u(v[1]), f2u(v[2]), f2u(v[3])}), rh, off);
  ST16(((u32x4){f2u(v[4]), f2u(v[5]), f2u(v[6]), f2u(v[7])}), rh, act ? off + 16 : OOB);
  u32x4 r = ln_row(v, act);
  ST16(r, rhn, act ? tiled_off(row, l) : OOB);
}

DI void att_wg(const Params& p, LAS char* lds, int s, int hg, int w, int l) {
  const int n16 = l & 15, g = l >> 4;
  const int hl = w >> 1, qt = w & 1, h = hg * 4 + hl;
  const __amdgpu_buffer_rsrc_t rk = ws_rsrc(p, WS_KT, (size_t)SEQ * NKT * 2), ru = ws_rsrc(p, WS_UT, (size_t)SEQ * NKT * 2);
  const __amdgpu_buffer_rsrc_t rkm = ws_rsrc(p, WS_KTM, (size_t)NSEG * KVM_B), rvm = ws_rsrc(p, WS_VTM, (size_t)NSEG * KVM_B);
  const __amdgpu_buffer_rsrc_t rpo = ws_rsrc(p, WS_PO, (size_t)4 * SEQ * DM * 4);
  const bf16x8* XF = (const bf16x8*)(p.ws + WS_XF) + ((size_t)(s * 2 + qt) * 14) * 64 + l;
  bf16x8 q[14];
#pragma unroll
  for (int ks = 0; ks < 14; ++ks) q[ks] = XF[ks * 64];
  flag_wait(flag_ptr(p, F_XG, s), 56);
  const unsigned ko = (unsigned)(((s * NH + h) * 28) * 64 + l) * 16;
  f32x4 s0 = {0, 0, 0, 0}, s1 = s0, sm = s0;
#pragma unroll
  for (int ks = 0; ks < 14; ++ks) s0 = MFMA16(LDF(rk, ko + ks * 1024), q[ks], s0);
  if (qt) {
#pragma unroll
    for (int ks = 0; ks < 14; ++ks) s1 = MFMA16(LDF(rk, ko + (14 + ks) * 1024), q[ks], s1);
  }
  bf16x8 uv[4][4];
  const unsigned uo = (unsigned)(((s * NH + hg * 4) * 28 + w) * 64 + l) * 16;
#pragma unroll
  for (int j = 0; j < 4; ++j)
#pragma unroll
    for (int k = 0; k < 4; ++k) uv[j][k] = LDF(ru, uo + (k * 28 + ((j < 3 || w < 4) ? j * 8 : 0)) * 1024);
  flag_wait(flag_ptr(p, F_CM, s), 32);
  const unsigned kmo = (unsigned)s * KVM_B + (unsigned)(((h >> 1) * 14) * 64 + l) * 16;
  bf16x8 vm[4];
#pragma unroll
  for (int j = 0; j < 4; ++j) vm[j] = LDF(rvm, (unsigned)s * KVM_B + (unsigned)((hg * 28 + w + ((j < 3 || w < 4) ? j * 8 : 0)) * 64 + l) * 16);
#pragma unroll
  for (int ks = 0; ks < 14; ++ks) sm = MFMA16(LDF(rkm, kmo + ks * 1024), q[ks], sm);
  const float NEG = -3.0e38f;
  const bool ownm = (g >> 1) == (h & 1);
  float v[12];
#pragma unroll
  for (int r = 0; r < 4; ++r) {
    v[r] = (qt || (4 * g + r) <= n16) ? s0[r] : NEG;
    v[4 + r] = (qt && (4 * g + r) <= n16) ? s1[r] : NEG;
    v[8 + r] = ownm ? sm[r] : NEG;
  }
  float mx = NEG;
#pragma unroll
  for (int r = 0; r < 12; ++r) mx = fmaxf(mx, v[r]);
  mx = x16_max(mx); mx = x32_max(mx);
  float sum = 0.f;
#pragma unroll
  for (int r = 0; r < 12; ++r) { v[r] = v[r] > -1.0e38f ? __builtin_amdgcn_exp2f(v[r] - mx) : 0.f; sum += v[r]; }
  sum = x16_sum(sum); sum = x32_sum(sum);
  const float rinv = 1.0f / sum;
  u32x4 px = {cvt_pk(v[0] * rinv, v[1] * rinv), cvt_pk(v[2] * rinv, v[3] * rinv), cvt_pk(v[4] * rinv, v[5] * rinv), cvt_pk(v[6] * rinv, v[7] * rinv)};
  *(LAS u32x4*)(lds + (hl * 2 + qt) * 1024 + l * 16) = px;
  if (ownm) *(LAS u32x2*)(lds + 8192 + qt * 1024 + l * 16 + (hl >> 1) * 8) = (u32x2){cvt_pk(v[8] * rinv, v[9] * rinv), cvt_pk(v[10] * rinv, v[11] * rinv)};
  wg_barrier();
  bf16x8 P[2][5];
#pragma unroll
  for (int t = 0; t < 2; ++t) {
#pragma unroll
    for (int k = 0; k < 4; ++k) P[t][k] = *(LAS bf16x8*)(lds + (k * 2 + t) * 1024 + l * 16);
    P[t][4] = *(LAS bf16x8*)(lds + 8192 + t * 1024 + l * 16);
  }
  const unsigned po = (unsigned)((hg * SEQ + s * SEG + n16) * DM + 4 * g + w * 16) * 4;
#pragma unroll
  for (int j = 0; j < 4; ++j) {
    f32x4 d0 = {0, 0, 0, 0}, d1 = d0;
#pragma unroll
    for (int k = 0; k < 4; ++k) { d0 = MFMA16(uv[j][k], P[0][k], d0); d1 = MFMA16(uv[j][k], P[1][k], d1); }
    d0 = MFMA16(vm[j], P[0][4], d0); d1 = MFMA16(vm[j], P[1][4], d1);
    if (j < 3 || w < 4) { ST16(__builtin_bit_cast(u32x4, d0), rpo, po + j * 512); ST16(__builtin_bit_cast(u32x4, d1), rpo, po + j * 512 + 16 * DM * 4); }
  }
  drain_barrier();
  if (w == 0) {
    unsigned t = 0;
    if (l == 0) t = __hip_atomic_fetch_add(flag_ptr(p, F_AT, s), 1u, __ATOMIC_RELAXED, __HIP_MEMORY_SCOPE_AGENT);
    t = __builtin_amdgcn_readfirstlane(t);
    if (l == 0) *(LAS unsigned*)(lds + 10240) = t;
  }
  wg_barrier();
  const unsigned ticket = __builtin_amdgcn_readfirstlane(*(LAS unsigned*)(lds + 10240));
  if (ticket == 3) {
#pragma unroll
    for (int r = 0; r < 4; ++r) comb_row(p, s * SEG + w * 4 + r, l);
    drain_barrier();
    if (w == 0 && l == 0) flag_add(flag_ptr(p, F_HN, s));
  }
  wg_barrier();
}

DI void glu1_wg(const Params& p, int fb, int w, int l) {
  const int ftl = w & 1, rt = (w >> 1) & 1, sp = w >> 2;
  const int ft = fb * 2 + ftl;
  const bf16x8* WG = (const bf16x8*)(p.ws + WS_WG) + (size_t)ft * 28 * 64 + l;
  const __amdgpu_buffer_rsrc_t rhn = ws_rsrc(p, WS_HN, (size_t)SEQ * DM * 2), rhg = ws_rsrc(p, WS_HG, (size_t)SEQ * FF * 2);
  bf16x8 wgt[14], wup[14];
#pragma unroll
  for (int ks = 0; ks < 14; ++ks) { wgt[ks] = WG[ks * 64]; wup[ks] = WG[(14 + ks) * 64]; }
  const int n16 = l & 15, g = l >> 4;
  for (int s = sp; s < NSEG; s += 2) {
    flag_wait(flag_ptr(p, F_HN, s), 1);
    const unsigned ho = (unsigned)(((s * 2 + rt) * 14) * 64 + l) * 16;
    bf16x8 x[14];
#pragma unroll
    for (int ks = 0; ks < 14; ++ks) x[ks] = LDF(rhn, ho + ks * 1024);
    f32x4 ag = {0, 0, 0, 0}, au = ag;
#pragma unroll
    for (int ks = 0; ks < 14; ++ks) { ag = MFMA16(wgt[ks], x[ks], ag); au = MFMA16(wup[ks], x[ks], au); }
    u32x2 o = {cvt_pk(ag[0] * au[0], ag[1] * au[1]), cvt_pk(ag[2] * au[2], ag[3] * au[3])};
    ST8(o, rhg, (unsigned)(((((s * 2 + rt) * 56 + fb) * 64 + (2 * ftl + (g >> 1)) * 16 + n16) * 8 + 4 * (g & 1)) * 2));
    asm volatile("s_waitcnt vmcnt(0)" ::: "memory");
    if (l == 0) flag_add(flag_ptr(p, F_G1, s));
  }
}

DI void glu2_wg(const Params& p, LAS char* lds, int o, int w, int l) {
  const bf16x8* WD = (const bf16x8*)(p.ws + WS_WD) + ((size_t)o * 56 + 7 * w) * 64 + l;
  const __amdgpu_buffer_rsrc_t rhg = ws_rsrc(p, WS_HG, (size_t)SEQ * FF * 2), rh = ws_rsrc(p, WS_H, (size_t)SEQ * DM * 4);
  bf16x8 wd[7];
#pragma unroll
  for (int k = 0; k < 7; ++k) wd[k] = WD[k * 64];
  const int tid = w * 64 + l;
  int buf = 0;
  for (int s = 0; s < NSEG; ++s) {
    flag_wait(flag_ptr(p, F_G1, s), 56 * 4);
    const unsigned ho = (unsigned)(((s * 2) * 56 + 7 * w) * 64 + l) * 16;
    bf16x8 a[7], b[7];
#pragma unroll
    for (int k = 0; k < 7; ++k) { a[k] = LDF(rhg, ho + k * 1024); b[k] = LDF(rhg, ho + (56 + k) * 1024); }
    const int row = s * SEG + ((tid >> 6) & 1) * 16 + (tid & 15), col = o * 16 + 4 * ((tid >> 4) & 3);
    u32x4 hv = {0, 0, 0, 0};
    if (tid < 128) hv = LD16(rh, (unsigned)(row * DM + col) * 4);
    f32x4 d0 = {0, 0, 0, 0}, d1 = d0;
#pragma unroll
    for (int k = 0; k < 7; ++k) { d0 = MFMA16(wd[k], a[k], d0); d1 = MFMA16(wd[k], b[k], d1); }
    LAS char* L = lds + buf * 16384;
    *(LAS f32x4*)(L + w * 2048 + l * 16) = d0;
    *(LAS f32x4*)(L + w * 2048 + 1024 + l * 16) = d1;
    wg_barrier();
    if (tid < 128) {
      f32x4 acc = __builtin_bit_cast(f32x4, hv);
#pragma unroll
      for (int k = 0; k < 8; ++k) acc += *(LAS f32x4*)(L + k * 2048 + tid * 16);
      *(u32x2*)(p.out + (size_t)row * DM + col) = (u32x2){cvt_pk(acc[0], acc[1]), cvt_pk(acc[2], acc[3])};
    }
    buf ^= 1;
  }
}

__global__ void __launch_bounds__(512, 2) k_main(Params p) {
  extern __shared__ __attribute__((aligned(16))) char smem[];
  LAS char* lds = (LAS char*)smem;
  const int xg = blockIdx.x & 7, r = blockIdx.x >> 3;
  const int w = __builtin_amdgcn_readfirstlane(threadIdx.x >> 6), l = threadIdx.x & 63;
  if (xg == 0) {
    const int cw = r;
    if (cw < 28) { if (w == 7) chain_wave<true, true>(p, cw, lds, w, l); else chain_wave<false, true>(p, cw, lds, w, l); }
    else { if (w == 7) chain_wave<true, false>(p, cw, lds, w, l); else chain_wave<false, false>(p, cw, lds, w, l); }
    return;
  }
  if (xg == 1 && r < 16) { helper_wave(p, r, lds, w, l); return; }
  const int wi = xg == 1 ? 192 + (r - 16) : (xg - 2) * 32 + r;
  if (wi < 168) { xgemm_wg(p, lds, wi % 56, wi / 56, 3, w, l); wg_barrier(); }
  if (wi < 56) { glu1_wg(p, wi, w, l); return; }
  if (wi >= 168 && wi < 196) { glu2_wg(p, lds, wi - 168, w, l); return; }
  const int ai = wi < 168 ? wi - 56 : 112 + (wi - 196);
  for (int s = ai >> 2; s < NSEG; s += 31) att_wg(p, lds, s, ai & 3, w, l);
}

extern "C" void kernel_launch(void* const* d_in, const int* in_sizes, int n_in,
                              void* d_out, int out_size, void* d_ws, size_t ws_size,
                              hipStream_t stream) {
  Params p;
  p.x = (const uint16_t*)d_in[0]; p.mem0 = (const uint16_t*)d_in[1];
  p.Wq = (const uint16_t*)d_in[2]; p.Wk = (const uint16_t*)d_in[3]; p.Wv = (const uint16_t*)d_in[4]; p.Wo = (const uint16_t*)d_in[5];
  p.wg = (const uint16_t*)d_in[8]; p.wu = (const uint16_t*)d_in[9]; p.wd = (const uint16_t*)d_in[10];
  p.out = (uint16_t*)d_out; p.ws = (char*)d_ws;
  (void)hipFuncSetAttribute((const void*)k_main, hipFuncAttributeMaxDynamicSharedMemorySize, CHAIN_LDS);
  k_prep<<<512, 256, 0, stream>>>(p);
  k_wt<<<392, 256, 0, stream>>>(p);
  k_main<<<256, 512, CHAIN_LDS, stream>>>(p);
}
```

```cpp
#include <hip/hip_runtime.h>
#include <hip/hip_bf16.h>
#include <stdint.h>

typedef __bf16 bf16x8 __attribute__((ext_vector_type(8)));
typedef __bf16 bf16x16 __attribute__((ext_vector_type(16)));
typedef __bf16 bf16x2 __attribute__((ext_vector_type(2)));
typedef float f32x4 __attribute__((ext_vector_type(4)));
typedef unsigned u32x4 __attribute__((ext_vector_type(4)));
typedef unsigned u32x2 __attribute__((ext_vector_type(2)));
typedef unsigned u32x8 __attribute__((ext_vector_type(8)));
typedef short s16x4 __attribute__((ext_vector_type(4)));
#define LAS __attribute__((address_space(3)))
#define DI __device__ __forceinline__

constexpr int DM = 448, NH = 16, HD = 256, NSEG = 64, SEG = 32, SEQ = 2048, NTOT = 14336, NKT = 7168, FF = 1792;
constexpr int MEMIMG_B = 7168;
constexpr int XM_OFF = 0, XS_OFF = 8192, XA_OFF = 9216, XCH_B = 9216 + 28 * 4096;
constexpr int KVM_B = NH * 8 * DM * 2;

constexpr int KR = 5, PRE0 = 4, XW = 1;
#define TUNE TM = 1900, T2 = 4300, TH = 4200, DB = 4, XSTAG = 8000
constexpr int TUNE;

constexpr int L_M = 0, L_T = 7168, T_STR = 640, L_P = L_T + 8 * T_STR, P_STR = 1056, L_WB = L_P + 8 * P_STR + 64;
constexpr int L_WB_AL = (L_WB + 255) & ~255;
constexpr int CHAIN_LDS = L_WB_AL + 7 * (14 - KR) * 2048;

constexpr size_t WS_MEMIMG = 0;
constexpr size_t WS_XCH = WS_MEMIMG + (size_t)(NSEG + 1) * MEMIMG_B;
constexpr size_t WS_SENT_END = WS_XCH + (size_t)NSEG * XCH_B;
constexpr size_t WS_XF = (WS_SENT_END + 4095) & ~(size_t)4095;
constexpr size_t WS_WT = WS_XF + (size_t)SEQ * DM * 2;
constexpr size_t WS_KT = WS_WT + (size_t)NTOT * DM * 2;
constexpr size_t WS_UT = WS_KT + (size_t)SEQ * NKT * 2;
constexpr size_t WS_KTM = WS_UT + (size_t)SEQ * NKT * 2;
constexpr size_t WS_VTM = WS_KTM + (size_t)NSEG * KVM_B;
constexpr size_t WS_PO = WS_VTM + (size_t)NSEG * KVM_B;
constexpr size_t WS_H = WS_PO + (size_t)4 * SEQ * DM * 4;
constexpr size_t WS_HN = WS_H + (size_t)SEQ * DM * 4;
constexpr size_t WS_WG = WS_HN + (size_t)SEQ * DM * 2;
constexpr size_t WS_WD = WS_WG + (size_t)2 * FF * DM * 2;
constexpr size_t WS_HG = WS_WD + (size_t)FF * DM * 2;
constexpr size_t WS_FLG = WS_HG + (size_t)SEQ * FF * 2;
constexpr size_t WS_END = WS_FLG + 6 * NSEG * 128;
constexpr int F_XG = 0, F_CM = 1, F_AT = 2, F_HN = 3, F_G1 = 4, F_Q = 5;
#define DSTAMP(c, k)

struct Params {
  const uint16_t* x; const uint16_t* mem0; const uint16_t* Wq; const uint16_t* Wk; const uint16_t* Wv; const uint16_t* Wo;
  const uint16_t* wg; const uint16_t* wu; const uint16_t* wd; uint16_t* out; char* ws;
};

DI unsigned cvt_pk(float a, float b) { bf16x2 r = {(__bf16)a, (__bf16)b}; return __builtin_bit_cast(unsigned, r); }
DI void wg_barrier() { asm volatile("s_waitcnt lgkmcnt(0)\n\ts_barrier" ::: "memory"); }
DI unsigned umax3(unsigned a, unsigned b, unsigned c) { return max(max(a, b), c); }
DI float fmax3(float a, float b, float c) { return fmaxf(fmaxf(a, b), c); }
DI float u2f(unsigned u) { return __builtin_bit_cast(float, u); }
DI unsigned f2u(float f) { return __builtin_bit_cast(unsigned, f); }
DI float blo(unsigned u) { return u2f(u << 16); }
DI float bhi(unsigned u) { return u2f(u & 0xffff0000u); }
template<int CTRL> DI float dpp_f(float x) { return u2f((unsigned)__builtin_amdgcn_update_dpp(0, (int)f2u(x), CTRL, 0xf, 0xf, true)); }
template<int CTRL> DI unsigned dpp_u(unsigned x) { return (unsigned)__builtin_amdgcn_update_dpp(0, (int)x, CTRL, 0xf, 0xf, true); }
DI float x32_max(float x) { auto r = __builtin_amdgcn_permlane32_swap(f2u(x), f2u(x), false, false); return fmaxf(u2f(r[0]), u2f(r[1])); }
DI float x32_sum(float x) { auto r = __builtin_amdgcn_permlane32_swap(f2u(x), f2u(x), false, false); return u2f(r[0]) + u2f(r[1]); }
DI float x16_max(float x) { auto r = __builtin_amdgcn_permlane16_swap(f2u(x), f2u(x), false, false); return fmaxf(u2f(r[0]), u2f(r[1])); }
DI float x16_sum(float x) { auto r = __builtin_amdgcn_permlane16_swap(f2u(x), f2u(x), false, false); return u2f(r[0]) + u2f(r[1]); }
DI float wave_sum(float v) {
  v += dpp_f<0xB1>(v); v += dpp_f<0x4E>(v); v += dpp_f<0x124>(v); v += dpp_f<0x128>(v);
  v = x16_sum(v); v = x32_sum(v); return v;
}
#define SMFMAC(a, b, c, idx) __builtin_amdgcn_smfmac_f32_16x16x64_bf16((a), (b), (c), (idx), 0, 0)
#define MFMA16(a, b, c) __builtin_amdgcn_mfma_f32_16x16x32_bf16((a), (b), (c), 0, 0, 0)
constexpr unsigned OOB = 0x7fffffffu;
#define LD16(r, off) __builtin_amdgcn_raw_buffer_load_b128((r), (off), 0, 16)
#define LD8(r, off) __builtin_amdgcn_raw_buffer_load_b64((r), (off), 0, 16)
#define CBAR() asm volatile("" ::: "memory")
#define SBAR() __builtin_amdgcn_sched_barrier(0)

DI unsigned* flag_ptr(const Params& p, int kind, int s) { return (unsigned*)(p.ws + WS_FLG + (size_t)(kind * NSEG + s) * 128); }
DI void flag_add(unsigned* f) { __hip_atomic_fetch_add(f, 1u, __ATOMIC_RELAXED, __HIP_MEMORY_SCOPE_AGENT); }
DI void flag_wait(const unsigned* f, unsigned want) {
  int budget = 1 << 20;
  while ((unsigned)__builtin_amdgcn_readfirstlane(__hip_atomic_load(f, __ATOMIC_RELAXED, __HIP_MEMORY_SCOPE_AGENT)) < want) {
    if (--budget < 0) break;
    __builtin_amdgcn_s_sleep(8);
  }
  asm volatile("" ::: "memory");
}
DI void drain_barrier() { asm volatile("s_waitcnt vmcnt(0) lgkmcnt(0)\n\ts_barrier" ::: "memory"); }
DI __amdgpu_buffer_rsrc_t ws_rsrc(const Params& p, size_t off, size_t bytes) { return __builtin_amdgcn_make_buffer_rsrc(p.ws + off, 0, (int)bytes, 0x00020000); }
#define LDF(r, off) __builtin_bit_cast(bf16x8, LD16((r), (off)))
#define ST16(v, r, off) __builtin_amdgcn_raw_buffer_store_b128((v), (r), (off), 0, 16)
#define ST8(v, r, off) __builtin_amdgcn_raw_buffer_store_b64((v), (r), (off), 0, 16)

DI u32x4 ln_row(float (&v)[8], bool act) {
  float s = 0.f;
#pragma unroll
  for (int j = 0; j < 8; ++j) s += v[j];
  s = wave_sum(act ? s : 0.f);
  const float mean = s * (1.0f / DM);
  float q = 0.f;
#pragma unroll
  for (int j = 0; j < 8; ++j) { float d = v[j] - mean; q += d * d; }
  q = wave_sum(act ? q : 0.f);
  const float rstd = 1.0f / sqrtf(q * (1.0f / DM) + 1e-5f);
  u32x4 r;
#pragma unroll
  for (int j = 0; j < 4; ++j) r[j] = cvt_pk((v[2 * j] - mean) * rstd, (v[2 * j + 1] - mean) * rstd);
  return r;
}
DI unsigned tiled_off(int row, int c8) { return (unsigned)((((row >> 4) * 14 + (c8 >> 2)) * 64 + (c8 & 3) * 16 + (row & 15)) * 16); }

constexpr int WQ_STR = 144, WP_OFF = 224 * WQ_STR, WO_STR = 240, WBUF = 336 * WQ_STR + 4608;
template<bool isB>
DI void wt_wg(const Params& p, LAS char* lds, int task, int w, int l) {
  const int n16 = l & 15, g = l >> 4, tid = w * 64 + l;
  const int mat = task >> 3, qe = (task >> 2) & 1, qn = task & 3;
  const int h = mat & 15;
  const uint16_t* Qg = (isB ? p.Wv : p.Wk) + (size_t)(qe * 224) * (NH * HD) + h * HD;
  const uint16_t* Pg = isB ? p.Wo + (size_t)h * HD * DM + qn * 112 : p.Wq + (size_t)(qn * 112) * (NH * HD) + h * HD;
  u32x4 r[4][6];
  const uint16_t* src[6]; int loff[6];
#pragma unroll
  for (int i = 0; i < 6; ++i) {
    const int c = min(i * 512 + tid, 2687), c2 = c - 1792;
    src[i] = c < 1792 ? Qg + (size_t)(c >> 3) * (NH * HD) + (c & 7) * 8
           : (isB ? Pg + (size_t)(c2 / 14) * DM + (c2 % 14) * 8 : Pg + (size_t)(c2 >> 3) * (NH * HD) + (c2 & 7) * 8);
    loff[i] = (c < 1792 || !isB) ? (c >> 3) * WQ_STR + (c & 7) * 16 : WP_OFF + (c2 / 14) * WO_STR + (c2 % 14) * 16;
  }
  const int strP = isB ? 64 * DM : 64, str3 = tid < 256 ? 64 : strP;
  const int rot = (qn + 2 * qe) & 3;
  auto gload = [&](int j) {
    const int kk = (j + rot) & 3;
#pragma unroll
    for (int i = 0; i < 6; ++i) r[j][i] = *(const u32x4*)(src[i] + kk * (i < 3 ? 64 : (i == 3 ? str3 : strP)));
  };
  gload(0);
  const int we = w >> 2, wn = w & 3;
  f32x4 acc[8][2];
#pragma unroll
  for (int a = 0; a < 8; ++a) { acc[a][0] = (f32x4){0, 0, 0, 0}; acc[a][1] = (f32x4){0, 0, 0, 0}; }
  const int qrow = 8 * (n16 >> 2) + (n16 & 3);
  gload(1); gload(2); gload(3);
  DSTAMP(blockIdx.x == 40 && tid == 0, 76)
#pragma unroll 1
  for (int it = 0; it < 2; ++it) {
    if (it) wg_barrier();
#pragma unroll
    for (int i = 0; i < 6; ++i) { *(LAS u32x4*)(lds + loff[i]) = r[0][i]; *(LAS u32x4*)(lds + WBUF + loff[i]) = r[1][i]; }
    wg_barrier();
    DSTAMP(blockIdx.x == 40 && tid == 0, 74 + it)
#pragma unroll 2
    for (int k4 = 0; k4 < 4; ++k4) {
      LAS char* L = lds + (k4 >> 1) * WBUF;
      const int k = k4 & 1;
      bf16x8 pb[2];
#pragma unroll
      for (int c = 0; c < 2; ++c) {
        const int nt = wn + 4 * c;
        if (isB) {
          LAS char* bb = L + WP_OFF + (k * 32 + 8 * g + (n16 >> 2)) * WO_STR + nt * 32 + (n16 & 3) * 8;
          s16x4 lo = __builtin_amdgcn_ds_read_tr16_b64_v4i16((LAS s16x4*)bb);
          s16x4 hi = __builtin_amdgcn_ds_read_tr16_b64_v4i16((LAS s16x4*)(bb + 4 * WO_STR));
          u32x2 l2 = __builtin_bit_cast(u32x2, lo), h2 = __builtin_bit_cast(u32x2, hi);
          pb[c] = __builtin_bit_cast(bf16x8, (u32x4){l2.x, l2.y, h2.x, h2.y});
        } else {
          pb[c] = *(LAS bf16x8*)(L + WP_OFF + (nt * 16 + n16) * WQ_STR + k * 64 + g * 16);
        }
      }
#pragma unroll
      for (int a = 0; a < 8; ++a) {
        const int ep = 4 * we + (a >> 1);
        bf16x8 qa = *(LAS bf16x8*)(L + (ep * 32 + (a & 1) * 4 + qrow) * WQ_STR + k * 64 + g * 16);
        acc[a][0] = MFMA16(qa, pb[0], acc[a][0]); acc[a][1] = MFMA16(qa, pb[1], acc[a][1]);
      }
    }
#pragma unroll
    for (int i = 0; i < 6; ++i) { r[0][i] = r[2][i]; r[1][i] = r[3][i]; }
  }
  DSTAMP(blockIdx.x == 40 && tid == 0, 77)
  const float sc = isB ? 1.0f : 0.0625f * 1.4426950408889634f;
  const __amdgpu_buffer_rsrc_t rw = ws_rsrc(p, WS_WT, (size_t)NTOT * DM * 2);
#pragma unroll
  for (int c = 0; c < 2; ++c)
#pragma unroll
    for (int m = 0; m < 4; ++m) {
      if ((c < 1 || wn < 3) && (m < 3 || we == 0)) {
        const int nt = ((isB ? NKT : 0) + h * DM + qn * 112) / 16 + wn + 4 * c, ks = qe * 7 + 4 * we + m;
        const f32x4 x = acc[2 * m][c] * sc, y = acc[2 * m + 1][c] * sc;
        ST16(((u32x4){cvt_pk(x[0], x[1]), cvt_pk(x[2], x[3]), cvt_pk(y[0], y[1]), cvt_pk(y[2], y[3])}), rw, (unsigned)((nt * 14 + ks) * 64 + l) * 16);
      }
    }
}

DI void pre_phase(const Params& p, LAS char* lds, int b, int w, int l) {
  const int tid = w * 64 + l;
  const __amdgpu_buffer_rsrc_t rs = ws_rsrc(p, 0, WS_XF + (size_t)SEQ * DM * 2);
  const bool act = l < 56;
  const int row = b * 8 + w;
  u32x4 xt = act ? *(const u32x4*)(p.x + (size_t)row * DM + l * 8) : (u32x4){0, 0, 0, 0};
  const int task = (b & 7) * 32 + (b >> 3);
  if (task >= 128) wt_wg<true>(p, lds, task, w, l); else wt_wg<false>(p, lds, task, w, l);
  {
    float v[8];
#pragma unroll
    for (int j = 0; j < 4; ++j) { v[2 * j] = blo(xt[j]); v[2 * j + 1] = bhi(xt[j]); }
    u32x4 r = ln_row(v, act);
    ST16(r, rs, act ? (unsigned)WS_XF + tiled_off(row, l) : OOB);
  }
  if (tid < 32) {
    const int i = b * 32 + tid;
    ST16(((u32x4){~0u, ~0u, ~0u, ~0u}), rs, i < XCH_B / 16 ? (unsigned)WS_XCH + i * 16 : (unsigned)MEMIMG_B + (i - XCH_B / 16) * 16);
  }
  if (b == 9 && tid < 448) {
    u32x4 t = *(const u32x4*)(p.mem0 + (tid & 7) * DM + (tid >> 3) * 8);
    t.x = min(t.x, 0xfffffffeu); t.y = min(t.y, 0xfffffffeu); t.z = min(t.z, 0xfffffffeu); t.w = min(t.w, 0xfffffffeu);
    ST16(t, rs, tid * 16);
  }
  DSTAMP(b == 40 && tid == 0, 72)
  drain_barrier();
  DSTAMP(b == 40 && tid == 0, 73)
  if (w == 0) {
    if (l == 0) {
      const unsigned t = __hip_atomic_fetch_add(flag_ptr(p, F_Q, 8 + (b & 7)), 1u, __ATOMIC_RELAXED, __HIP_MEMORY_SCOPE_AGENT);
      if (t == 31) flag_add(flag_ptr(p, F_Q, 16));
    }
    int budget = 1 << 20;
    while ((unsigned)__builtin_amdgcn_readfirstlane(__hip_atomic_load(flag_ptr(p, F_Q, 16), __ATOMIC_RELAXED, __HIP_MEMORY_SCOPE_AGENT)) < 8u) {
      if (--budget < 0) break;
      __builtin_amdgcn_s_sleep(2);
    }
  }
  wg_barrier();
}

DI void xgemm_wg(const Params& p, LAS char* lds, int nb, int seg0, int step, int w, int l) {
  const int n16 = l & 15, g = l >> 4;
  const int n0 = nb * 256 + w * 32;
  const bool isU = n0 >= NKT;
  const int r0 = isU ? n0 + n16 : n0 + 8 * (n16 >> 2) + (n16 & 3);
  const int r1 = isU ? r0 + 16 : r0 + 4;
  const __amdgpu_buffer_rsrc_t rw = ws_rsrc(p, WS_WT, (size_t)NTOT * DM * 2), rxf = ws_rsrc(p, WS_XF, (size_t)SEQ * DM * 2);
  const unsigned W0 = (unsigned)((r0 >> 4) * 14 * 64 + g * 16 + (r0 & 15)) * 16, W1 = (unsigned)((r1 >> 4) * 14 * 64 + g * 16 + (r1 & 15)) * 16;
  bf16x8 w0[14], w1[14];
#pragma unroll
  for (int ks = 0; ks < 14; ++ks) { w0[ks] = LDF(rw, W0 + ks * 1024); w1[ks] = LDF(rw, W1 + ks * 1024); }
  const int nn = isU ? n0 - NKT : n0;
  const int hh = nn / DM, cc = nn % DM;
  const int tid = w * 64 + l;
  u32x4 pre[4];
  auto gload = [&](int s) {
#pragma unroll
    for (int i = 0; i < 4; ++i) { const int c = i * 512 + tid; pre[i] = LD16(rxf, c < 1792 ? (unsigned)(s * 1792 + c) * 16 : OOB); }
  };
  gload(seg0);
  int buf = 0, prev = -1;
  const __amdgpu_buffer_rsrc_t rout = ws_rsrc(p, isU ? WS_UT : WS_KT, (size_t)SEQ * NKT * 2);
  const __amdgpu_buffer_rsrc_t rsent = ws_rsrc(p, 0, WS_SENT_END);
  for (int s = seg0; s < NSEG; s += step) {
    LAS char* L = lds + buf * 28672;
#pragma unroll
    for (int i = 0; i < 4; ++i) { const int c = i * 512 + tid; if (c < 1792) *(LAS u32x4*)(L + c * 16) = pre[i]; }
    asm volatile("s_waitcnt vmcnt(0)" ::: "memory");
    if (s + step < NSEG) gload(s + step);
    wg_barrier();
    if (tid == 0 && prev >= 0) flag_add(flag_ptr(p, F_XG, prev));
    f32x4 a00 = {0, 0, 0, 0}, a01 = a00, a10 = a00, a11 = a00;
#pragma unroll
    for (int ks = 0; ks < 14; ++ks) {
      bf16x8 x0 = *(LAS bf16x8*)(L + ks * 1024 + l * 16), x1 = *(LAS bf16x8*)(L + (14 + ks) * 1024 + l * 16);
      if (isU) { a00 = MFMA16(x0, w0[ks], a00); a01 = MFMA16(x1, w0[ks], a01); a10 = MFMA16(x0, w1[ks], a10); a11 = MFMA16(x1, w1[ks], a11); }
      else     { a00 = MFMA16(w0[ks], x0, a00); a01 = MFMA16(w1[ks], x0, a01); a10 = MFMA16(w0[ks], x1, a10); a11 = MFMA16(w1[ks], x1, a11); }
    }
    u32x4 o0 = {cvt_pk(a00[0], a00[1]), cvt_pk(a00[2], a00[3]), cvt_pk(a01[0], a01[1]), cvt_pk(a01[2], a01[3])};
    u32x4 o1 = {cvt_pk(a10[0], a10[1]), cvt_pk(a10[2], a10[3]), cvt_pk(a11[0], a11[1]), cvt_pk(a11[2], a11[3])};
    {
      const unsigned off = (unsigned)((((s * NH + hh) * 28 + (isU ? (cc >> 4) : (cc >> 5))) * 64 + l) * 16);
      ST16(o0, rout, off); ST16(o1, rout, off + (isU ? 1024 : 14 * 1024));
    }
    if (s + 1 < NSEG) {
      const int i = nb * 147 + tid;
      if (tid < 147 && i < (XCH_B + MEMIMG_B) / 16) {
        const unsigned off = i < XCH_B / 16 ? (unsigned)(WS_XCH + (size_t)(s + 1) * XCH_B) + i * 16 : (unsigned)((s + 2) * MEMIMG_B) + (i - XCH_B / 16) * 16;
        ST16(((u32x4){~0u, ~0u, ~0u, ~0u}), rsent, off);
      }
    }
    prev = s;
    buf ^= 1;
  }
  drain_barrier();
  if (tid == 0 && prev >= 0) flag_add(flag_ptr(p, F_XG, prev));
}
DI bf16x16 load_wfrag(__amdgpu_buffer_rsrc_t rw, unsigned base, int k) {
  u32x2 alo = LD8(rw, base + k * 1024), ahi = LD8(rw, base + k * 1024 + 512);
  u32x2 blo_ = LD8(rw, base + (k + 7) * 1024), bhi_ = LD8(rw, base + (k + 7) * 1024 + 512);
  u32x8 r = {alo.x, blo_.x, alo.y, blo_.y, ahi.x, bhi_.x, ahi.y, bhi_.y};
  return __builtin_bit_cast(bf16x16, r);
}
DI f32x4 comb32(f32x4 a, f32x4 b) {
  f32x4 r;
#pragma unroll
  for (int i = 0; i < 4; ++i) { float fa = a[i], fb = b[i]; auto t = __builtin_amdgcn_permlane32_swap(f2u(fa), f2u(fb), false, false); r[i] = u2f(t[0]) + u2f(t[1]); }
  return r;
}
DI bf16x16 lds_wfrag(LAS char* d) { u32x4 t0 = *(LAS u32x4*)d, t1 = *(LAS u32x4*)(d + 1024);
  u32x8 t = {t0.x, t0.y, t0.z, t0.w, t1.x, t1.y, t1.z, t1.w}; return __builtin_bit_cast(bf16x16, t); }
DI unsigned now32() { return (unsigned)__builtin_amdgcn_s_memtime(); }
DI void wait_until(unsigned t0, unsigned d) { while (now32() - t0 < d) { } }
DI unsigned mx4(u32x4 v) { return max(umax3(v.x, v.y, v.z), v.w); }
DI bool isbad(unsigned m) { return __builtin_amdgcn_ballot_w64(m == 0xffffffffu) != 0; }

template<bool W7, bool OUT>
DI void chain_wave(const Params& p, const int cw, LAS char* lds, const int w, const int l) {
  constexpr int KRW = W7 ? 14 : KR;
  constexpr int NL = 14 - KRW;
  constexpr int PRE = W7 ? 0 : PRE0;
  const int n16 = l & 15, g = l >> 4, tid = w * 64 + l;
  const int h = cw >> 1, c = cw & 1, o = cw;
  const int idx = n16 < 8 ? 0x4444 : 0xEEEE;
  const __amdgpu_buffer_rsrc_t rw = ws_rsrc(p, WS_WT, (size_t)NTOT * DM * 2);
  const unsigned WX = ((g >> 1) * 16 + n16) * 16 + (g & 1) * 8;
  const __amdgpu_buffer_rsrc_t rmem = __builtin_amdgcn_make_buffer_rsrc(p.ws + WS_MEMIMG, 0, (NSEG + 1) * MEMIMG_B, 0x00020000);
  const __amdgpu_buffer_rsrc_t rx = __builtin_amdgcn_make_buffer_rsrc(p.ws + WS_XCH, 0, NSEG * XCH_B, 0x00020000);
  const __amdgpu_buffer_rsrc_t rktm = __builtin_amdgcn_make_buffer_rsrc(p.ws + WS_KTM, 0, NSEG * KVM_B, 0x00020000);
  const __amdgpu_buffer_rsrc_t rvtm = __builtin_amdgcn_make_buffer_rsrc(p.ws + WS_VTM, 0, NSEG * KVM_B, 0x00020000);
  bf16x16 wa[W7 ? 1 : 14];
  bf16x16 wb[OUT ? KRW : 1];
  if (!W7) {
    const unsigned ra = WX + (unsigned)(h * 28 + 14 * c + 2 * w) * 14336;
#pragma unroll
    for (int u = 0; u < 14; ++u) wa[u] = load_wfrag(rw, ra + (u & 1) * 14336, u >> 1);
  }
  LAS char* wl = lds + L_WB_AL + w * (14 - KR) * 2048 + l * 16;
  if (OUT) {
    const unsigned ra = WX + (unsigned)(448 + (2 * w) * 28 + o) * 14336;
#pragma unroll
    for (int u = 0; u < 14; ++u) {
      bf16x16 f = load_wfrag(rw, ra + (u & 1) * 28 * 14336, u >> 1);
      if (u < KRW) wb[u] = f;
      else { u32x8 t = __builtin_bit_cast(u32x8, f); LAS char* d = wl + (u - KRW) * 2048;
        *(LAS u32x4*)d = (u32x4){t[0], t[1], t[2], t[3]}; *(LAS u32x4*)(d + 1024) = (u32x4){t[4], t[5], t[6], t[7]}; }
    }
  }
  if (W7) { if (l < T_STR / 16) *(LAS u32x4*)(lds + L_T + 7 * T_STR + l * 16) = (u32x4){0, 0, 0, 0}; }
  unsigned off_mem = tid * 16;
  const int hb = n16 >> 3, i8 = n16 & 7;
  unsigned off_mm = XM_OFF + w * 1024 + (hb * 16 + (g & 1) * 8 + i8) * 16;
  unsigned off_ms = XS_OFF + w * 128 + n16 * 8;
  unsigned off_ax = XA_OFF + (o * 8 + w) * 512 + hb * 256 + g * 64 + i8 * 8;
  const bool own_m = hb == (g >> 1);
  const bool st_m = n16 < 8 && g < 2;
  unsigned st_mm = st_m ? XM_OFF + (h >> 1) * 1024 + c * 512 + ((h & 1) * 16 + g * 8 + i8) * 16 : OOB;
  const unsigned inc_mm = st_m ? XCH_B : 0;
  const int gq = (l >> 1) & 3;
  unsigned st_mem = ((l & 3) == 0) ? MEMIMG_B + o * 256 + (gq >> 1) * 128 + (l >> 3) * 16 : OOB;
  const unsigned inc_mem = ((l & 3) == 0) ? MEMIMG_B : 0;
  unsigned st_vtm = ((((w >> 1) * 28 + o) * 64 + l) * 8 + (w & 1) * 4) * 2;
  unsigned st_ktm = ((((h >> 1) * 14 + 7 * c + (g >> 1)) * 4 + 2 * hb + (g & 1)) * 16 + (h & 1) * 8 + i8) * 16;
  LAS char* a_rd = lds + L_M + hb * 7 * 512 + g * 128 + i8 * 16;
  int budget = 1 << 20;
  unsigned tb1 = 0;
  wg_barrier();
  for (int s = 0; s < NSEG; ++s) {
    bf16x16 tw[NL > 0 ? NL : 1];
    if (OUT) {
#pragma unroll
      for (int j = 0; j < PRE; ++j) tw[j] = lds_wfrag(wl + j * 2048);
      SBAR();
    }
    if (!W7) {
      if (s) wait_until(tb1, T2);
      CBAR();
      u32x4 v = LD16(rmem, off_mem);
      while (__builtin_expect(isbad(mx4(v)), 0)) { if (--budget < 0) break; if (!OUT) __builtin_amdgcn_s_sleep(1); CBAR(); v = LD16(rmem, off_mem); }
      *(LAS u32x4*)(lds + L_M + tid * 16) = v;
      off_mem += MEMIMG_B;
    }
    if (W7) asm volatile("s_waitcnt vmcnt(0)" ::: "memory");
    wg_barrier();
    tb1 = now32();
    if (W7 && l == 0 && s > 0) flag_add(flag_ptr(p, F_CM, s - 1));
    bf16x8 af[7];
#pragma unroll
    for (int k = 0; k < 7; ++k) af[k] = *(LAS bf16x8*)(a_rd + k * 512);
    f32x4 vt1 = {0, 0, 0, 0}, vt2 = {0, 0, 0, 0};
    if (!W7) {
      f32x4 acc1 = {0, 0, 0, 0}, acc2 = {0, 0, 0, 0};
#pragma unroll
      for (int k = 0; k < 7; ++k) { acc1 = SMFMAC(af[k], wa[2 * k], acc1, idx); acc2 = SMFMAC(af[k], wa[2 * k + 1], acc2, idx); }
      f32x4 acc = comb32(acc1, acc2);
      u32x2 pk = {cvt_pk(acc[0], acc[1]), cvt_pk(acc[2], acc[3])};
      *(LAS u32x2*)(lds + L_T + w * T_STR + n16 * 40 + g * 8) = pk;
      wg_barrier();
    } else {
      bf16x8 mb[4];
#pragma unroll
      for (int q = 0; q < 4; ++q) { int ks = min(7 * c + 2 * q + (g >> 1), 13); mb[q] = *(LAS bf16x8*)(lds + L_M + ks * 512 + (2 * hb + (g & 1)) * 128 + i8 * 16); }
      if (OUT) {
#pragma unroll
        for (int k = 0; k < 7; ++k) { vt1 = SMFMAC(af[k], wb[2 * k], vt1, idx); vt2 = SMFMAC(af[k], wb[2 * k + 1], vt2, idx); }
      }
      asm volatile("" : "+v"(vt1), "+v"(vt2));
      SBAR();
      __builtin_amdgcn_s_setprio(3);
      wg_barrier();
      f32x4 d2 = {0, 0, 0, 0}, d3 = {0, 0, 0, 0};
      bf16x8 kt[4];
#pragma unroll
      for (int q = 0; q < 4; ++q) {
        LAS char* b = lds + L_T + (2 * q + (g >> 1)) * T_STR + (8 * (g & 1) + (n16 >> 2)) * 40 + (n16 & 3) * 8;
        s16x4 lo = __builtin_amdgcn_ds_read_tr16_b64_v4i16((LAS s16x4*)b);
        s16x4 hi = __builtin_amdgcn_ds_read_tr16_b64_v4i16((LAS s16x4*)(b + 160));
        u32x2 l2 = __builtin_bit_cast(u32x2, lo), h2 = __builtin_bit_cast(u32x2, hi);
        kt[q] = __builtin_bit_cast(bf16x8, (u32x4){l2.x, l2.y, h2.x, h2.y});
      }
      d2 = MFMA16(kt[0], mb[0], d2); d3 = MFMA16(kt[1], mb[1], d3); d2 = MFMA16(kt[2], mb[2], d2); d3 = MFMA16(kt[3], mb[3], d3);
      u32x4 s2;
#pragma unroll
      for (int r = 0; r < 4; ++r) { float f = d2[r] + d3[r]; auto t = __builtin_amdgcn_permlane32_swap(f2u(f), f2u(f), false, false);
        s2[r] = min(f2u(f + dpp_f<0x128>(u2f(t[1]))), 0xfffffffeu); }
      __builtin_amdgcn_raw_buffer_store_b128(s2, rx, st_mm, 0, 16);
      __builtin_amdgcn_s_setprio(0);
      CBAR();
#pragma unroll
      for (int q = 0; q < 4; ++q) __builtin_amdgcn_raw_buffer_store_b128(__builtin_bit_cast(u32x4, kt[q]), rktm, (q == 3 && g >= 2) ? OOB : st_ktm + q * 2048, 0, 16);
      st_mm += inc_mm; st_ktm += KVM_B;
    }
    if (OUT) {
      if (!W7) {
        __builtin_amdgcn_s_sleep(DB);
#pragma unroll
        for (int u = 0; u < 14; ++u) {
          if (u < KRW) { if (u & 1) vt2 = SMFMAC(af[u >> 1], wb[u], vt2, idx); else vt1 = SMFMAC(af[u >> 1], wb[u], vt1, idx);
            if (u >= KRW - XW && PRE + (u - (KRW - XW)) < NL) tw[PRE + (u - (KRW - XW))] = lds_wfrag(wl + (PRE + (u - (KRW - XW))) * 2048); }
          else { if (u & 1) vt2 = SMFMAC(af[u >> 1], tw[u - KRW], vt2, idx); else vt1 = SMFMAC(af[u >> 1], tw[u - KRW], vt1, idx);
            if (u - KRW + PRE + XW < NL) tw[u - KRW + PRE + XW] = lds_wfrag(wl + (u - KRW + PRE + XW) * 2048); }
          SBAR();
        }
      }
      f32x4 vt = comb32(vt1, vt2);
      u32x2 vpk = {cvt_pk(vt[0], vt[1]), cvt_pk(vt[2], vt[3])};
      if (__builtin_expect(s + 1 < NSEG, 1)) {
        bf16x8 va = __builtin_bit_cast(bf16x8, (u32x4){vpk.x, vpk.y, 0u, 0u});
        wait_until(tb1, TM);
        CBAR();
        u32x4 m0 = LD16(rx, off_mm), m1 = LD16(rx, off_mm + 512);
        u32x2 axu = LD8(rx, off_ax), ms = LD8(rx, off_ms);
        while (__builtin_expect(isbad(max(mx4(m0), mx4(m1))), 0)) { if (--budget < 0) break; CBAR(); m0 = LD16(rx, off_mm); m1 = LD16(rx, off_mm + 512); }
        while (__builtin_expect(isbad(max(umax3(axu.x, axu.y, ms.x), ms.y)), 0)) { if (--budget < 0) break; __builtin_amdgcn_s_sleep(1); CBAR(); axu = LD8(rx, off_ax); ms = LD8(rx, off_ms); }
        off_mm += XCH_B; off_ms += XCH_B; off_ax += XCH_B;
        const float mx = u2f(ms.x), sumx = u2f(ms.y);
        f32x4 mv = __builtin_bit_cast(f32x4, m0) + __builtin_bit_cast(f32x4, m1);
        float mm = fmaxf(fmax3(mv[0], mv[1], mv[2]), fmaxf(mv[3], mx));
        mm = x16_max(mm);
        float em[4];
#pragma unroll
        for (int r = 0; r < 4; ++r) em[r] = __builtin_amdgcn_exp2f(mv[r] - mm);
        float fx = __builtin_amdgcn_exp2f(mx - mm);
        unsigned pm0 = cvt_pk(em[0], em[1]), pm1 = cvt_pk(em[2], em[3]);
        bf16x8 pm = __builtin_bit_cast(bf16x8, (u32x4){own_m ? pm0 : 0u, own_m ? pm1 : 0u, 0u, 0u});
        f32x4 axs = (f32x4){blo(axu.x), bhi(axu.x), blo(axu.y), bhi(axu.y)} * fx;
        f32x4 am = MFMA16(va, pm, axs);
        float summ = x16_sum((em[0] + em[1]) + (em[2] + em[3]));
        float rinv = __builtin_amdgcn_rcpf(fx * sumx + summ);
        f32x4 r4;
#pragma unroll
        for (int r = 0; r < 4; ++r) { float t = am[r] * rinv; r4[r] = t + dpp_f<0x128>(t); }
        *(LAS f32x4*)(lds + L_P + w * P_STR + n16 * 64 + g * 16) = r4;
        __builtin_amdgcn_raw_buffer_store_b64(vpk, rvtm, st_vtm, 0, 16);
        wg_barrier();
        if (w == 0) {
          LAS char* b = lds + L_P + (l & 1) * 4 * P_STR + (l >> 3) * 64 + gq * 16;
          f32x4 t0 = *(LAS f32x4*)b, t1 = *(LAS f32x4*)(b + P_STR), t2 = *(LAS f32x4*)(b + 2 * P_STR), t3 = *(LAS f32x4*)(b + 3 * P_STR);
          f32x4 t = (t0 + t1) + (t2 + t3);
#pragma unroll
          for (int r = 0; r < 4; ++r) t[r] += dpp_f<0xB1>(t[r]);
          unsigned p0 = min(cvt_pk(t[0], t[1]), 0xfffffffeu), p1 = min(cvt_pk(t[2], t[3]), 0xfffffffeu);
          unsigned q0 = dpp_u<0x4E>(p0), q1 = dpp_u<0x4E>(p1);
          __builtin_amdgcn_raw_buffer_store_b128((u32x4){p0, p1, q0, q1}, rmem, st_mem, 0, 16);
          st_mem += inc_mem;
        }
      } else {
        __builtin_amdgcn_raw_buffer_store_b64(vpk, rvtm, st_vtm, 0, 16);
      }
      st_vtm += KVM_B;
    } else {
      wg_barrier();
    }
  }
  drain_barrier();
  if (W7 && l == 0) flag_add(flag_ptr(p, F_CM, NSEG - 1));
}

constexpr int H_P = 0, HP_STR = 1056, H_E = 7 * HP_STR + 32;
DI void helper_wave(const Params& p, const int hid, LAS char* lds, const int w, const int l) {
  const int n16 = l & 15, g = l >> 4, hb = n16 >> 3, i8 = n16 & 7;
  const int h = hid >> 1, hf = hid & 1;
  const __amdgpu_buffer_rsrc_t rmem = __builtin_amdgcn_make_buffer_rsrc(p.ws + WS_MEMIMG, 0, (NSEG + 1) * MEMIMG_B, 0x00020000);
  const __amdgpu_buffer_rsrc_t rx = __builtin_amdgcn_make_buffer_rsrc(p.ws + WS_XCH, 0, NSEG * XCH_B, 0x00020000);
  const __amdgpu_buffer_rsrc_t rk = ws_rsrc(p, WS_KT, (size_t)SEQ * NKT * 2), ru = ws_rsrc(p, WS_UT, (size_t)SEQ * NKT * 2);
  const int wk = w < 7 ? w : 0;
  unsigned off_mem = (wk + 7 * hb) * 512 + g * 128 + i8 * 16;
  const int o0 = hf * 14 + 2 * wk;
  const unsigned kp = (unsigned)((h * 28 + wk) * 64 + l) * 16, up = (unsigned)((h * 28 + o0) * 64 + l) * 16;
  unsigned st_ax = n16 < 8 ? XA_OFF + (o0 * 8 + (h >> 1)) * 512 + (h & 1) * 256 + g * 64 + n16 * 8 : OOB;
  const unsigned inc_ax = n16 < 8 ? XCH_B : 0;
  unsigned st_ms = (l < 8 && hf == 0) ? XS_OFF + (h >> 1) * 128 + ((h & 1) * 8 + l) * 8 : OOB;
  const unsigned inc_ms = (l < 8 && hf == 0) ? XCH_B : 0;
  bf16x8 k00, k01, k10, k11, u[2];
  auto prefetch = [&](int s) {
    flag_wait(flag_ptr(p, F_XG, s), 56);
    const unsigned k = kp + (unsigned)s * (SEG * NKT * 2), q = up + (unsigned)s * (SEG * NKT * 2);
    k00 = LDF(rk, k); k01 = LDF(rk, k + 7 * 1024); k10 = LDF(rk, k + 14 * 1024); k11 = LDF(rk, k + 21 * 1024);
    u[0] = LDF(ru, q); u[1] = LDF(ru, q + 1024);
  };
  if (w < 7) prefetch(0);
  int budget = 1 << 20;
  unsigned tb1 = 0;
  for (int s = 0; s + 1 < NSEG; ++s) {
    if (w < 7) {
      if (s) wait_until(tb1, TH);
      CBAR();
      u32x4 v = LD16(rmem, off_mem);
      while (__builtin_expect(isbad(mx4(v)), 0)) { if (--budget < 0) break; __builtin_amdgcn_s_sleep(1); CBAR(); v = LD16(rmem, off_mem); }
      off_mem += MEMIMG_B;
      bf16x8 f = __builtin_bit_cast(bf16x8, v);
      asm volatile("" : "+v"(u[0]), "+v"(u[1]));
      f32x4 z = {0, 0, 0, 0};
      f32x4 a0 = MFMA16(k00, f, z), a1 = MFMA16(k01, f, z), b0 = MFMA16(k10, f, z), b1 = MFMA16(k11, f, z);
      f32x4 r;
#pragma unroll
      for (int q = 0; q < 4; ++q) { float own = hb ? b1[q] : a0[q]; float oth = hb ? a1[q] : b0[q]; r[q] = own + dpp_f<0x128>(oth); }
      *(LAS f32x4*)(lds + H_P + w * HP_STR + l * 16) = r;
    }
    wg_barrier();
    tb1 = now32();
    if (w == 0 || w == 7) {
      f32x4 t[7];
#pragma unroll
      for (int k = 0; k < 7; ++k) t[k] = *(LAS f32x4*)(lds + H_P + k * HP_STR + l * 16);
      f32x4 sc = ((t[0] + t[1]) + (t[2] + t[3])) + ((t[4] + t[5]) + t[6]);
      float mx = fmaxf(fmax3(sc[0], sc[1], sc[2]), sc[3]);
      mx = fmaxf(mx, dpp_f<0x128>(mx)); mx = x32_max(mx); mx = x16_max(mx);
      float e0 = __builtin_amdgcn_exp2f(sc[0] - mx), e1 = __builtin_amdgcn_exp2f(sc[1] - mx), e2 = __builtin_amdgcn_exp2f(sc[2] - mx), e3 = __builtin_amdgcn_exp2f(sc[3] - mx);
      if (w == 0) {
        *(LAS u32x2*)(lds + H_E + (g * 16 + i8) * 16 + hb * 8) = (u32x2){cvt_pk(e0, e1), cvt_pk(e2, e3)};
        wg_barrier();
      } else {
        wg_barrier();
        float sm = (e0 + e1) + (e2 + e3);
        sm += dpp_f<0x128>(sm); sm = x32_sum(sm); sm = x16_sum(sm);
        ST8(((u32x2){min(f2u(mx), 0xfffffffeu), min(f2u(sm), 0xfffffffeu)}), rx, st_ms);
        st_ms += inc_ms;
      }
    } else {
      wg_barrier();
    }
    if (w < 7) {
      bf16x8 pe = *(LAS bf16x8*)(lds + H_E + (g * 16 + i8) * 16);
      f32x4 z = {0, 0, 0, 0};
      f32x4 d0 = MFMA16(u[0], pe, z), d1 = MFMA16(u[1], pe, z);
      u32x2 s0 = {min(cvt_pk(d0[0], d0[1]), 0xfffffffeu), min(cvt_pk(d0[2], d0[3]), 0xfffffffeu)};
      u32x2 s1 = {min(cvt_pk(d1[0], d1[1]), 0xfffffffeu), min(cvt_pk(d1[2], d1[3]), 0xfffffffeu)};
      ST8(s0, rx, st_ax);
      ST8(s1, rx, n16 < 8 ? st_ax + 4096 : OOB);
    }
    st_ax += inc_ax;
    CBAR();
    if (w < 7) prefetch(s + 1);
  }
}

DI void comb_row(const Params& p, int row, int l) {
  const bool act = l < 56;
  const __amdgpu_buffer_rsrc_t rpo = ws_rsrc(p, WS_PO, (size_t)4 * SEQ * DM * 4), rh = ws_rsrc(p, WS_H, (size_t)SEQ * DM * 4), rhn = ws_rsrc(p, WS_HN, (size_t)SEQ * DM * 2);
  float v[8];
  const unsigned off = act ? (unsigned)(row * DM + l * 8) * 4 : OOB;
  u32x4 t = act ? *(const u32x4*)(p.x + (size_t)row * DM + l * 8) : (u32x4){0, 0, 0, 0};
  f32x4 a = {0, 0, 0, 0}, b = a;
#pragma unroll
  for (int k = 0; k < 4; ++k) {
    a += __builtin_bit_cast(f32x4, LD16(rpo, act ? off + k * (SEQ * DM * 4) : OOB));
    b += __builtin_bit_cast(f32x4, LD16(rpo, act ? off + k * (SEQ * DM * 4) + 16 : OOB));
  }
#pragma unroll
  for (int j = 0; j < 2; ++j) { v[2 * j] = blo(t[j]) + a[2 * j]; v[2 * j + 1] = bhi(t[j]) + a[2 * j + 1]; v[4 + 2 * j] = blo(t[2 + j]) + b[2 * j]; v[5 + 2 * j] = bhi(t[2 + j]) + b[2 * j + 1]; }
  ST16(((u32x4){f2u(v[0]), f2u(v[1]), f2u(v[2]), f2u(v[3])}), rh, off);
  ST16(((u32x4){f2u(v[4]), f2u(v[5]), f2u(v[6]), f2u(v[7])}), rh, act ? off + 16 : OOB);
  u32x4 r = ln_row(v, act);
  ST16(r, rhn, act ? tiled_off(row, l) : OOB);
}

DI void att_wg(const Params& p, LAS char* lds, int s, int hg, int w, int l) {
  const int n16 = l & 15, g = l >> 4;
  const int hl = w >> 1, qt = w & 1, h = hg * 4 + hl;
  const __amdgpu_buffer_rsrc_t rk = ws_rsrc(p, WS_KT, (size_t)SEQ * NKT * 2), ru = ws_rsrc(p, WS_UT, (size_t)SEQ * NKT * 2);
  const __amdgpu_buffer_rsrc_t rkm = ws_rsrc(p, WS_KTM, (size_t)NSEG * KVM_B), rvm = ws_rsrc(p, WS_VTM, (size_t)NSEG * KVM_B);
  const __amdgpu_buffer_rsrc_t rpo = ws_rsrc(p, WS_PO, (size_t)4 * SEQ * DM * 4);
  const __amdgpu_buffer_rsrc_t rxf = ws_rsrc(p, WS_XF, (size_t)SEQ * DM * 2);
  bf16x8 q[14];
#pragma unroll
  for (int ks = 0; ks < 14; ++ks) q[ks] = LDF(rxf, (unsigned)(((s * 2 + qt) * 14 + ks) * 64 + l) * 16);
  flag_wait(flag_ptr(p, F_XG, s), 56);
  const unsigned ko = (unsigned)(((s * NH + h) * 28) * 64 + l) * 16;
  f32x4 s0 = {0, 0, 0, 0}, s1 = s0, sm = s0;
#pragma unroll
  for (int ks = 0; ks < 14; ++ks) s0 = MFMA16(LDF(rk, ko + ks * 1024), q[ks], s0);
  if (qt) {
#pragma unroll
    for (int ks = 0; ks < 14; ++ks) s1 = MFMA16(LDF(rk, ko + (14 + ks) * 1024), q[ks], s1);
  }
  bf16x8 uv[4][4];
  const unsigned uo = (unsigned)(((s * NH + hg * 4) * 28 + w) * 64 + l) * 16;
#pragma unroll
  for (int j = 0; j < 4; ++j)
#pragma unroll
    for (int k = 0; k < 4; ++k) uv[j][k] = LDF(ru, uo + (k * 28 + ((j < 3 || w < 4) ? j * 8 : 0)) * 1024);
  flag_wait(flag_ptr(p, F_CM, s), 32);
  DSTAMP(s == 63 && hg == 0 && w == 0 && l == 0, 3)
  const unsigned kmo = (unsigned)s * KVM_B + (unsigned)(((h >> 1) * 14) * 64 + l) * 16;
  bf16x8 vm[4];
#pragma unroll
  for (int j = 0; j < 4; ++j) vm[j] = LDF(rvm, (unsigned)s * KVM_B + (unsigned)((hg * 28 + w + ((j < 3 || w < 4) ? j * 8 : 0)) * 64 + l) * 16);
#pragma unroll
  for (int ks = 0; ks < 14; ++ks) sm = MFMA16(LDF(rkm, kmo + ks * 1024), q[ks], sm);
  const float NEG = -3.0e38f;
  const bool ownm = (g >> 1) == (h & 1);
  float v[12];
#pragma unroll
  for (int r = 0; r < 4; ++r) {
    v[r] = (qt || (4 * g + r) <= n16) ? s0[r] : NEG;
    v[4 + r] = (qt && (4 * g + r) <= n16) ? s1[r] : NEG;
    v[8 + r] = ownm ? sm[r] : NEG;
  }
  float mx = NEG;
#pragma unroll
  for (int r = 0; r < 12; ++r) mx = fmaxf(mx, v[r]);
  mx = x16_max(mx); mx = x32_max(mx);
  float sum = 0.f;
#pragma unroll
  for (int r = 0; r < 12; ++r) { v[r] = v[r] > -1.0e38f ? __builtin_amdgcn_exp2f(v[r] - mx) : 0.f; sum += v[r]; }
  sum = x16_sum(sum); sum = x32_sum(sum);
  const float rinv = 1.0f / sum;
  u32x4 px = {cvt_pk(v[0] * rinv, v[1] * rinv), cvt_pk(v[2] * rinv, v[3] * rinv), cvt_pk(v[4] * rinv, v[5] * rinv), cvt_pk(v[6] * rinv, v[7] * rinv)};
  *(LAS u32x4*)(lds + (hl * 2 + qt) * 1024 + l * 16) = px;
  if (ownm) *(LAS u32x2*)(lds + 8192 + qt * 1024 + l * 16 + (hl >> 1) * 8) = (u32x2){cvt_pk(v[8] * rinv, v[9] * rinv), cvt_pk(v[10] * rinv, v[11] * rinv)};
  wg_barrier();
  bf16x8 P[2][5];
#pragma unroll
  for (int t = 0; t < 2; ++t) {
#pragma unroll
    for (int k = 0; k < 4; ++k) P[t][k] = *(LAS bf16x8*)(lds + (k * 2 + t) * 1024 + l * 16);
    P[t][4] = *(LAS bf16x8*)(lds + 8192 + t * 1024 + l * 16);
  }
  const unsigned po = (unsigned)((hg * SEQ + s * SEG + n16) * DM + 4 * g + w * 16) * 4;
#pragma unroll
  for (int j = 0; j < 4; ++j) {
    f32x4 d0 = {0, 0, 0, 0}, d1 = d0;
#pragma unroll
    for (int k = 0; k < 4; ++k) { d0 = MFMA16(uv[j][k], P[0][k], d0); d1 = MFMA16(uv[j][k], P[1][k], d1); }
    d0 = MFMA16(vm[j], P[0][4], d0); d1 = MFMA16(vm[j], P[1][4], d1);
    if (j < 3 || w < 4) { ST16(__builtin_bit_cast(u32x4, d0), rpo, po + j * 512); ST16(__builtin_bit_cast(u32x4, d1), rpo, po + j * 512 + 16 * DM * 4); }
  }
  drain_barrier();
  if (w == 0) {
    if (l == 0) flag_add(flag_ptr(p, F_AT, s));
    flag_wait(flag_ptr(p, F_AT, s), 4);
  }
  wg_barrier();
  DSTAMP(s == 63 && hg == 0 && w == 0 && l == 0, 4)
  comb_row(p, s * SEG + hg * 8 + w, l);
  drain_barrier();
  if (w == 0 && l == 0) flag_add(flag_ptr(p, F_HN, s));
}

constexpr int L_GW = 57344, G1_STR = 72, G2_STR = 40;
DI void glu1_stage(const Params& p, LAS char* lds, int fb, int tid) {
#pragma unroll
  for (int i = 0; i < 7; ++i) {
    const int c = i * 512 + tid, row = c >> 2, gu = row >= 448, d = row - 448 * gu;
    u32x4 v = *(const u32x4*)((gu ? p.wu : p.wg) + (size_t)d * FF + fb * 32 + (c & 3) * 8);
    LAS char* dst = lds + L_GW + row * G1_STR + (c & 3) * 16;
    *(LAS u32x2*)dst = (u32x2){v.x, v.y}; *(LAS u32x2*)(dst + 8) = (u32x2){v.z, v.w};
  }
}
DI void glu2_stage(const Params& p, LAS char* lds, int o, int tid) {
#pragma unroll
  for (int i = 0; i < 7; ++i) {
    const int c = i * 512 + tid, row = c >> 1;
    u32x4 v = *(const u32x4*)(p.wd + (size_t)row * DM + o * 16 + (c & 1) * 8);
    LAS char* dst = lds + L_GW + row * G2_STR + (c & 1) * 16;
    *(LAS u32x2*)dst = (u32x2){v.x, v.y}; *(LAS u32x2*)(dst + 8) = (u32x2){v.z, v.w};
  }
}
DI bf16x8 tr_frag(LAS char* b, int str) {
  s16x4 lo = __builtin_amdgcn_ds_read_tr16_b64_v4i16((LAS s16x4*)b);
  s16x4 hi = __builtin_amdgcn_ds_read_tr16_b64_v4i16((LAS s16x4*)(b + 4 * str));
  u32x2 l2 = __builtin_bit_cast(u32x2, lo), h2 = __builtin_bit_cast(u32x2, hi);
  return __builtin_bit_cast(bf16x8, (u32x4){l2.x, l2.y, h2.x, h2.y});
}
DI void glu1_wg(const Params& p, LAS char* lds, int fb, int w, int l) {
  const int ftl = w & 1, rt = (w >> 1) & 1, sp = w >> 2;
  const __amdgpu_buffer_rsrc_t rhn = ws_rsrc(p, WS_HN, (size_t)SEQ * DM * 2), rhg = ws_rsrc(p, WS_HG, (size_t)SEQ * FF * 2);
  bf16x8 wgt[14], wup[14];
#pragma unroll
  for (int ks = 0; ks < 14; ++ks) {
    LAS char* b = lds + L_GW + (32 * ks + 8 * (l >> 4) + ((l & 15) >> 2)) * G1_STR + ftl * 32 + (l & 3) * 8;
    wgt[ks] = tr_frag(b, G1_STR); wup[ks] = tr_frag(b + 448 * G1_STR, G1_STR);
  }
  const int n16 = l & 15, g = l >> 4;
  if (w == 0) *(LAS unsigned*)(lds + l * 4) = 0u;
  wg_barrier();
  for (int s = sp; s < NSEG; s += 2) {
    flag_wait(flag_ptr(p, F_HN, s), 4);
    DSTAMP(s == 63 && fb == 0 && w == 4 && l == 0, 5)
    const unsigned ho = (unsigned)(((s * 2 + rt) * 14) * 64 + l) * 16;
    bf16x8 x[14];
#pragma unroll
    for (int ks = 0; ks < 14; ++ks) x[ks] = LDF(rhn, ho + ks * 1024);
    f32x4 ag = {0, 0, 0, 0}, au = ag;
#pragma unroll
    for (int ks = 0; ks < 14; ++ks) { ag = MFMA16(wgt[ks], x[ks], ag); au = MFMA16(wup[ks], x[ks], au); }
    u32x2 o = {cvt_pk(ag[0] * au[0], ag[1] * au[1]), cvt_pk(ag[2] * au[2], ag[3] * au[3])};
    ST8(o, rhg, (unsigned)(((((s * 2 + rt) * 56 + fb) * 64 + (2 * ftl + (g >> 1)) * 16 + n16) * 8 + 4 * (g & 1)) * 2));
    asm volatile("s_waitcnt vmcnt(0)" ::: "memory");
    if (l == 0) {
      const unsigned t = __hip_atomic_fetch_add((LAS unsigned*)(lds + s * 4), 1u, __ATOMIC_RELAXED, __HIP_MEMORY_SCOPE_WORKGROUP);
      if (t == 3) flag_add(flag_ptr(p, F_G1, s));
    }
  }
}

DI void glu2_wg(const Params& p, LAS char* lds, int o, int s0, int w, int l) {
  const __amdgpu_buffer_rsrc_t rhg = ws_rsrc(p, WS_HG, (size_t)SEQ * FF * 2), rh = ws_rsrc(p, WS_H, (size_t)SEQ * DM * 4);
  bf16x8 wd[7];
#pragma unroll
  for (int k = 0; k < 7; ++k) wd[k] = tr_frag(lds + L_GW + (32 * (7 * w + k) + 8 * (l >> 4) + ((l & 15) >> 2)) * G2_STR + (l & 3) * 8, G2_STR);
  const int tid = w * 64 + l;
  int buf = 0;
  for (int s = s0; s < NSEG; s += 2) {
    if (w == 0) flag_wait(flag_ptr(p, F_G1, s), 56);
    wg_barrier();
    DSTAMP(s == 63 && o == 0 && tid == 0, 6)
    const unsigned ho = (unsigned)(((s * 2) * 56 + 7 * w) * 64 + l) * 16;
    bf16x8 a[7], b[7];
#pragma unroll
    for (int k = 0; k < 7; ++k) { a[k] = LDF(rhg, ho + k * 1024); b[k] = LDF(rhg, ho + (56 + k) * 1024); }
    const int row = s * SEG + ((tid >> 6) & 1) * 16 + (tid & 15), col = o * 16 + 4 * ((tid >> 4) & 3);
    u32x4 hv = {0, 0, 0, 0};
    if (tid < 128) hv = LD16(rh, (unsigned)(row * DM + col) * 4);
    f32x4 d0 = {0, 0, 0, 0}, d1 = d0;
#pragma unroll
    for (int k = 0; k < 7; ++k) { d0 = MFMA16(wd[k], a[k], d0); d1 = MFMA16(wd[k], b[k], d1); }
    LAS char* L = lds + buf * 16384;
    *(LAS f32x4*)(L + w * 2048 + l * 16) = d0;
    *(LAS f32x4*)(L + w * 2048 + 1024 + l * 16) = d1;
    wg_barrier();
    if (tid < 128) {
      f32x4 acc = __builtin_bit_cast(f32x4, hv);
#pragma unroll
      for (int k = 0; k < 8; ++k) acc += *(LAS f32x4*)(L + k * 2048 + tid * 16);
      *(u32x2*)(p.out + (size_t)row * DM + col) = (u32x2){cvt_pk(acc[0], acc[1]), cvt_pk(acc[2], acc[3])};
    }
    buf ^= 1;
  }
}

__global__ void __launch_bounds__(512, 2) k_main(Params p) {
  extern __shared__ __attribute__((aligned(16))) char smem[];
  LAS char* lds = (LAS char*)smem;
  const int xg = blockIdx.x & 7, r = blockIdx.x >> 3;
  const int w = __builtin_amdgcn_readfirstlane(threadIdx.x >> 6), l = threadIdx.x & 63;
  pre_phase(p, lds, blockIdx.x, w, l);
  DSTAMP(blockIdx.x == 40 && threadIdx.x == 0, 7)
  if (xg == 0) {
    const int cw = r;
    if (cw < 28) { if (w == 7) chain_wave<true, true>(p, cw, lds, w, l); else chain_wave<false, true>(p, cw, lds, w, l); }
    else { if (w == 7) chain_wave<true, false>(p, cw, lds, w, l); else chain_wave<false, false>(p, cw, lds, w, l); }
    return;
  }
  if (xg == 1) { helper_wave(p, r, lds, w, l); return; }
  const int wi = (xg - 2) * 32 + r;
  if (wi < 168) { const unsigned t0 = now32(); wait_until(t0, (wi / 56) * XSTAG); xgemm_wg(p, lds, wi % 56, wi / 56, 3, w, l); }
  if (wi < 56) glu1_stage(p, lds, wi, threadIdx.x); else if (wi < 112) glu2_stage(p, lds, (wi - 56) % 28, threadIdx.x);
  wg_barrier();
  if (wi < 56) { glu1_wg(p, lds, wi, w, l); return; }
  if (wi < 112) {
    glu2_wg(p, lds, (wi - 56) % 28, (wi - 56) / 28, w, l);
    return;
  }
  for (;;) {
    if (threadIdx.x == 0) *(LAS unsigned*)(lds + 10244) = __hip_atomic_fetch_add(flag_ptr(p, F_Q, 0), 1u, __ATOMIC_RELAXED, __HIP_MEMORY_SCOPE_AGENT);
    wg_barrier();
    const unsigned t = __builtin_amdgcn_readfirstlane(*(LAS unsigned*)(lds + 10244));
    if (t >= NSEG * 4) break;
    att_wg(p, lds, t >> 2, t & 3, w, l);
  }
}

extern "C" void kernel_launch(void* const* d_in, const int* in_sizes, int n_in,
                              void* d_out, int out_size, void* d_ws, size_t ws_size,
                              hipStream_t stream) {
  Params p;
  p.x = (const uint16_t*)d_in[0]; p.mem0 = (const uint16_t*)d_in[1];
  p.Wq = (const uint16_t*)d_in[2]; p.Wk = (const uint16_t*)d_in[3]; p.Wv = (const uint16_t*)d_in[4]; p.Wo = (const uint16_t*)d_in[5];
  p.wg = (const uint16_t*)d_in[8]; p.wu = (const uint16_t*)d_in[9]; p.wd = (const uint16_t*)d_in[10];
  p.out = (uint16_t*)d_out; p.ws = (char*)d_ws;
  (void)hipFuncSetAttribute((const void*)k_main, hipFuncAttributeMaxDynamicSharedMemorySize, CHAIN_LDS);
  (void)hipMemsetAsync(p.ws + WS_FLG, 0, WS_END - WS_FLG, stream);
  k_main<<<256, 512, CHAIN_LDS, stream>>>(p);
}
```

```cpp
#include <hip/hip_runtime.h>
#include <hip/hip_bf16.h>
#include <stdint.h>

typedef __bf16 bf16x8 __attribute__((ext_vector_type(8)));
typedef __bf16 bf16x16 __attribute__((ext_vector_type(16)));
typedef __bf16 bf16x2 __attribute__((ext_vector_type(2)));
typedef float f32x4 __attribute__((ext_vector_type(4)));
typedef unsigned u32x4 __attribute__((ext_vector_type(4)));
typedef unsigned u32x2 __attribute__((ext_vector_type(2)));
typedef unsigned u32x8 __attribute__((ext_vector_type(8)));
typedef short s16x4 __attribute__((ext_vector_type(4)));
#define LAS __attribute__((address_space(3)))
#define DI __device__ __forceinline__

constexpr int DM = 448, NH = 16, HD = 256, NSEG = 64, SEG = 32, SEQ = 2048, NTOT = 14336, NKT = 7168, FF = 1792;
constexpr int MEMIMG_B = 7168;
constexpr int XM_OFF = 0, XS_OFF = 8192, XA_OFF = 9216, XCH_B = 9216 + 28 * 4096;
constexpr int KVM_B = NH * 8 * DM * 2;

constexpr int KR = 5, PRE0 = 4, XW = 1;
#define TUNE TM = 2000, T2 = 4400, TH = 4300, DB = 4
constexpr int TUNE;

constexpr int L_M = 0, L_T = 7168, T_STR = 640, L_P = L_T + 8 * T_STR, P_STR = 1056, L_WB = L_P + 8 * P_STR + 64;
constexpr int L_WB_AL = (L_WB + 255) & ~255;
constexpr int CHAIN_LDS = L_WB_AL + 7 * (14 - KR) * 2048;

constexpr size_t WS_MEMIMG = 0;
constexpr size_t WS_XCH = WS_MEMIMG + (size_t)(NSEG + 1) * MEMIMG_B;
constexpr size_t WS_SENT_END = WS_XCH + (size_t)NSEG * XCH_B;
constexpr size_t WS_XF = (WS_SENT_END + 4095) & ~(size_t)4095;
constexpr size_t WS_WT = WS_XF + (size_t)SEQ * DM * 2;
constexpr size_t WS_KT = WS_WT + (size_t)NTOT * DM * 2;
constexpr size_t WS_UT = WS_KT + (size_t)SEQ * NKT * 2;
constexpr size_t WS_KTM = WS_UT + (size_t)SEQ * NKT * 2;
constexpr size_t WS_VTM = WS_KTM + (size_t)NSEG * KVM_B;
constexpr size_t WS_PO = WS_VTM + (size_t)NSEG * KVM_B;
constexpr size_t WS_H = WS_PO + (size_t)4 * SEQ * DM * 4;
constexpr size_t WS_HN = WS_H + (size_t)SEQ * DM * 4;
constexpr size_t WS_WG = WS_HN + (size_t)SEQ * DM * 2;
constexpr size_t WS_WD = WS_WG + (size_t)2 * FF * DM * 2;
constexpr size_t WS_HG = WS_WD + (size_t)FF * DM * 2;
constexpr size_t WS_FLG = WS_HG + (size_t)SEQ * FF * 2;
constexpr size_t WS_END = WS_FLG + 6 * NSEG * 128;
constexpr int F_XG = 0, F_CM = 1, F_AT = 2, F_HN = 3, F_G1 = 4, F_Q = 5;
#define DSTAMP(c, k)

struct Params {
  const uint16_t* x; const uint16_t* mem0; const uint16_t* Wq; const uint16_t* Wk; const uint16_t* Wv; const uint16_t* Wo;
  const uint16_t* wg; const uint16_t* wu; const uint16_t* wd; uint16_t* out; char* ws;
};

DI unsigned cvt_pk(float a, float b) { bf16x2 r = {(__bf16)a, (__bf16)b}; return __builtin_bit_cast(unsigned, r); }
DI void wg_barrier() { asm volatile("s_waitcnt lgkmcnt(0)\n\ts_barrier" ::: "memory"); }
DI unsigned umax3(unsigned a, unsigned b, unsigned c) { return max(max(a, b), c); }
DI float fmax3(float a, float b, float c) { return fmaxf(fmaxf(a, b), c); }
DI float u2f(unsigned u) { return __builtin_bit_cast(float, u); }
DI unsigned f2u(float f) { return __builtin_bit_cast(unsigned, f); }
DI float blo(unsigned u) { return u2f(u << 16); }
DI float bhi(unsigned u) { return u2f(u & 0xffff0000u); }
template<int CTRL> DI float dpp_f(float x) { return u2f((unsigned)__builtin_amdgcn_update_dpp(0, (int)f2u(x), CTRL, 0xf, 0xf, true)); }
template<int CTRL> DI unsigned dpp_u(unsigned x) { return (unsigned)__builtin_amdgcn_update_dpp(0, (int)x, CTRL, 0xf, 0xf, true); }
DI float x32_max(float x) { auto r = __builtin_amdgcn_permlane32_swap(f2u(x), f2u(x), false, false); return fmaxf(u2f(r[0]), u2f(r[1])); }
DI float x32_sum(float x) { auto r = __builtin_amdgcn_permlane32_swap(f2u(x), f2u(x), false, false); return u2f(r[0]) + u2f(r[1]); }
DI float x16_max(float x) { auto r = __builtin_amdgcn_permlane16_swap(f2u(x), f2u(x), false, false); return fmaxf(u2f(r[0]), u2f(r[1])); }
DI float x16_sum(float x) { auto r = __builtin_amdgcn_permlane16_swap(f2u(x), f2u(x), false, false); return u2f(r[0]) + u2f(r[1]); }
DI float wave_sum(float v) {
  v += dpp_f<0xB1>(v); v += dpp_f<0x4E>(v); v += dpp_f<0x124>(v); v += dpp_f<0x128>(v);
  v = x16_sum(v); v = x32_sum(v); return v;
}
#define SMFMAC(a, b, c, idx) __builtin_amdgcn_smfmac_f32_16x16x64_bf16((a), (b), (c), (idx), 0, 0)
#define MFMA16(a, b, c) __builtin_amdgcn_mfma_f32_16x16x32_bf16((a), (b), (c), 0, 0, 0)
constexpr unsigned OOB = 0x7fffffffu;
#define LD16(r, off) __builtin_amdgcn_raw_buffer_load_b128((r), (off), 0, 16)
#define LD8(r, off) __builtin_amdgcn_raw_buffer_load_b64((r), (off), 0, 16)
#define CBAR() asm volatile("" ::: "memory")
#define SBAR() __builtin_amdgcn_sched_barrier(0)

DI unsigned* flag_ptr(const Params& p, int kind, int s) { return (unsigned*)(p.ws + WS_FLG + (size_t)(kind * NSEG + s) * 128); }
DI void flag_add(unsigned* f) { __hip_atomic_fetch_add(f, 1u, __ATOMIC_RELAXED, __HIP_MEMORY_SCOPE_AGENT); }
DI void flag_wait(const unsigned* f, unsigned want) {
  int budget = 1 << 20;
  while ((unsigned)__builtin_amdgcn_readfirstlane(__hip_atomic_load(f, __ATOMIC_RELAXED, __HIP_MEMORY_SCOPE_AGENT)) < want) {
    if (--budget < 0) break;
    __builtin_amdgcn_s_sleep(8);
  }
  asm volatile("" ::: "memory");
}
DI void drain_barrier() { asm volatile("s_waitcnt vmcnt(0) lgkmcnt(0)\n\ts_barrier" ::: "memory"); }
DI __amdgpu_buffer_rsrc_t ws_rsrc(const Params& p, size_t off, size_t bytes) { return __builtin_amdgcn_make_buffer_rsrc(p.ws + off, 0, (int)bytes, 0x00020000); }
#define LDF(r, off) __builtin_bit_cast(bf16x8, LD16((r), (off)))
#define ST16(v, r, off) __builtin_amdgcn_raw_buffer_store_b128((v), (r), (off), 0, 16)
#define ST8(v, r, off) __builtin_amdgcn_raw_buffer_store_b64((v), (r), (off), 0, 16)

DI u32x4 ln_row(float (&v)[8], bool act) {
  float s = 0.f;
#pragma unroll
  for (int j = 0; j < 8; ++j) s += v[j];
  s = wave_sum(act ? s : 0.f);
  const float mean = s * (1.0f / DM);
  float q = 0.f;
#pragma unroll
  for (int j = 0; j < 8; ++j) { float d = v[j] - mean; q += d * d; }
  q = wave_sum(act ? q : 0.f);
  const float rstd = 1.0f / sqrtf(q * (1.0f / DM) + 1e-5f);
  u32x4 r;
#pragma unroll
  for (int j = 0; j < 4; ++j) r[j] = cvt_pk((v[2 * j] - mean) * rstd, (v[2 * j + 1] - mean) * rstd);
  return r;
}
DI unsigned tiled_off(int row, int c8) { return (unsigned)((((row >> 4) * 14 + (c8 >> 2)) * 64 + (c8 & 3) * 16 + (row & 15)) * 16); }

constexpr int WQ_STR = 144, WP_OFF = 224 * WQ_STR, WO_STR = 464, WBUF = 2 * 224 * WQ_STR;
constexpr int PRE_LDS = 2 * WBUF + 8192;
template<bool isB>
DI void wt_wg(const Params& p, LAS char* lds, int task, int w, int l) {
  const int n16 = l & 15, g = l >> 4, tid = w * 64 + l;
  const int mat = task >> 2, qe = (task >> 1) & 1, qn = task & 1;
  const int h = mat & 15;
  const uint16_t* Qg = (isB ? p.Wv : p.Wk) + (size_t)(qe * 224) * (NH * HD) + h * HD;
  const uint16_t* Pg = isB ? p.Wo + (size_t)h * HD * DM + qn * 224 : p.Wq + (size_t)(qn * 224) * (NH * HD) + h * HD;
  u32x4 r[2][7];
  const uint16_t* src[7]; int loff[7];
#pragma unroll
  for (int i = 0; i < 7; ++i) {
    const int c = i * 512 + tid, c2 = c - 1792;
    src[i] = c < 1792 ? Qg + (size_t)(c >> 3) * (NH * HD) + (c & 7) * 8
           : (isB ? Pg + (size_t)(c2 / 28) * DM + (c2 % 28) * 8 : Pg + (size_t)(c2 >> 3) * (NH * HD) + (c2 & 7) * 8);
    loff[i] = (c < 1792 || !isB) ? (c >> 3) * WQ_STR + (c & 7) * 16 : WP_OFF + (c2 / 28) * WO_STR + (c2 % 28) * 16;
  }
  const int strP = isB ? 64 * DM : 64, str3 = tid < 256 ? 64 : strP;
  auto gload = [&](int kc, u32x4 (&d)[7]) {
#pragma unroll
    for (int i = 0; i < 7; ++i) { d[i] = *(const u32x4*)src[i]; src[i] += (i < 3 ? 64 : (i == 3 ? str3 : strP)); asm volatile("" : "+v"(src[i])); }
  };
  auto lstore = [&](LAS char* L, u32x4 (&d)[7]) {
#pragma unroll
    for (int i = 0; i < 7; ++i) *(LAS u32x4*)(L + loff[i]) = d[i];
  };
  const int we = w >> 2, wn = w & 3;
  f32x4 acc[8][4];
#pragma unroll
  for (int a = 0; a < 8; ++a)
#pragma unroll
    for (int c = 0; c < 4; ++c) acc[a][c] = (f32x4){0, 0, 0, 0};
  const int qrow = 8 * (n16 >> 2) + (n16 & 3);
  gload(0, r[0]); gload(1, r[1]);
#pragma unroll
  for (int kc = 0; kc < 4; ++kc) {
    LAS char* L = lds + (kc & 1) * WBUF;
    SBAR();
    lstore(L, r[kc & 1]);
    if (kc + 2 < 4) gload(kc + 2, r[kc & 1]);
    wg_barrier();
#pragma unroll
    for (int k = 0; k < 2; ++k) {
      bf16x8 pb[4];
#pragma unroll
      for (int c = 0; c < 4; ++c) {
        const int nt = wn + 4 * c;
        if (isB) {
          LAS char* bb = L + WP_OFF + (k * 32 + 8 * g + (n16 >> 2)) * WO_STR + nt * 32 + (n16 & 3) * 8;
          s16x4 lo = __builtin_amdgcn_ds_read_tr16_b64_v4i16((LAS s16x4*)bb);
          s16x4 hi = __builtin_amdgcn_ds_read_tr16_b64_v4i16((LAS s16x4*)(bb + 4 * WO_STR));
          u32x2 l2 = __builtin_bit_cast(u32x2, lo), h2 = __builtin_bit_cast(u32x2, hi);
          pb[c] = __builtin_bit_cast(bf16x8, (u32x4){l2.x, l2.y, h2.x, h2.y});
        } else {
          pb[c] = *(LAS bf16x8*)(L + WP_OFF + (nt * 16 + n16) * WQ_STR + k * 64 + g * 16);
        }
      }
#pragma unroll
      for (int a = 0; a < 8; ++a) {
        const int ep = 4 * we + (a >> 1);
        bf16x8 qa = *(LAS bf16x8*)(L + (ep * 32 + (a & 1) * 4 + qrow) * WQ_STR + k * 64 + g * 16);
#pragma unroll
        for (int c = 0; c < 4; ++c) acc[a][c] = MFMA16(qa, pb[c], acc[a][c]);
        if (a & 1) SBAR();
      }
    }
  }
  const float sc = isB ? 1.0f : 0.0625f * 1.4426950408889634f;
  u32x4* WX = (u32x4*)(p.ws + WS_WT);
#pragma unroll
  for (int c = 0; c < 4; ++c)
#pragma unroll
    for (int m = 0; m < 4; ++m) {
      if ((c < 3 || wn < 2) && (m < 3 || we == 0)) {
        const int nt = ((isB ? NKT : 0) + h * DM + qn * 224) / 16 + wn + 4 * c, ks = qe * 7 + 4 * we + m;
        const f32x4 x = acc[2 * m][c] * sc, y = acc[2 * m + 1][c] * sc;
        WX[((size_t)nt * 14 + ks) * 64 + l] = (u32x4){cvt_pk(x[0], x[1]), cvt_pk(x[2], x[3]), cvt_pk(y[0], y[1]), cvt_pk(y[2], y[3])};
      }
    }
}

constexpr int PRE_WT_BLOCKS = 128, PRE_LN_BLOCKS = 32;
__global__ void __launch_bounds__(512, 2) k_pre(Params p) {
  extern __shared__ __attribute__((aligned(16))) char smem[];
  const int tid = threadIdx.x, l = tid & 63, w = __builtin_amdgcn_readfirstlane(tid >> 6);
  if (blockIdx.x < PRE_WT_BLOCKS) {
    const int task = (blockIdx.x & 7) * 16 + (blockIdx.x >> 3);
    if (task >= 64) wt_wg<true>(p, (LAS char*)smem, task, w, l); else wt_wg<false>(p, (LAS char*)smem, task, w, l);
    return;
  }
  const int b = blockIdx.x - PRE_WT_BLOCKS;
  {
    const bool act = l < 56;
    u32x4 t[8];
#pragma unroll
    for (int i = 0; i < 8; ++i) t[i] = act ? *(const u32x4*)(p.x + (size_t)(b * 64 + w * 8 + i) * DM + l * 8) : (u32x4){0, 0, 0, 0};
#pragma unroll
    for (int i = 0; i < 8; ++i) {
      float v[8];
#pragma unroll
      for (int j = 0; j < 4; ++j) { v[2 * j] = blo(t[i][j]); v[2 * j + 1] = bhi(t[i][j]); }
      u32x4 r = ln_row(v, act);
      if (act) *(u32x4*)(p.ws + WS_XF + tiled_off(b * 64 + w * 8 + i, l)) = r;
    }
  }
  {
    const int i = b * 512 + tid;
    if (i < XCH_B / 16) ((u32x4*)(p.ws + WS_XCH))[i] = (u32x4){~0u, ~0u, ~0u, ~0u};
    else if (i < XCH_B / 16 + MEMIMG_B / 16) ((u32x4*)(p.ws + WS_MEMIMG + MEMIMG_B))[i - XCH_B / 16] = (u32x4){~0u, ~0u, ~0u, ~0u};
  }
  if (b == 30) { for (int i = tid; i < 6 * NSEG * 8; i += 512) ((u32x4*)(p.ws + WS_FLG))[i] = (u32x4){0, 0, 0, 0}; }
  if (b == 31) {
    for (int c = tid; c < 448; c += 512) {
      const int row = c & 7, kg = c >> 3;
      u32x4 t = *(const u32x4*)(p.mem0 + row * DM + kg * 8);
      t.x = min(t.x, 0xfffffffeu); t.y = min(t.y, 0xfffffffeu); t.z = min(t.z, 0xfffffffeu); t.w = min(t.w, 0xfffffffeu);
      *(u32x4*)(p.ws + WS_MEMIMG + c * 16) = t;
    }
  }
}

DI void xgemm_wg(const Params& p, LAS char* lds, int nb, int seg0, int step, int w, int l) {
  const int n16 = l & 15, g = l >> 4;
  const int n0 = nb * 256 + w * 32;
  const bool isU = n0 >= NKT;
  const int r0 = isU ? n0 + n16 : n0 + 8 * (n16 >> 2) + (n16 & 3);
  const int r1 = isU ? r0 + 16 : r0 + 4;
  const bf16x8* W0 = (const bf16x8*)(p.ws + WS_WT) + (size_t)(r0 >> 4) * 14 * 64 + g * 16 + (r0 & 15);
  const bf16x8* W1 = (const bf16x8*)(p.ws + WS_WT) + (size_t)(r1 >> 4) * 14 * 64 + g * 16 + (r1 & 15);
  bf16x8 w0[14], w1[14];
#pragma unroll
  for (int ks = 0; ks < 14; ++ks) { w0[ks] = W0[ks * 64]; w1[ks] = W1[ks * 64]; }
  const int nn = isU ? n0 - NKT : n0;
  const int hh = nn / DM, cc = nn % DM;
  const int tid = w * 64 + l;
  const u32x4* XF = (const u32x4*)(p.ws + WS_XF);
  u32x4 pre[4];
  auto gload = [&](int s) {
#pragma unroll
    for (int i = 0; i < 4; ++i) { const int c = i * 512 + tid; if (c < 1792) pre[i] = XF[(size_t)s * 1792 + c]; }
  };
  gload(seg0);
  int buf = 0, prev = -1;
  const __amdgpu_buffer_rsrc_t rout = ws_rsrc(p, isU ? WS_UT : WS_KT, (size_t)SEQ * NKT * 2);
  const __amdgpu_buffer_rsrc_t rsent = ws_rsrc(p, 0, WS_SENT_END);
  for (int s = seg0; s < NSEG; s += step) {
    LAS char* L = lds + buf * 28672;
#pragma unroll
    for (int i = 0; i < 4; ++i) { const int c = i * 512 + tid; if (c < 1792) *(LAS u32x4*)(L + c * 16) = pre[i]; }
    asm volatile("s_waitcnt vmcnt(0)" ::: "memory");
    if (s + step < NSEG) gload(s + step);
    wg_barrier();
    if (tid == 0 && prev >= 0) flag_add(flag_ptr(p, F_XG, prev));
    f32x4 a00 = {0, 0, 0, 0}, a01 = a00, a10 = a00, a11 = a00;
#pragma unroll
    for (int ks = 0; ks < 14; ++ks) {
      bf16x8 x0 = *(LAS bf16x8*)(L + ks * 1024 + l * 16), x1 = *(LAS bf16x8*)(L + (14 + ks) * 1024 + l * 16);
      if (isU) { a00 = MFMA16(x0, w0[ks], a00); a01 = MFMA16(x1, w0[ks], a01); a10 = MFMA16(x0, w1[ks], a10); a11 = MFMA16(x1, w1[ks], a11); }
      else     { a00 = MFMA16(w0[ks], x0, a00); a01 = MFMA16(w1[ks], x0, a01); a10 = MFMA16(w0[ks], x1, a10); a11 = MFMA16(w1[ks], x1, a11); }
    }
    u32x4 o0 = {cvt_pk(a00[0], a00[1]), cvt_pk(a00[2], a00[3]), cvt_pk(a01[0], a01[1]), cvt_pk(a01[2], a01[3])};
    u32x4 o1 = {cvt_pk(a10[0], a10[1]), cvt_pk(a10[2], a10[3]), cvt_pk(a11[0], a11[1]), cvt_pk(a11[2], a11[3])};
    {
      const unsigned off = (unsigned)((((s * NH + hh) * 28 + (isU ? (cc >> 4) : (cc >> 5))) * 64 + l) * 16);
      ST16(o0, rout, off); ST16(o1, rout, off + (isU ? 1024 : 14 * 1024));
    }
    if (s + 1 < NSEG) {
      const int i = nb * 147 + tid;
      if (tid < 147 && i < (XCH_B + MEMIMG_B) / 16) {
        const unsigned off = i < XCH_B / 16 ? (unsigned)(WS_XCH + (size_t)(s + 1) * XCH_B) + i * 16 : (unsigned)((s + 2) * MEMIMG_B) + (i - XCH_B / 16) * 16;
        ST16(((u32x4){~0u, ~0u, ~0u, ~0u}), rsent, off);
      }
    }
    prev = s;
    buf ^= 1;
  }
  drain_barrier();
  if (tid == 0 && prev >= 0) flag_add(flag_ptr(p, F_XG, prev));
}
DI bf16x16 load_wfrag(const char* base, int k) {
  u32x2 alo = *(const u32x2*)(base + k * 1024), ahi = *(const u32x2*)(base + k * 1024 + 512);
  u32x2 blo_ = *(const u32x2*)(base + (k + 7) * 1024), bhi_ = *(const u32x2*)(base + (k + 7) * 1024 + 512);
  u32x8 r = {alo.x, blo_.x, alo.y, blo_.y, ahi.x, bhi_.x, ahi.y, bhi_.y};
  return __builtin_bit_cast(bf16x16, r);
}
DI f32x4 comb32(f32x4 a, f32x4 b) {
  f32x4 r;
#pragma unroll
  for (int i = 0; i < 4; ++i) { float fa = a[i], fb = b[i]; auto t = __builtin_amdgcn_permlane32_swap(f2u(fa), f2u(fb), false, false); r[i] = u2f(t[0]) + u2f(t[1]); }
  return r;
}
DI bf16x16 lds_wfrag(LAS char* d) { u32x4 t0 = *(LAS u32x4*)d, t1 = *(LAS u32x4*)(d + 1024);
  u32x8 t = {t0.x, t0.y, t0.z, t0.w, t1.x, t1.y, t1.z, t1.w}; return __builtin_bit_cast(bf16x16, t); }
DI unsigned now32() { return (unsigned)__builtin_amdgcn_s_memtime(); }
DI void wait_until(unsigned t0, unsigned d) { while (now32() - t0 < d) { } }
DI unsigned mx4(u32x4 v) { return max(umax3(v.x, v.y, v.z), v.w); }
DI bool isbad(unsigned m) { return __builtin_amdgcn_ballot_w64(m == 0xffffffffu) != 0; }

template<bool W7, bool OUT>
DI void chain_wave(const Params& p, const int cw, LAS char* lds, const int w, const int l) {
  constexpr int KRW = W7 ? 14 : KR;
  constexpr int NL = 14 - KRW;
  constexpr int PRE = W7 ? 0 : PRE0;
  const int n16 = l & 15, g = l >> 4, tid = w * 64 + l;
  const int h = cw >> 1, c = cw & 1, o = cw;
  const int idx = n16 < 8 ? 0x4444 : 0xEEEE;
  const char* WX = p.ws + WS_WT + ((g >> 1) * 16 + n16) * 16 + (g & 1) * 8;
  const __amdgpu_buffer_rsrc_t rmem = __builtin_amdgcn_make_buffer_rsrc(p.ws + WS_MEMIMG, 0, (NSEG + 1) * MEMIMG_B, 0x00020000);
  const __amdgpu_buffer_rsrc_t rx = __builtin_amdgcn_make_buffer_rsrc(p.ws + WS_XCH, 0, NSEG * XCH_B, 0x00020000);
  const __amdgpu_buffer_rsrc_t rktm = __builtin_amdgcn_make_buffer_rsrc(p.ws + WS_KTM, 0, NSEG * KVM_B, 0x00020000);
  const __amdgpu_buffer_rsrc_t rvtm = __builtin_amdgcn_make_buffer_rsrc(p.ws + WS_VTM, 0, NSEG * KVM_B, 0x00020000);
  bf16x16 wa[W7 ? 1 : 14];
  bf16x16 wb[OUT ? KRW : 1];
  if (!W7) {
    const char* ra = WX + (size_t)(h * 28 + 14 * c + 2 * w) * 14336;
#pragma unroll
    for (int u = 0; u < 14; ++u) wa[u] = load_wfrag(ra + (u & 1) * 14336, u >> 1);
  }
  LAS char* wl = lds + L_WB_AL + w * (14 - KR) * 2048 + l * 16;
  if (OUT) {
    const char* ra = WX + (size_t)(448 + (2 * w) * 28 + o) * 14336;
#pragma unroll
    for (int u = 0; u < 14; ++u) {
      bf16x16 f = load_wfrag(ra + (size_t)(u & 1) * 28 * 14336, u >> 1);
      if (u < KRW) wb[u] = f;
      else { u32x8 t = __builtin_bit_cast(u32x8, f); LAS char* d = wl + (u - KRW) * 2048;
        *(LAS u32x4*)d = (u32x4){t[0], t[1], t[2], t[3]}; *(LAS u32x4*)(d + 1024) = (u32x4){t[4], t[5], t[6], t[7]}; }
    }
  }
  if (W7) { if (l < T_STR / 16) *(LAS u32x4*)(lds + L_T + 7 * T_STR + l * 16) = (u32x4){0, 0, 0, 0}; }
  unsigned off_mem = tid * 16;
  const int hb = n16 >> 3, i8 = n16 & 7;
  unsigned off_mm = XM_OFF + w * 1024 + (hb * 16 + (g & 1) * 8 + i8) * 16;
  unsigned off_ms = XS_OFF + w * 128 + n16 * 8;
  unsigned off_ax = XA_OFF + (o * 8 + w) * 512 + hb * 256 + g * 64 + i8 * 8;
  const bool own_m = hb == (g >> 1);
  const bool st_m = n16 < 8 && g < 2;
  unsigned st_mm = st_m ? XM_OFF + (h >> 1) * 1024 + c * 512 + ((h & 1) * 16 + g * 8 + i8) * 16 : OOB;
  const unsigned inc_mm = st_m ? XCH_B : 0;
  const int gq = (l >> 1) & 3;
  unsigned st_mem = ((l & 3) == 0) ? MEMIMG_B + o * 256 + (gq >> 1) * 128 + (l >> 3) * 16 : OOB;
  const unsigned inc_mem = ((l & 3) == 0) ? MEMIMG_B : 0;
  unsigned st_vtm = ((((w >> 1) * 28 + o) * 64 + l) * 8 + (w & 1) * 4) * 2;
  unsigned st_ktm = ((((h >> 1) * 14 + 7 * c + (g >> 1)) * 4 + 2 * hb + (g & 1)) * 16 + (h & 1) * 8 + i8) * 16;
  LAS char* a_rd = lds + L_M + hb * 7 * 512 + g * 128 + i8 * 16;
  int budget = 1 << 20;
  unsigned tb1 = 0;
  wg_barrier();
  for (int s = 0; s < NSEG; ++s) {
    bf16x16 tw[NL > 0 ? NL : 1];
    if (OUT) {
#pragma unroll
      for (int j = 0; j < PRE; ++j) tw[j] = lds_wfrag(wl + j * 2048);
      SBAR();
    }
    if (!W7) {
      if (s) wait_until(tb1, T2);
      CBAR();
      u32x4 v = LD16(rmem, off_mem);
      while (__builtin_expect(isbad(mx4(v)), 0)) { if (--budget < 0) break; if (!OUT) __builtin_amdgcn_s_sleep(1); CBAR(); v = LD16(rmem, off_mem); }
      *(LAS u32x4*)(lds + L_M + tid * 16) = v;
      off_mem += MEMIMG_B;
    }
    if (W7) asm volatile("s_waitcnt vmcnt(0)" ::: "memory");
    wg_barrier();
    tb1 = now32();
    if (W7 && l == 0 && s > 0) flag_add(flag_ptr(p, F_CM, s - 1));
    bf16x8 af[7];
#pragma unroll
    for (int k = 0; k < 7; ++k) af[k] = *(LAS bf16x8*)(a_rd + k * 512);
    f32x4 vt1 = {0, 0, 0, 0}, vt2 = {0, 0, 0, 0};
    if (!W7) {
      f32x4 acc1 = {0, 0, 0, 0}, acc2 = {0, 0, 0, 0};
#pragma unroll
      for (int k = 0; k < 7; ++k) { acc1 = SMFMAC(af[k], wa[2 * k], acc1, idx); acc2 = SMFMAC(af[k], wa[2 * k + 1], acc2, idx); }
      f32x4 acc = comb32(acc1, acc2);
      u32x2 pk = {cvt_pk(acc[0], acc[1]), cvt_pk(acc[2], acc[3])};
      *(LAS u32x2*)(lds + L_T + w * T_STR + n16 * 40 + g * 8) = pk;
      wg_barrier();
    } else {
      bf16x8 mb[4];
#pragma unroll
      for (int q = 0; q < 4; ++q) { int ks = min(7 * c + 2 * q + (g >> 1), 13); mb[q] = *(LAS bf16x8*)(lds + L_M + ks * 512 + (2 * hb + (g & 1)) * 128 + i8 * 16); }
      if (OUT) {
#pragma unroll
        for (int k = 0; k < 7; ++k) { vt1 = SMFMAC(af[k], wb[2 * k], vt1, idx); vt2 = SMFMAC(af[k], wb[2 * k + 1], vt2, idx); }
      }
      asm volatile("" : "+v"(vt1), "+v"(vt2));
      SBAR();
      __builtin_amdgcn_s_setprio(3);
      wg_barrier();
      f32x4 d2 = {0, 0, 0, 0}, d3 = {0, 0, 0, 0};
      bf16x8 kt[4];
#pragma unroll
      for (int q = 0; q < 4; ++q) {
        LAS char* b = lds + L_T + (2 * q + (g >> 1)) * T_STR + (8 * (g & 1) + (n16 >> 2)) * 40 + (n16 & 3) * 8;
        s16x4 lo = __builtin_amdgcn_ds_read_tr16_b64_v4i16((LAS s16x4*)b);
        s16x4 hi = __builtin_amdgcn_ds_read_tr16_b64_v4i16((LAS s16x4*)(b + 160));
        u32x2 l2 = __builtin_bit_cast(u32x2, lo), h2 = __builtin_bit_cast(u32x2, hi);
        kt[q] = __builtin_bit_cast(bf16x8, (u32x4){l2.x, l2.y, h2.x, h2.y});
      }
      d2 = MFMA16(kt[0], mb[0], d2); d3 = MFMA16(kt[1], mb[1], d3); d2 = MFMA16(kt[2], mb[2], d2); d3 = MFMA16(kt[3], mb[3], d3);
      u32x4 s2;
#pragma unroll
      for (int r = 0; r < 4; ++r) { float f = d2[r] + d3[r]; auto t = __builtin_amdgcn_permlane32_swap(f2u(f), f2u(f), false, false);
        s2[r] = min(f2u(f + dpp_f<0x128>(u2f(t[1]))), 0xfffffffeu); }
      __builtin_amdgcn_raw_buffer_store_b128(s2, rx, st_mm, 0, 16);
      __builtin_amdgcn_s_setprio(0);
      CBAR();
#pragma unroll
      for (int q = 0; q < 4; ++q) __builtin_amdgcn_raw_buffer_store_b128(__builtin_bit_cast(u32x4, kt[q]), rktm, (q == 3 && g >= 2) ? OOB : st_ktm + q * 2048, 0, 16);
      st_mm += inc_mm; st_ktm += KVM_B;
    }
    if (OUT) {
      if (!W7) {
        __builtin_amdgcn_s_sleep(DB);
#pragma unroll
        for (int u = 0; u < 14; ++u) {
          if (u < KRW) { if (u & 1) vt2 = SMFMAC(af[u >> 1], wb[u], vt2, idx); else vt1 = SMFMAC(af[u >> 1], wb[u], vt1, idx);
            if (u >= KRW - XW && PRE + (u - (KRW - XW)) < NL) tw[PRE + (u - (KRW - XW))] = lds_wfrag(wl + (PRE + (u - (KRW - XW))) * 2048); }
          else { if (u & 1) vt2 = SMFMAC(af[u >> 1], tw[u - KRW], vt2, idx); else vt1 = SMFMAC(af[u >> 1], tw[u - KRW], vt1, idx);
            if (u - KRW + PRE + XW < NL) tw[u - KRW + PRE + XW] = lds_wfrag(wl + (u - KRW + PRE + XW) * 2048); }
          SBAR();
        }
      }
      f32x4 vt = comb32(vt1, vt2);
      u32x2 vpk = {cvt_pk(vt[0], vt[1]), cvt_pk(vt[2], vt[3])};
      if (__builtin_expect(s + 1 < NSEG, 1)) {
        bf16x8 va = __builtin_bit_cast(bf16x8, (u32x4){vpk.x, vpk.y, 0u, 0u});
        wait_until(tb1, TM);
        CBAR();
        u32x4 m0 = LD16(rx, off_mm), m1 = LD16(rx, off_mm + 512);
        u32x2 axu = LD8(rx, off_ax), ms = LD8(rx, off_ms);
        while (__builtin_expect(isbad(max(mx4(m0), mx4(m1))), 0)) { if (--budget < 0) break; CBAR(); m0 = LD16(rx, off_mm); m1 = LD16(rx, off_mm + 512); }
        while (__builtin_expect(isbad(max(umax3(axu.x, axu.y, ms.x), ms.y)), 0)) { if (--budget < 0) break; __builtin_amdgcn_s_sleep(1); CBAR(); axu = LD8(rx, off_ax); ms = LD8(rx, off_ms); }
        off_mm += XCH_B; off_ms += XCH_B; off_ax += XCH_B;
        const float mx = u2f(ms.x), sumx = u2f(ms.y);
        f32x4 mv = __builtin_bit_cast(f32x4, m0) + __builtin_bit_cast(f32x4, m1);
        float mm = fmaxf(fmax3(mv[0], mv[1], mv[2]), fmaxf(mv[3], mx));
        mm = x16_max(mm);
        float em[4];
#pragma unroll
        for (int r = 0; r < 4; ++r) em[r] = __builtin_amdgcn_exp2f(mv[r] - mm);
        float fx = __builtin_amdgcn_exp2f(mx - mm);
        unsigned pm0 = cvt_pk(em[0], em[1]), pm1 = cvt_pk(em[2], em[3]);
        bf16x8 pm = __builtin_bit_cast(bf16x8, (u32x4){own_m ? pm0 : 0u, own_m ? pm1 : 0u, 0u, 0u});
        f32x4 axs = (f32x4){blo(axu.x), bhi(axu.x), blo(axu.y), bhi(axu.y)} * fx;
        f32x4 am = MFMA16(va, pm, axs);
        float summ = x16_sum((em[0] + em[1]) + (em[2] + em[3]));
        float rinv = __builtin_amdgcn_rcpf(fx * sumx + summ);
        f32x4 r4;
#pragma unroll
        for (int r = 0; r < 4; ++r) { float t = am[r] * rinv; r4[r] = t + dpp_f<0x128>(t); }
        *(LAS f32x4*)(lds + L_P + w * P_STR + n16 * 64 + g * 16) = r4;
        __builtin_amdgcn_raw_buffer_store_b64(vpk, rvtm, st_vtm, 0, 16);
        wg_barrier();
        if (w == 0) {
          LAS char* b = lds + L_P + (l & 1) * 4 * P_STR + (l >> 3) * 64 + gq * 16;
          f32x4 t0 = *(LAS f32x4*)b, t1 = *(LAS f32x4*)(b + P_STR), t2 = *(LAS f32x4*)(b + 2 * P_STR), t3 = *(LAS f32x4*)(b + 3 * P_STR);
          f32x4 t = (t0 + t1) + (t2 + t3);
#pragma unroll
          for (int r = 0; r < 4; ++r) t[r] += dpp_f<0xB1>(t[r]);
          unsigned p0 = min(cvt_pk(t[0], t[1]), 0xfffffffeu), p1 = min(cvt_pk(t[2], t[3]), 0xfffffffeu);
          unsigned q0 = dpp_u<0x4E>(p0), q1 = dpp_u<0x4E>(p1);
          __builtin_amdgcn_raw_buffer_store_b128((u32x4){p0, p1, q0, q1}, rmem, st_mem, 0, 16);
          st_mem += inc_mem;
        }
      } else {
        __builtin_amdgcn_raw_buffer_store_b64(vpk, rvtm, st_vtm, 0, 16);
      }
      st_vtm += KVM_B;
    } else {
      wg_barrier();
    }
  }
  drain_barrier();
  if (W7 && l == 0) flag_add(flag_ptr(p, F_CM, NSEG - 1));
}

constexpr int H_P = 0, HP_STR = 1056, H_E = 7 * HP_STR + 32;
DI void helper_wave(const Params& p, const int hid, LAS char* lds, const int w, const int l) {
  const int n16 = l & 15, g = l >> 4, hb = n16 >> 3, i8 = n16 & 7;
  const int h = hid >> 1, hf = hid & 1;
  const __amdgpu_buffer_rsrc_t rmem = __builtin_amdgcn_make_buffer_rsrc(p.ws + WS_MEMIMG, 0, (NSEG + 1) * MEMIMG_B, 0x00020000);
  const __amdgpu_buffer_rsrc_t rx = __builtin_amdgcn_make_buffer_rsrc(p.ws + WS_XCH, 0, NSEG * XCH_B, 0x00020000);
  const __amdgpu_buffer_rsrc_t rk = ws_rsrc(p, WS_KT, (size_t)SEQ * NKT * 2), ru = ws_rsrc(p, WS_UT, (size_t)SEQ * NKT * 2);
  const int wk = w < 7 ? w : 0;
  unsigned off_mem = (wk + 7 * hb) * 512 + g * 128 + i8 * 16;
  const int o0 = hf * 14 + 2 * wk;
  const unsigned kp = (unsigned)((h * 28 + wk) * 64 + l) * 16, up = (unsigned)((h * 28 + o0) * 64 + l) * 16;
  unsigned st_ax = n16 < 8 ? XA_OFF + (o0 * 8 + (h >> 1)) * 512 + (h & 1) * 256 + g * 64 + n16 * 8 : OOB;
  const unsigned inc_ax = n16 < 8 ? XCH_B : 0;
  unsigned st_ms = (l < 8 && hf == 0) ? XS_OFF + (h >> 1) * 128 + ((h & 1) * 8 + l) * 8 : OOB;
  const unsigned inc_ms = (l < 8 && hf == 0) ? XCH_B : 0;
  bf16x8 k00, k01, k10, k11, u[2];
  auto prefetch = [&](int s) {
    flag_wait(flag_ptr(p, F_XG, s), 56);
    const unsigned k = kp + (unsigned)s * (SEG * NKT * 2), q = up + (unsigned)s * (SEG * NKT * 2);
    k00 = LDF(rk, k); k01 = LDF(rk, k + 7 * 1024); k10 = LDF(rk, k + 14 * 1024); k11 = LDF(rk, k + 21 * 1024);
    u[0] = LDF(ru, q); u[1] = LDF(ru, q + 1024);
  };
  if (w < 7) prefetch(0);
  int budget = 1 << 20;
  unsigned tb1 = 0;
  for (int s = 0; s + 1 < NSEG; ++s) {
    if (w < 7) {
      if (s) wait_until(tb1, TH);
      CBAR();
      u32x4 v = LD16(rmem, off_mem);
      while (__builtin_expect(isbad(mx4(v)), 0)) { if (--budget < 0) break; __builtin_amdgcn_s_sleep(1); CBAR(); v = LD16(rmem, off_mem); }
      off_mem += MEMIMG_B;
      bf16x8 f = __builtin_bit_cast(bf16x8, v);
      asm volatile("" : "+v"(u[0]), "+v"(u[1]));
      f32x4 z = {0, 0, 0, 0};
      f32x4 a0 = MFMA16(k00, f, z), a1 = MFMA16(k01, f, z), b0 = MFMA16(k10, f, z), b1 = MFMA16(k11, f, z);
      f32x4 r;
#pragma unroll
      for (int q = 0; q < 4; ++q) { float own = hb ? b1[q] : a0[q]; float oth = hb ? a1[q] : b0[q]; r[q] = own + dpp_f<0x128>(oth); }
      *(LAS f32x4*)(lds + H_P + w * HP_STR + l * 16) = r;
    }
    wg_barrier();
    tb1 = now32();
    if (w == 0 || w == 7) {
      f32x4 t[7];
#pragma unroll
      for (int k = 0; k < 7; ++k) t[k] = *(LAS f32x4*)(lds + H_P + k * HP_STR + l * 16);
      f32x4 sc = ((t[0] + t[1]) + (t[2] + t[3])) + ((t[4] + t[5]) + t[6]);
      float mx = fmaxf(fmax3(sc[0], sc[1], sc[2]), sc[3]);
      mx = fmaxf(mx, dpp_f<0x128>(mx)); mx = x32_max(mx); mx = x16_max(mx);
      float e0 = __builtin_amdgcn_exp2f(sc[0] - mx), e1 = __builtin_amdgcn_exp2f(sc[1] - mx), e2 = __builtin_amdgcn_exp2f(sc[2] - mx), e3 = __builtin_amdgcn_exp2f(sc[3] - mx);
      if (w == 0) {
        *(LAS u32x2*)(lds + H_E + (g * 16 + i8) * 16 + hb * 8) = (u32x2){cvt_pk(e0, e1), cvt_pk(e2, e3)};
        wg_barrier();
      } else {
        wg_barrier();
        float sm = (e0 + e1) + (e2 + e3);
        sm += dpp_f<0x128>(sm); sm = x32_sum(sm); sm = x16_sum(sm);
        ST8(((u32x2){min(f2u(mx), 0xfffffffeu), min(f2u(sm), 0xfffffffeu)}), rx, st_ms);
        st_ms += inc_ms;
      }
    } else {
      wg_barrier();
    }
    if (w < 7) {
      bf16x8 pe = *(LAS bf16x8*)(lds + H_E + (g * 16 + i8) * 16);
      f32x4 z = {0, 0, 0, 0};
      f32x4 d0 = MFMA16(u[0], pe, z), d1 = MFMA16(u[1], pe, z);
      u32x2 s0 = {min(cvt_pk(d0[0], d0[1]), 0xfffffffeu), min(cvt_pk(d0[2], d0[3]), 0xfffffffeu)};
      u32x2 s1 = {min(cvt_pk(d1[0], d1[1]), 0xfffffffeu), min(cvt_pk(d1[2], d1[3]), 0xfffffffeu)};
      ST8(s0, rx, st_ax);
      ST8(s1, rx, n16 < 8 ? st_ax + 4096 : OOB);
    }
    st_ax += inc_ax;
    CBAR();
    if (w < 7) prefetch(s + 1);
  }
}

DI void comb_row(const Params& p, int row, int l) {
  const bool act = l < 56;
  const __amdgpu_buffer_rsrc_t rpo = ws_rsrc(p, WS_PO, (size_t)4 * SEQ * DM * 4), rh = ws_rsrc(p, WS_H, (size_t)SEQ * DM * 4), rhn = ws_rsrc(p, WS_HN, (size_t)SEQ * DM * 2);
  float v[8];
  const unsigned off = act ? (unsigned)(row * DM + l * 8) * 4 : OOB;
  u32x4 t = act ? *(const u32x4*)(p.x + (size_t)row * DM + l * 8) : (u32x4){0, 0, 0, 0};
  f32x4 a = {0, 0, 0, 0}, b = a;
#pragma unroll
  for (int k = 0; k < 4; ++k) {
    a += __builtin_bit_cast(f32x4, LD16(rpo, act ? off + k * (SEQ * DM * 4) : OOB));
    b += __builtin_bit_cast(f32x4, LD16(rpo, act ? off + k * (SEQ * DM * 4) + 16 : OOB));
  }
#pragma unroll
  for (int j = 0; j < 2; ++j) { v[2 * j] = blo(t[j]) + a[2 * j]; v[2 * j + 1] = bhi(t[j]) + a[2 * j + 1]; v[4 + 2 * j] = blo(t[2 + j]) + b[2 * j]; v[5 + 2 * j] = bhi(t[2 + j]) + b[2 * j + 1]; }
  ST16(((u32x4){f2u(v[0]), f2u(v[1]), f2u(v[2]), f2u(v[3])}), rh, off);
  ST16(((u32x4){f2u(v[4]), f2u(v[5]), f2u(v[6]), f2u(v[7])}), rh, act ? off + 16 : OOB);
  u32x4 r = ln_row(v, act);
  ST16(r, rhn, act ? tiled_off(row, l) : OOB);
}

DI void att_wg(const Params& p, LAS char* lds, int s, int hg, int w, int l) {
  const int n16 = l & 15, g = l >> 4;
  const int hl = w >> 1, qt = w & 1, h = hg * 4 + hl;
  const __amdgpu_buffer_rsrc_t rk = ws_rsrc(p, WS_KT, (size_t)SEQ * NKT * 2), ru = ws_rsrc(p, WS_UT, (size_t)SEQ * NKT * 2);
  const __amdgpu_buffer_rsrc_t rkm = ws_rsrc(p, WS_KTM, (size_t)NSEG * KVM_B), rvm = ws_rsrc(p, WS_VTM, (size_t)NSEG * KVM_B);
  const __amdgpu_buffer_rsrc_t rpo = ws_rsrc(p, WS_PO, (size_t)4 * SEQ * DM * 4);
  const bf16x8* XF = (const bf16x8*)(p.ws + WS_XF) + ((size_t)(s * 2 + qt) * 14) * 64 + l;
  bf16x8 q[14];
#pragma unroll
  for (int ks = 0; ks < 14; ++ks) q[ks] = XF[ks * 64];
  flag_wait(flag_ptr(p, F_XG, s), 56);
  const unsigned ko = (unsigned)(((s * NH + h) * 28) * 64 + l) * 16;
  f32x4 s0 = {0, 0, 0, 0}, s1 = s0, sm = s0;
#pragma unroll
  for (int ks = 0; ks < 14; ++ks) s0 = MFMA16(LDF(rk, ko + ks * 1024), q[ks], s0);
  if (qt) {
#pragma unroll
    for (int ks = 0; ks < 14; ++ks) s1 = MFMA16(LDF(rk, ko + (14 + ks) * 1024), q[ks], s1);
  }
  bf16x8 uv[4][4];
  const unsigned uo = (unsigned)(((s * NH + hg * 4) * 28 + w) * 64 + l) * 16;
#pragma unroll
  for (int j = 0; j < 4; ++j)
#pragma unroll
    for (int k = 0; k < 4; ++k) uv[j][k] = LDF(ru, uo + (k * 28 + ((j < 3 || w < 4) ? j * 8 : 0)) * 1024);
  flag_wait(flag_ptr(p, F_CM, s), 32);
  DSTAMP(s == 63 && hg == 0 && w == 0 && l == 0, 3)
  const unsigned kmo = (unsigned)s * KVM_B + (unsigned)(((h >> 1) * 14) * 64 + l) * 16;
  bf16x8 vm[4];
#pragma unroll
  for (int j = 0; j < 4; ++j) vm[j] = LDF(rvm, (unsigned)s * KVM_B + (unsigned)((hg * 28 + w + ((j < 3 || w < 4) ? j * 8 : 0)) * 64 + l) * 16);
#pragma unroll
  for (int ks = 0; ks < 14; ++ks) sm = MFMA16(LDF(rkm, kmo + ks * 1024), q[ks], sm);
  const float NEG = -3.0e38f;
  const bool ownm = (g >> 1) == (h & 1);
  float v[12];
#pragma unroll
  for (int r = 0; r < 4; ++r) {
    v[r] = (qt || (4 * g + r) <= n16) ? s0[r] : NEG;
    v[4 + r] = (qt && (4 * g + r) <= n16) ? s1[r] : NEG;
    v[8 + r] = ownm ? sm[r] : NEG;
  }
  float mx = NEG;
#pragma unroll
  for (int r = 0; r < 12; ++r) mx = fmaxf(mx, v[r]);
  mx = x16_max(mx); mx = x32_max(mx);
  float sum = 0.f;
#pragma unroll
  for (int r = 0; r < 12; ++r) { v[r] = v[r] > -1.0e38f ? __builtin_amdgcn_exp2f(v[r] - mx) : 0.f; sum += v[r]; }
  sum = x16_sum(sum); sum = x32_sum(sum);
  const float rinv = 1.0f / sum;
  u32x4 px = {cvt_pk(v[0] * rinv, v[1] * rinv), cvt_pk(v[2] * rinv, v[3] * rinv), cvt_pk(v[4] * rinv, v[5] * rinv), cvt_pk(v[6] * rinv, v[7] * rinv)};
  *(LAS u32x4*)(lds + (hl * 2 + qt) * 1024 + l * 16) = px;
  if (ownm) *(LAS u32x2*)(lds + 8192 + qt * 1024 + l * 16 + (hl >> 1) * 8) = (u32x2){cvt_pk(v[8] * rinv, v[9] * rinv), cvt_pk(v[10] * rinv, v[11] * rinv)};
  wg_barrier();
  bf16x8 P[2][5];
#pragma unroll
  for (int t = 0; t < 2; ++t) {
#pragma unroll
    for (int k = 0; k < 4; ++k) P[t][k] = *(LAS bf16x8*)(lds + (k * 2 + t) * 1024 + l * 16);
    P[t][4] = *(LAS bf16x8*)(lds + 8192 + t * 1024 + l * 16);
  }
  const unsigned po = (unsigned)((hg * SEQ + s * SEG + n16) * DM + 4 * g + w * 16) * 4;
#pragma unroll
  for (int j = 0; j < 4; ++j) {
    f32x4 d0 = {0, 0, 0, 0}, d1 = d0;
#pragma unroll
    for (int k = 0; k < 4; ++k) { d0 = MFMA16(uv[j][k], P[0][k], d0); d1 = MFMA16(uv[j][k], P[1][k], d1); }
    d0 = MFMA16(vm[j], P[0][4], d0); d1 = MFMA16(vm[j], P[1][4], d1);
    if (j < 3 || w < 4) { ST16(__builtin_bit_cast(u32x4, d0), rpo, po + j * 512); ST16(__builtin_bit_cast(u32x4, d1), rpo, po + j * 512 + 16 * DM * 4); }
  }
  drain_barrier();
  if (w == 0) {
    if (l == 0) flag_add(flag_ptr(p, F_AT, s));
    flag_wait(flag_ptr(p, F_AT, s), 4);
  }
  wg_barrier();
  DSTAMP(s == 63 && hg == 0 && w == 0 && l == 0, 4)
  comb_row(p, s * SEG + hg * 8 + w, l);
  drain_barrier();
  if (w == 0 && l == 0) flag_add(flag_ptr(p, F_HN, s));
}

constexpr int L_GW = 57344, G1_STR = 72, G2_STR = 40;
DI void glu1_stage(const Params& p, LAS char* lds, int fb, int tid) {
#pragma unroll
  for (int i = 0; i < 7; ++i) {
    const int c = i * 512 + tid, row = c >> 2, gu = row >= 448, d = row - 448 * gu;
    u32x4 v = *(const u32x4*)((gu ? p.wu : p.wg) + (size_t)d * FF + fb * 32 + (c & 3) * 8);
    LAS char* dst = lds + L_GW + row * G1_STR + (c & 3) * 16;
    *(LAS u32x2*)dst = (u32x2){v.x, v.y}; *(LAS u32x2*)(dst + 8) = (u32x2){v.z, v.w};
  }
}
DI void glu2_stage(const Params& p, LAS char* lds, int o, int tid) {
#pragma unroll
  for (int i = 0; i < 7; ++i) {
    const int c = i * 512 + tid, row = c >> 1;
    u32x4 v = *(const u32x4*)(p.wd + (size_t)row * DM + o * 16 + (c & 1) * 8);
    LAS char* dst = lds + L_GW + row * G2_STR + (c & 1) * 16;
    *(LAS u32x2*)dst = (u32x2){v.x, v.y}; *(LAS u32x2*)(dst + 8) = (u32x2){v.z, v.w};
  }
}
DI bf16x8 tr_frag(LAS char* b, int str) {
  s16x4 lo = __builtin_amdgcn_ds_read_tr16_b64_v4i16((LAS s16x4*)b);
  s16x4 hi = __builtin_amdgcn_ds_read_tr16_b64_v4i16((LAS s16x4*)(b + 4 * str));
  u32x2 l2 = __builtin_bit_cast(u32x2, lo), h2 = __builtin_bit_cast(u32x2, hi);
  return __builtin_bit_cast(bf16x8, (u32x4){l2.x, l2.y, h2.x, h2.y});
}
DI void glu1_wg(const Params& p, LAS char* lds, int fb, int w, int l) {
  const int ftl = w & 1, rt = (w >> 1) & 1, sp = w >> 2;
  const __amdgpu_buffer_rsrc_t rhn = ws_rsrc(p, WS_HN, (size_t)SEQ * DM * 2), rhg = ws_rsrc(p, WS_HG, (size_t)SEQ * FF * 2);
  bf16x8 wgt[14], wup[14];
#pragma unroll
  for (int ks = 0; ks < 14; ++ks) {
    LAS char* b = lds + L_GW + (32 * ks + 8 * (l >> 4) + ((l & 15) >> 2)) * G1_STR + ftl * 32 + (l & 3) * 8;
    wgt[ks] = tr_frag(b, G1_STR); wup[ks] = tr_frag(b + 448 * G1_STR, G1_STR);
  }
  const int n16 = l & 15, g = l >> 4;
  if (w == 0) *(LAS unsigned*)(lds + l * 4) = 0u;
  wg_barrier();
  for (int s = sp; s < NSEG; s += 2) {
    flag_wait(flag_ptr(p, F_HN, s), 4);
    DSTAMP(s == 63 && fb == 0 && w == 4 && l == 0, 5)
    const unsigned ho = (unsigned)(((s * 2 + rt) * 14) * 64 + l) * 16;
    bf16x8 x[14];
#pragma unroll
    for (int ks = 0; ks < 14; ++ks) x[ks] = LDF(rhn, ho + ks * 1024);
    f32x4 ag = {0, 0, 0, 0}, au = ag;
#pragma unroll
    for (int ks = 0; ks < 14; ++ks) { ag = MFMA16(wgt[ks], x[ks], ag); au = MFMA16(wup[ks], x[ks], au); }
    u32x2 o = {cvt_pk(ag[0] * au[0], ag[1] * au[1]), cvt_pk(ag[2] * au[2], ag[3] * au[3])};
    ST8(o, rhg, (unsigned)(((((s * 2 + rt) * 56 + fb) * 64 + (2 * ftl + (g >> 1)) * 16 + n16) * 8 + 4 * (g & 1)) * 2));
    asm volatile("s_waitcnt vmcnt(0)" ::: "memory");
    if (l == 0) {
      const unsigned t = __hip_atomic_fetch_add((LAS unsigned*)(lds + s * 4), 1u, __ATOMIC_RELAXED, __HIP_MEMORY_SCOPE_WORKGROUP);
      if (t == 3) flag_add(flag_ptr(p, F_G1, s));
    }
  }
}

DI void glu2_wg(const Params& p, LAS char* lds, int o, int s0, int w, int l) {
  const __amdgpu_buffer_rsrc_t rhg = ws_rsrc(p, WS_HG, (size_t)SEQ * FF * 2), rh = ws_rsrc(p, WS_H, (size_t)SEQ * DM * 4);
  bf16x8 wd[7];
#pragma unroll
  for (int k = 0; k < 7; ++k) wd[k] = tr_frag(lds + L_GW + (32 * (7 * w + k) + 8 * (l >> 4) + ((l & 15) >> 2)) * G2_STR + (l & 3) * 8, G2_STR);
  const int tid = w * 64 + l;
  int buf = 0;
  for (int s = s0; s < NSEG; s += 2) {
    if (w == 0) flag_wait(flag_ptr(p, F_G1, s), 56);
    wg_barrier();
    DSTAMP(s == 63 && o == 0 && tid == 0, 6)
    const unsigned ho = (unsigned)(((s * 2) * 56 + 7 * w) * 64 + l) * 16;
    bf16x8 a[7], b[7];
#pragma unroll
    for (int k = 0; k < 7; ++k) { a[k] = LDF(rhg, ho + k * 1024); b[k] = LDF(rhg, ho + (56 + k) * 1024); }
    const int row = s * SEG + ((tid >> 6) & 1) * 16 + (tid & 15), col = o * 16 + 4 * ((tid >> 4) & 3);
    u32x4 hv = {0, 0, 0, 0};
    if (tid < 128) hv = LD16(rh, (unsigned)(row * DM + col) * 4);
    f32x4 d0 = {0, 0, 0, 0}, d1 = d0;
#pragma unroll
    for (int k = 0; k < 7; ++k) { d0 = MFMA16(wd[k], a[k], d0); d1 = MFMA16(wd[k], b[k], d1); }
    LAS char* L = lds + buf * 16384;
    *(LAS f32x4*)(L + w * 2048 + l * 16) = d0;
    *(LAS f32x4*)(L + w * 2048 + 1024 + l * 16) = d1;
    wg_barrier();
    if (tid < 128) {
      f32x4 acc = __builtin_bit_cast(f32x4, hv);
#pragma unroll
      for (int k = 0; k < 8; ++k) acc += *(LAS f32x4*)(L + k * 2048 + tid * 16);
      *(u32x2*)(p.out + (size_t)row * DM + col) = (u32x2){cvt_pk(acc[0], acc[1]), cvt_pk(acc[2], acc[3])};
    }
    buf ^= 1;
  }
}

__global__ void __launch_bounds__(512, 2) k_main(Params p) {
  extern __shared__ __attribute__((aligned(16))) char smem[];
  LAS char* lds = (LAS char*)smem;
  const int xg = blockIdx.x & 7, r = blockIdx.x >> 3;
  const int w = __builtin_amdgcn_readfirstlane(threadIdx.x >> 6), l = threadIdx.x & 63;
  if (xg == 0) {
    const int cw = r;
    if (cw < 28) { if (w == 7) chain_wave<true, true>(p, cw, lds, w, l); else chain_wave<false, true>(p, cw, lds, w, l); }
    else { if (w == 7) chain_wave<true, false>(p, cw, lds, w, l); else chain_wave<false, false>(p, cw, lds, w, l); }
    return;
  }
  if (xg == 1) { helper_wave(p, r, lds, w, l); return; }
  const int wi = (xg - 2) * 32 + r;
  if (wi < 168) xgemm_wg(p, lds, wi % 56, wi / 56, 3, w, l);
  if (wi < 56) glu1_stage(p, lds, wi, threadIdx.x); else if (wi < 112) glu2_stage(p, lds, (wi - 56) % 28, threadIdx.x);
  wg_barrier();
  if (wi < 56) { glu1_wg(p, lds, wi, w, l); return; }
  if (wi < 112) {
    glu2_wg(p, lds, (wi - 56) % 28, (wi - 56) / 28, w, l);
    return;
  }
  for (;;) {
    if (threadIdx.x == 0) *(LAS unsigned*)(lds + 10244) = __hip_atomic_fetch_add(flag_ptr(p, F_Q, 0), 1u, __ATOMIC_RELAXED, __HIP_MEMORY_SCOPE_AGENT);
    wg_barrier();
    const unsigned t = __builtin_amdgcn_readfirstlane(*(LAS unsigned*)(lds + 10244));
    if (t >= NSEG * 4) break;
    att_wg(p, lds, t >> 2, t & 3, w, l);
  }
}

extern "C" void kernel_launch(void* const* d_in, const int* in_sizes, int n_in,
                              void* d_out, int out_size, void* d_ws, size_t ws_size,
                              hipStream_t stream) {
  Params p;
  p.x = (const uint16_t*)d_in[0]; p.mem0 = (const uint16_t*)d_in[1];
  p.Wq = (const uint16_t*)d_in[2]; p.Wk = (const uint16_t*)d_in[3]; p.Wv = (const uint16_t*)d_in[4]; p.Wo = (const uint16_t*)d_in[5];
  p.wg = (const uint16_t*)d_in[8]; p.wu = (const uint16_t*)d_in[9]; p.wd = (const uint16_t*)d_in[10];
  p.out = (uint16_t*)d_out; p.ws = (char*)d_ws;
  (void)hipFuncSetAttribute((const void*)k_main, hipFuncAttributeMaxDynamicSharedMemorySize, CHAIN_LDS);
  (void)hipFuncSetAttribute((const void*)k_pre, hipFuncAttributeMaxDynamicSharedMemorySize, PRE_LDS);
  k_pre<<<PRE_WT_BLOCKS + PRE_LN_BLOCKS, 512, PRE_LDS, stream>>>(p);
  k_main<<<256, 512, CHAIN_LDS, stream>>>(p);
}
```

```cpp
#include <hip/hip_runtime.h>
#include <hip/hip_bf16.h>
#include <stdint.h>

typedef __bf16 bf16x8 __attribute__((ext_vector_type(8)));
typedef __bf16 bf16x16 __attribute__((ext_vector_type(16)));
typedef __bf16 bf16x2 __attribute__((ext_vector_type(2)));
typedef float f32x4 __attribute__((ext_vector_type(4)));
typedef unsigned u32x4 __attribute__((ext_vector_type(4)));
typedef unsigned u32x2 __attribute__((ext_vector_type(2)));
typedef unsigned u32x8 __attribute__((ext_vector_type(8)));
typedef short s16x4 __attribute__((ext_vector_type(4)));
#define LAS __attribute__((address_space(3)))
#define DI __device__ __forceinline__

constexpr int DM = 448, NH = 16, HD = 256, NSEG = 64, SEG = 32, SEQ = 2048, NTOT = 14336, NKT = 7168, FF = 1792;
constexpr int MEMIMG_B = 7168;
constexpr int XM_OFF = 0, XS_OFF = 8192, XA_OFF = 9216, XCH_B = 9216 + 28 * 4096;
constexpr int KVM_B = NH * 8 * DM * 2;

constexpr int KR = 5, XW = 1;
#define TUNE TM = 1900, MP = -1, T2 = 4300, TH = 4000, DB = 4, XSTAG = 8000, PRE0 = 3
constexpr int TUNE;

constexpr int L_M = 0, L_T = 7168, T_STR = 640, L_P = L_T + 8 * T_STR, P_STR = 1056, L_WB = L_P + 8 * P_STR + 64;
constexpr int L_WB_AL = (L_WB + 255) & ~255;
constexpr int CHAIN_LDS = L_WB_AL + 7 * (14 - KR) * 2048;

constexpr size_t WS_MEMIMG = 0;
constexpr size_t WS_XCH = WS_MEMIMG + (size_t)(NSEG + 1) * MEMIMG_B;
constexpr size_t WS_SENT_END = WS_XCH + (size_t)NSEG * XCH_B;
constexpr size_t WS_XF = (WS_SENT_END + 4095) & ~(size_t)4095;
constexpr size_t WS_WT = WS_XF + (size_t)SEQ * DM * 2;
constexpr size_t WS_KT = WS_WT + (size_t)NTOT * DM * 2;
constexpr size_t WS_UT = WS_KT + (size_t)SEQ * NKT * 2;
constexpr size_t WS_KTM = WS_UT + (size_t)SEQ * NKT * 2;
constexpr size_t WS_VTM = WS_KTM + (size_t)NSEG * KVM_B;
constexpr size_t WS_PO = WS_VTM + (size_t)NSEG * KVM_B;
constexpr size_t WS_H = WS_PO + (size_t)4 * SEQ * DM * 4;
constexpr size_t WS_HN = WS_H + (size_t)SEQ * DM * 4;
constexpr size_t WS_WG = WS_HN + (size_t)SEQ * DM * 2;
constexpr size_t WS_WD = WS_WG + (size_t)2 * FF * DM * 2;
constexpr size_t WS_HG = WS_WD + (size_t)FF * DM * 2;
constexpr size_t WS_FLG = WS_HG + (size_t)SEQ * FF * 2;
constexpr size_t WS_END = WS_FLG + 6 * NSEG * 128;
constexpr int F_XG = 0, F_CM = 1, F_AT = 2, F_HN = 3, F_G1 = 4, F_Q = 5;
#define DSTAMP(c, k)
#define CS(k)

struct Params {
  const uint16_t* x; const uint16_t* mem0; const uint16_t* Wq; const uint16_t* Wk; const uint16_t* Wv; const uint16_t* Wo;
  const uint16_t* wg; const uint16_t* wu; const uint16_t* wd; uint16_t* out; char* ws;
};

DI unsigned cvt_pk(float a, float b) { bf16x2 r = {(__bf16)a, (__bf16)b}; return __builtin_bit_cast(unsigned, r); }
DI void wg_barrier() { asm volatile("s_waitcnt lgkmcnt(0)\n\ts_barrier" ::: "memory"); }
DI unsigned umax3(unsigned a, unsigned b, unsigned c) { return max(max(a, b), c); }
DI float fmax3(float a, float b, float c) { return fmaxf(fmaxf(a, b), c); }
DI float u2f(unsigned u) { return __builtin_bit_cast(float, u); }
DI unsigned f2u(float f) { return __builtin_bit_cast(unsigned, f); }
DI float blo(unsigned u) { return u2f(u << 16); }
DI float bhi(unsigned u) { return u2f(u & 0xffff0000u); }
template<int CTRL> DI float dpp_f(float x) { return u2f((unsigned)__builtin_amdgcn_update_dpp(0, (int)f2u(x), CTRL, 0xf, 0xf, true)); }
template<int CTRL> DI unsigned dpp_u(unsigned x) { return (unsigned)__builtin_amdgcn_update_dpp(0, (int)x, CTRL, 0xf, 0xf, true); }
DI float x32_max(float x) { auto r = __builtin_amdgcn_permlane32_swap(f2u(x), f2u(x), false, false); return fmaxf(u2f(r[0]), u2f(r[1])); }
DI float x32_sum(float x) { auto r = __builtin_amdgcn_permlane32_swap(f2u(x), f2u(x), false, false); return u2f(r[0]) + u2f(r[1]); }
DI float x16_max(float x) { auto r = __builtin_amdgcn_permlane16_swap(f2u(x), f2u(x), false, false); return fmaxf(u2f(r[0]), u2f(r[1])); }
DI float x16_sum(float x) { auto r = __builtin_amdgcn_permlane16_swap(f2u(x), f2u(x), false, false); return u2f(r[0]) + u2f(r[1]); }
DI float wave_sum(float v) {
  v += dpp_f<0xB1>(v); v += dpp_f<0x4E>(v); v += dpp_f<0x124>(v); v += dpp_f<0x128>(v);
  v = x16_sum(v); v = x32_sum(v); return v;
}
#define SMFMAC(a, b, c, idx) __builtin_amdgcn_smfmac_f32_16x16x64_bf16((a), (b), (c), (idx), 0, 0)
#define MFMA16(a, b, c) __builtin_amdgcn_mfma_f32_16x16x32_bf16((a), (b), (c), 0, 0, 0)
constexpr unsigned OOB = 0x7fffffffu;
#define LD16(r, off) __builtin_amdgcn_raw_buffer_load_b128((r), (off), 0, 16)
#define LD8(r, off) __builtin_amdgcn_raw_buffer_load_b64((r), (off), 0, 16)
#define CBAR() asm volatile("" ::: "memory")
#define SBAR() __builtin_amdgcn_sched_barrier(0)

DI unsigned* flag_ptr(const Params& p, int kind, int s) { return (unsigned*)(p.ws + WS_FLG + (size_t)(kind * NSEG + s) * 128); }
DI void flag_add(unsigned* f) { __hip_atomic_fetch_add(f, 1u, __ATOMIC_RELAXED, __HIP_MEMORY_SCOPE_AGENT); }
DI void flag_wait(const unsigned* f, unsigned want) {
  int budget = 1 << 20;
  while ((unsigned)__builtin_amdgcn_readfirstlane(__hip_atomic_load(f, __ATOMIC_RELAXED, __HIP_MEMORY_SCOPE_AGENT)) < want) {
    if (--budget < 0) break;
    __builtin_amdgcn_s_sleep(8);
  }
  asm volatile("" ::: "memory");
}
DI void drain_barrier() { asm volatile("s_waitcnt vmcnt(0) lgkmcnt(0)\n\ts_barrier" ::: "memory"); }
DI __amdgpu_buffer_rsrc_t ws_rsrc(const Params& p, size_t off, size_t bytes) { return __builtin_amdgcn_make_buffer_rsrc(p.ws + off, 0, (int)bytes, 0x00020000); }
#define LDF(r, off) __builtin_bit_cast(bf16x8, LD16((r), (off)))
#define ST16(v, r, off) __builtin_amdgcn_raw_buffer_store_b128((v), (r), (off), 0, 16)
#define ST8(v, r, off) __builtin_amdgcn_raw_buffer_store_b64((v), (r), (off), 0, 16)

DI u32x4 ln_row(float (&v)[8], bool act) {
  float s = 0.f;
#pragma unroll
  for (int j = 0; j < 8; ++j) s += v[j];
  s = wave_sum(act ? s : 0.f);
  const float mean = s * (1.0f / DM);
  float q = 0.f;
#pragma unroll
  for (int j = 0; j < 8; ++j) { float d = v[j] - mean; q += d * d; }
  q = wave_sum(act ? q : 0.f);
  const float rstd = 1.0f / sqrtf(q * (1.0f / DM) + 1e-5f);
  u32x4 r;
#pragma unroll
  for (int j = 0; j < 4; ++j) r[j] = cvt_pk((v[2 * j] - mean) * rstd, (v[2 * j + 1] - mean) * rstd);
  return r;
}
DI unsigned tiled_off(int row, int c8) { return (unsigned)((((row >> 4) * 14 + (c8 >> 2)) * 64 + (c8 & 3) * 16 + (row & 15)) * 16); }

constexpr int WQ_STR = 144, WP_OFF = 224 * WQ_STR, WO_STR = 240, WBUF = 336 * WQ_STR + 4608;
template<bool isB>
DI void wt_wg(const Params& p, LAS char* lds, int task, int w, int l) {
  const int n16 = l & 15, g = l >> 4, tid = w * 64 + l;
  const int mat = task >> 3, qe = (task >> 2) & 1, qn = task & 3;
  const int h = mat & 15;
  const uint16_t* Qg = (isB ? p.Wv : p.Wk) + (size_t)(qe * 224) * (NH * HD) + h * HD;
  const uint16_t* Pg = isB ? p.Wo + (size_t)h * HD * DM + qn * 112 : p.Wq + (size_t)(qn * 112) * (NH * HD) + h * HD;
  u32x4 r[4][6];
  const uint16_t* src[6]; int loff[6];
#pragma unroll
  for (int i = 0; i < 6; ++i) {
    const int c = min(i * 512 + tid, 2687), c2 = c - 1792;
    src[i] = c < 1792 ? Qg + (size_t)(c >> 3) * (NH * HD) + (c & 7) * 8
           : (isB ? Pg + (size_t)(c2 / 14) * DM + (c2 % 14) * 8 : Pg + (size_t)(c2 >> 3) * (NH * HD) + (c2 & 7) * 8);
    loff[i] = (c < 1792 || !isB) ? (c >> 3) * WQ_STR + (c & 7) * 16 : WP_OFF + (c2 / 14) * WO_STR + (c2 % 14) * 16;
  }
  const int strP = isB ? 64 * DM : 64, str3 = tid < 256 ? 64 : strP;
  const int rot = (qn + 2 * qe) & 3;
  auto gload = [&](int j) {
    const int kk = (j + rot) & 3;
#pragma unroll
    for (int i = 0; i < 6; ++i) r[j][i] = *(const u32x4*)(src[i] + kk * (i < 3 ? 64 : (i == 3 ? str3 : strP)));
  };
  gload(0);
  const int we = w >> 2, wn = w & 3;
  f32x4 acc[8][2];
#pragma unroll
  for (int a = 0; a < 8; ++a) { acc[a][0] = (f32x4){0, 0, 0, 0}; acc[a][1] = (f32x4){0, 0, 0, 0}; }
  const int qrow = 8 * (n16 >> 2) + (n16 & 3);
  gload(1); gload(2); gload(3);
  DSTAMP(blockIdx.x == 40 && tid == 0, 76)
#pragma unroll 1
  for (int it = 0; it < 2; ++it) {
    if (it) wg_barrier();
#pragma unroll
    for (int i = 0; i < 6; ++i) { *(LAS u32x4*)(lds + loff[i]) = r[0][i]; *(LAS u32x4*)(lds + WBUF + loff[i]) = r[1][i]; }
    wg_barrier();
    DSTAMP(blockIdx.x == 40 && tid == 0, 74 + it)
#pragma unroll 2
    for (int k4 = 0; k4 < 4; ++k4) {
      LAS char* L = lds + (k4 >> 1) * WBUF;
      const int k = k4 & 1;
      bf16x8 pb[2];
#pragma unroll
      for (int c = 0; c < 2; ++c) {
        const int nt = wn + 4 * c;
        if (isB) {
          LAS char* bb = L + WP_OFF + (k * 32 + 8 * g + (n16 >> 2)) * WO_STR + nt * 32 + (n16 & 3) * 8;
          s16x4 lo = __builtin_amdgcn_ds_read_tr16_b64_v4i16((LAS s16x4*)bb);
          s16x4 hi = __builtin_amdgcn_ds_read_tr16_b64_v4i16((LAS s16x4*)(bb + 4 * WO_STR));
          u32x2 l2 = __builtin_bit_cast(u32x2, lo), h2 = __builtin_bit_cast(u32x2, hi);
          pb[c] = __builtin_bit_cast(bf16x8, (u32x4){l2.x, l2.y, h2.x, h2.y});
        } else {
          pb[c] = *(LAS bf16x8*)(L + WP_OFF + (nt * 16 + n16) * WQ_STR + k * 64 + g * 16);
        }
      }
#pragma unroll
      for (int a = 0; a < 8; ++a) {
        const int ep = 4 * we + (a >> 1);
        bf16x8 qa = *(LAS bf16x8*)(L + (ep * 32 + (a & 1) * 4 + qrow) * WQ_STR + k * 64 + g * 16);
        acc[a][0] = MFMA16(qa, pb[0], acc[a][0]); acc[a][1] = MFMA16(qa, pb[1], acc[a][1]);
      }
    }
#pragma unroll
    for (int i = 0; i < 6; ++i) { r[0][i] = r[2][i]; r[1][i] = r[3][i]; }
  }
  DSTAMP(blockIdx.x == 40 && tid == 0, 77)
  const float sc = isB ? 1.0f : 0.0625f * 1.4426950408889634f;
  const __amdgpu_buffer_rsrc_t rw = ws_rsrc(p, WS_WT, (size_t)NTOT * DM * 2);
#pragma unroll
  for (int c = 0; c < 2; ++c)
#pragma unroll
    for (int m = 0; m < 4; ++m) {
      if ((c < 1 || wn < 3) && (m < 3 || we == 0)) {
        const int nt = ((isB ? NKT : 0) + h * DM + qn * 112) / 16 + wn + 4 * c, ks = qe * 7 + 4 * we + m;
        const f32x4 x = acc[2 * m][c] * sc, y = acc[2 * m + 1][c] * sc;
        ST16(((u32x4){cvt_pk(x[0], x[1]), cvt_pk(x[2], x[3]), cvt_pk(y[0], y[1]), cvt_pk(y[2], y[3])}), rw, (unsigned)((nt * 14 + ks) * 64 + l) * 16);
      }
    }
}

DI void pre_phase(const Params& p, LAS char* lds, int b, int w, int l) {
  const int tid = w * 64 + l;
  const __amdgpu_buffer_rsrc_t rs = ws_rsrc(p, 0, WS_XF + (size_t)SEQ * DM * 2);
  const bool act = l < 56;
  const int row = b * 8 + w;
  u32x4 xt = act ? *(const u32x4*)(p.x + (size_t)row * DM + l * 8) : (u32x4){0, 0, 0, 0};
  const int task = (b & 7) * 32 + (b >> 3);
  if (task >= 128) wt_wg<true>(p, lds, task, w, l); else wt_wg<false>(p, lds, task, w, l);
  {
    float v[8];
#pragma unroll
    for (int j = 0; j < 4; ++j) { v[2 * j] = blo(xt[j]); v[2 * j + 1] = bhi(xt[j]); }
    u32x4 r = ln_row(v, act);
    ST16(r, rs, act ? (unsigned)WS_XF + tiled_off(row, l) : OOB);
  }
  if (tid < 32) {
    const int i = b * 32 + tid;
    ST16(((u32x4){~0u, ~0u, ~0u, ~0u}), rs, i < XCH_B / 16 ? (unsigned)WS_XCH + i * 16 : (unsigned)MEMIMG_B + (i - XCH_B / 16) * 16);
  }
  if (b == 9 && tid < 448) {
    u32x4 t = *(const u32x4*)(p.mem0 + (tid & 7) * DM + (tid >> 3) * 8);
    t.x = min(t.x, 0xfffffffeu); t.y = min(t.y, 0xfffffffeu); t.z = min(t.z, 0xfffffffeu); t.w = min(t.w, 0xfffffffeu);
    ST16(t, rs, tid * 16);
  }
  DSTAMP(b == 40 && tid == 0, 72)
  drain_barrier();
  DSTAMP(b == 40 && tid == 0, 73)
  if (w == 0) {
    if (l == 0) {
      const unsigned t = __hip_atomic_fetch_add(flag_ptr(p, F_Q, 8 + (b & 7)), 1u, __ATOMIC_RELAXED, __HIP_MEMORY_SCOPE_AGENT);
      if (t == 31) flag_add(flag_ptr(p, F_Q, 16));
    }
    int budget = 1 << 20;
    while ((unsigned)__builtin_amdgcn_readfirstlane(__hip_atomic_load(flag_ptr(p, F_Q, 16), __ATOMIC_RELAXED, __HIP_MEMORY_SCOPE_AGENT)) < 8u) {
      if (--budget < 0) break;
      __builtin_amdgcn_s_sleep(2);
    }
  }
  wg_barrier();
}

DI void xgemm_wg(const Params& p, LAS char* lds, int nb, int seg0, int step, int w, int l) {
  const int n16 = l & 15, g = l >> 4;
  const int n0 = nb * 256 + w * 32;
  const bool isU = n0 >= NKT;
  const int r0 = isU ? n0 + n16 : n0 + 8 * (n16 >> 2) + (n16 & 3);
  const int r1 = isU ? r0 + 16 : r0 + 4;
  const __amdgpu_buffer_rsrc_t rw = ws_rsrc(p, WS_WT, (size_t)NTOT * DM * 2), rxf = ws_rsrc(p, WS_XF, (size_t)SEQ * DM * 2);
  const unsigned W0 = (unsigned)((r0 >> 4) * 14 * 64 + g * 16 + (r0 & 15)) * 16, W1 = (unsigned)((r1 >> 4) * 14 * 64 + g * 16 + (r1 & 15)) * 16;
  bf16x8 w0[14], w1[14];
#pragma unroll
  for (int ks = 0; ks < 14; ++ks) { w0[ks] = LDF(rw, W0 + ks * 1024); w1[ks] = LDF(rw, W1 + ks * 1024); }
  const int nn = isU ? n0 - NKT : n0;
  const int hh = nn / DM, cc = nn % DM;
  const int tid = w * 64 + l;
  u32x4 pre[4];
  auto gload = [&](int s) {
#pragma unroll
    for (int i = 0; i < 4; ++i) { const int c = i * 512 + tid; pre[i] = LD16(rxf, c < 1792 ? (unsigned)(s * 1792 + c) * 16 : OOB); }
  };
  gload(seg0);
  int buf = 0, prev = -1;
  const __amdgpu_buffer_rsrc_t rout = ws_rsrc(p, isU ? WS_UT : WS_KT, (size_t)SEQ * NKT * 2);
  const __amdgpu_buffer_rsrc_t rsent = ws_rsrc(p, 0, WS_SENT_END);
  for (int s = seg0; s < NSEG; s += step) {
    LAS char* L = lds + buf * 28672;
#pragma unroll
    for (int i = 0; i < 4; ++i) { const int c = i * 512 + tid; if (c < 1792) *(LAS u32x4*)(L + c * 16) = pre[i]; }
    asm volatile("s_waitcnt vmcnt(0)" ::: "memory");
    if (s + step < NSEG) gload(s + step);
    wg_barrier();
    if (tid == 0 && prev >= 0) flag_add(flag_ptr(p, F_XG, prev));
    f32x4 a00 = {0, 0, 0, 0}, a01 = a00, a10 = a00, a11 = a00;
#pragma unroll
    for (int ks = 0; ks < 14; ++ks) {
      bf16x8 x0 = *(LAS bf16x8*)(L + ks * 1024 + l * 16), x1 = *(LAS bf16x8*)(L + (14 + ks) * 1024 + l * 16);
      if (isU) { a00 = MFMA16(x0, w0[ks], a00); a01 = MFMA16(x1, w0[ks], a01); a10 = MFMA16(x0, w1[ks], a10); a11 = MFMA16(x1, w1[ks], a11); }
      else     { a00 = MFMA16(w0[ks], x0, a00); a01 = MFMA16(w1[ks], x0, a01); a10 = MFMA16(w0[ks], x1, a10); a11 = MFMA16(w1[ks], x1, a11); }
    }
    u32x4 o0 = {cvt_pk(a00[0], a00[1]), cvt_pk(a00[2], a00[3]), cvt_pk(a01[0], a01[1]), cvt_pk(a01[2], a01[3])};
    u32x4 o1 = {cvt_pk(a10[0], a10[1]), cvt_pk(a10[2], a10[3]), cvt_pk(a11[0], a11[1]), cvt_pk(a11[2], a11[3])};
    {
      const unsigned off = (unsigned)((((s * NH + hh) * 28 + (isU ? (cc >> 4) : (cc >> 5))) * 64 + l) * 16);
      ST16(o0, rout, off); ST16(o1, rout, off + (isU ? 1024 : 14 * 1024));
    }
    if (s + 1 < NSEG) {
      const int i = nb * 147 + tid;
      if (tid < 147 && i < (XCH_B + MEMIMG_B) / 16) {
        const unsigned off = i < XCH_B / 16 ? (unsigned)(WS_XCH + (size_t)(s + 1) * XCH_B) + i * 16 : (unsigned)((s + 2) * MEMIMG_B) + (i - XCH_B / 16) * 16;
        ST16(((u32x4){~0u, ~0u, ~0u, ~0u}), rsent, off);
      }
    }
    prev = s;
    buf ^= 1;
  }
  drain_barrier();
  if (tid == 0 && prev >= 0) flag_add(flag_ptr(p, F_XG, prev));
}
DI bf16x16 load_wfrag(__amdgpu_buffer_rsrc_t rw, unsigned base, int k) {
  u32x2 alo = LD8(rw, base + k * 1024), ahi = LD8(rw, base + k * 1024 + 512);
  u32x2 blo_ = LD8(rw, base + (k + 7) * 1024), bhi_ = LD8(rw, base + (k + 7) * 1024 + 512);
  u32x8 r = {alo.x, blo_.x, alo.y, blo_.y, ahi.x, bhi_.x, ahi.y, bhi_.y};
  return __builtin_bit_cast(bf16x16, r);
}
DI f32x4 comb32(f32x4 a, f32x4 b) {
  f32x4 r;
#pragma unroll
  for (int i = 0; i < 4; ++i) { float fa = a[i], fb = b[i]; auto t = __builtin_amdgcn_permlane32_swap(f2u(fa), f2u(fb), false, false); r[i] = u2f(t[0]) + u2f(t[1]); }
  return r;
}
DI bf16x16 lds_wfrag(LAS char* d) { u32x4 t0 = *(LAS u32x4*)d, t1 = *(LAS u32x4*)(d + 1024);
  u32x8 t = {t0.x, t0.y, t0.z, t0.w, t1.x, t1.y, t1.z, t1.w}; return __builtin_bit_cast(bf16x16, t); }
DI unsigned now32() { return (unsigned)__builtin_amdgcn_s_memtime(); }
DI void wait_until(unsigned t0, unsigned d) { while (now32() - t0 < d) { } }
DI void wait_until_asm(unsigned t0, unsigned d) {
  asm volatile("1:\n\ts_memtime vcc\n\ts_waitcnt lgkmcnt(0)\n\ts_sub_u32 vcc_lo, vcc_lo, %0\n\ts_cmp_lt_u32 vcc_lo, %1\n\ts_cbranch_scc1 1b"
               :: "s"(t0), "s"(d) : "vcc", "scc", "memory");
}
DI unsigned mx4(u32x4 v) { return max(umax3(v.x, v.y, v.z), v.w); }
DI bool isbad(unsigned m) { return __builtin_amdgcn_ballot_w64(m == 0xffffffffu) != 0; }

template<bool W7, bool OUT>
DI void chain_wave(const Params& p, const int cw, LAS char* lds, const int w, const int l) {
  constexpr int KRW = W7 ? 14 : KR;
  constexpr int NL = 14 - KRW;
  constexpr int PRE = W7 ? 0 : PRE0;
  const int n16 = l & 15, g = l >> 4, tid = w * 64 + l;
  const int h = cw >> 1, c = cw & 1, o = cw;
  const int idx = n16 < 8 ? 0x4444 : 0xEEEE;
  const __amdgpu_buffer_rsrc_t rw = ws_rsrc(p, WS_WT, (size_t)NTOT * DM * 2);
  const unsigned WX = ((g >> 1) * 16 + n16) * 16 + (g & 1) * 8;
  const __amdgpu_buffer_rsrc_t rmem = __builtin_amdgcn_make_buffer_rsrc(p.ws + WS_MEMIMG, 0, (NSEG + 1) * MEMIMG_B, 0x00020000);
  const __amdgpu_buffer_rsrc_t rx = __builtin_amdgcn_make_buffer_rsrc(p.ws + WS_XCH, 0, NSEG * XCH_B, 0x00020000);
  const __amdgpu_buffer_rsrc_t rktm = __builtin_amdgcn_make_buffer_rsrc(p.ws + WS_KTM, 0, NSEG * KVM_B, 0x00020000);
  const __amdgpu_buffer_rsrc_t rvtm = __builtin_amdgcn_make_buffer_rsrc(p.ws + WS_VTM, 0, NSEG * KVM_B, 0x00020000);
  bf16x16 wa[W7 ? 1 : 14];
  bf16x16 wb[OUT ? KRW : 1];
  if (!W7) {
    const unsigned ra = WX + (unsigned)(h * 28 + 14 * c + 2 * w) * 14336;
#pragma unroll
    for (int u = 0; u < 14; ++u) wa[u] = load_wfrag(rw, ra + (u & 1) * 14336, u >> 1);
  }
  LAS char* wl = lds + L_WB_AL + w * (14 - KR) * 2048 + l * 16;
  if (OUT) {
    const unsigned ra = WX + (unsigned)(448 + (2 * w) * 28 + o) * 14336;
#pragma unroll
    for (int u = 0; u < 14; ++u) {
      bf16x16 f = load_wfrag(rw, ra + (u & 1) * 28 * 14336, u >> 1);
      if (u < KRW) wb[u] = f;
      else { u32x8 t = __builtin_bit_cast(u32x8, f); LAS char* d = wl + (u - KRW) * 2048;
        *(LAS u32x4*)d = (u32x4){t[0], t[1], t[2], t[3]}; *(LAS u32x4*)(d + 1024) = (u32x4){t[4], t[5], t[6], t[7]}; }
    }
  }
  if (W7) { if (l < T_STR / 16) *(LAS u32x4*)(lds + L_T + 7 * T_STR + l * 16) = (u32x4){0, 0, 0, 0}; }
  unsigned off_mem = tid * 16;
  const int hb = n16 >> 3, i8 = n16 & 7;
  unsigned off_mm = XM_OFF + w * 1024 + (hb * 16 + (g & 1) * 8 + i8) * 16;
  unsigned off_ms = XS_OFF + w * 128 + n16 * 8;
  unsigned off_ax = XA_OFF + (o * 8 + w) * 512 + hb * 256 + g * 64 + i8 * 8;
  const bool own_m = hb == (g >> 1);
  const bool st_m = n16 < 8 && g < 2;
  unsigned st_mm = st_m ? XM_OFF + (h >> 1) * 1024 + c * 512 + ((h & 1) * 16 + g * 8 + i8) * 16 : OOB;
  const unsigned inc_mm = st_m ? XCH_B : 0;
  const int gq = (l >> 1) & 3;
  unsigned st_mem = ((l & 3) == 0) ? MEMIMG_B + o * 256 + (gq >> 1) * 128 + (l >> 3) * 16 : OOB;
  const unsigned inc_mem = ((l & 3) == 0) ? MEMIMG_B : 0;
  unsigned st_vtm = ((w * 28 + o) * 64 + l) * 8;
  unsigned st_ktm = ((((h >> 1) * 14 + 7 * c + (g >> 1)) * 4 + 2 * hb + (g & 1)) * 16 + (h & 1) * 8 + i8) * 16;
  LAS char* a_rd = lds + L_M + hb * 7 * 512 + g * 128 + i8 * 16;
  int budget = 1 << 20;
  unsigned tb1 = 0;
  wg_barrier();
  for (int s = 0; s < NSEG; ++s) {
    bf16x16 tw[NL > 0 ? NL : 1];
    if (OUT) {
#pragma unroll
      for (int j = 0; j < PRE; ++j) tw[j] = lds_wfrag(wl + j * 2048);
      SBAR();
    }
    if (!W7) {
      if (s) wait_until(tb1, T2);
      CBAR();
      u32x4 v = LD16(rmem, off_mem);
      if (__builtin_expect(isbad(mx4(v)), 0)) { do { CBAR(); v = LD16(rmem, off_mem); } while (isbad(mx4(v)) && --budget > 0); }
      CS(9)
      *(LAS u32x4*)(lds + L_M + tid * 16) = v;
      off_mem += MEMIMG_B;
    }
    if (W7) asm volatile("s_waitcnt vmcnt(0)" ::: "memory");
    wg_barrier();
    tb1 = now32();
    if (W7 && l == 0 && s > 0) flag_add(flag_ptr(p, F_CM, s - 1));
    bf16x8 af[7];
#pragma unroll
    for (int k = 0; k < 7; ++k) af[k] = *(LAS bf16x8*)(a_rd + k * 512);
    f32x4 vt1 = {0, 0, 0, 0}, vt2 = {0, 0, 0, 0};
    if (!W7) {
      f32x4 acc1 = {0, 0, 0, 0}, acc2 = {0, 0, 0, 0};
#pragma unroll
      for (int k = 0; k < 7; ++k) { acc1 = SMFMAC(af[k], wa[2 * k], acc1, idx); acc2 = SMFMAC(af[k], wa[2 * k + 1], acc2, idx); }
      f32x4 acc = comb32(acc1, acc2);
      u32x2 pk = {cvt_pk(acc[0], acc[1]), cvt_pk(acc[2], acc[3])};
      *(LAS u32x2*)(lds + L_T + w * T_STR + n16 * 40 + g * 8) = pk;
      wg_barrier();
      CS(0)
    } else {
      bf16x8 mb[4];
#pragma unroll
      for (int q = 0; q < 4; ++q) { int ks = min(7 * c + 2 * q + (g >> 1), 13); mb[q] = *(LAS bf16x8*)(lds + L_M + ks * 512 + (2 * hb + (g & 1)) * 128 + i8 * 16); }
      if (OUT) {
#pragma unroll
        for (int k = 0; k < 7; ++k) { vt1 = SMFMAC(af[k], wb[2 * k], vt1, idx); vt2 = SMFMAC(af[k], wb[2 * k + 1], vt2, idx); }
      }
      asm volatile("" : "+v"(vt1), "+v"(vt2));
      SBAR();
      __builtin_amdgcn_s_setprio(3);
      wg_barrier();
      f32x4 d2 = {0, 0, 0, 0}, d3 = {0, 0, 0, 0};
      bf16x8 kt[4];
#pragma unroll
      for (int q = 0; q < 4; ++q) {
        LAS char* b = lds + L_T + (2 * q + (g >> 1)) * T_STR + (8 * (g & 1) + (n16 >> 2)) * 40 + (n16 & 3) * 8;
        s16x4 lo = __builtin_amdgcn_ds_read_tr16_b64_v4i16((LAS s16x4*)b);
        s16x4 hi = __builtin_amdgcn_ds_read_tr16_b64_v4i16((LAS s16x4*)(b + 160));
        u32x2 l2 = __builtin_bit_cast(u32x2, lo), h2 = __builtin_bit_cast(u32x2, hi);
        kt[q] = __builtin_bit_cast(bf16x8, (u32x4){l2.x, l2.y, h2.x, h2.y});
      }
      d2 = MFMA16(kt[0], mb[0], d2); d3 = MFMA16(kt[1], mb[1], d3); d2 = MFMA16(kt[2], mb[2], d2); d3 = MFMA16(kt[3], mb[3], d3);
      u32x4 s2;
#pragma unroll
      for (int r = 0; r < 4; ++r) { float f = d2[r] + d3[r]; auto t = __builtin_amdgcn_permlane32_swap(f2u(f), f2u(f), false, false);
        s2[r] = min(f2u(f + dpp_f<0x128>(u2f(t[1]))), 0xfffffffeu); }
      __builtin_amdgcn_raw_buffer_store_b128(s2, rx, st_mm, 0, 16);
      CS(0)
      __builtin_amdgcn_s_setprio(0);
      CBAR();
#pragma unroll
      for (int q = 0; q < 4; ++q) __builtin_amdgcn_raw_buffer_store_b128(__builtin_bit_cast(u32x4, kt[q]), rktm, (q == 3 && g >= 2) ? OOB : st_ktm + q * 2048, 0, 16);
      st_mm += inc_mm; st_ktm += KVM_B;
    }
    if (OUT) {
      u32x4 m0, m1; u32x2 axu, ms;
#define XLOADS() { SBAR(); wait_until_asm(tb1, TM); CBAR(); m0 = LD16(rx, off_mm); m1 = LD16(rx, off_mm + 512); axu = LD8(rx, off_ax); ms = LD8(rx, off_ms); SBAR(); }
      if (!W7) {
        __builtin_amdgcn_s_sleep(DB);
#pragma unroll
        for (int u = 0; u < 14; ++u) {
          if (u < KRW) { if (u & 1) vt2 = SMFMAC(af[u >> 1], wb[u], vt2, idx); else vt1 = SMFMAC(af[u >> 1], wb[u], vt1, idx);
            if (u >= KRW - XW && PRE + (u - (KRW - XW)) < NL) tw[PRE + (u - (KRW - XW))] = lds_wfrag(wl + (PRE + (u - (KRW - XW))) * 2048); }
          else { if (u & 1) vt2 = SMFMAC(af[u >> 1], tw[u - KRW], vt2, idx); else vt1 = SMFMAC(af[u >> 1], tw[u - KRW], vt1, idx);
            if (u - KRW + PRE + XW < NL) tw[u - KRW + PRE + XW] = lds_wfrag(wl + (u - KRW + PRE + XW) * 2048); }
          SBAR();
          if (u == MP) XLOADS()
        }
      }
      f32x4 vt = comb32(vt1, vt2);
      u32x2 vpk = {cvt_pk(vt[0], vt[1]), cvt_pk(vt[2], vt[3])};
      if (__builtin_expect(s + 1 < NSEG, 1)) {
        bf16x8 va = __builtin_bit_cast(bf16x8, (u32x4){vpk.x, vpk.y, 0u, 0u});
        if (W7 || MP < 0) XLOADS()
        CS(1)
        if (__builtin_expect(isbad(max(mx4(m0), mx4(m1))), 0)) { do { CBAR(); m0 = LD16(rx, off_mm); m1 = LD16(rx, off_mm + 512); } while (isbad(max(mx4(m0), mx4(m1))) && --budget > 0); }
        CS(2)
        f32x4 mv = __builtin_bit_cast(f32x4, m0) + __builtin_bit_cast(f32x4, m1);
        float mm = fmaxf(fmax3(mv[0], mv[1], mv[2]), mv[3]);
        mm = x16_max(mm);
        float em[4];
#pragma unroll
        for (int r = 0; r < 4; ++r) em[r] = __builtin_amdgcn_exp2f(mv[r] - mm);
        unsigned pm0 = cvt_pk(em[0], em[1]), pm1 = cvt_pk(em[2], em[3]);
        bf16x8 pm = __builtin_bit_cast(bf16x8, (u32x4){own_m ? pm0 : 0u, own_m ? pm1 : 0u, 0u, 0u});
        f32x4 am = MFMA16(va, pm, ((f32x4){0, 0, 0, 0}));
        float summ = x16_sum((em[0] + em[1]) + (em[2] + em[3]));
        asm volatile("" : "+v"(am), "+v"(summ), "+v"(mm));
        SBAR();
        CS(3)
        if (__builtin_expect(isbad(max(umax3(axu.x, axu.y, ms.x), ms.y)), 0)) { do { __builtin_amdgcn_s_sleep(1); CBAR(); axu = LD8(rx, off_ax); ms = LD8(rx, off_ms); } while (isbad(max(umax3(axu.x, axu.y, ms.x), ms.y)) && --budget > 0); }
        CS(4)
        off_mm += XCH_B; off_ms += XCH_B; off_ax += XCH_B;
        const float mx = u2f(ms.x), sumx = u2f(ms.y);
        const float mt = fmaxf(mx, mm);
        const float fx = __builtin_amdgcn_exp2f(mx - mt), fm = __builtin_amdgcn_exp2f(mm - mt);
        const float rinv = __builtin_amdgcn_rcpf(fx * sumx + fm * summ);
        const float cx = fx * rinv, cm = fm * rinv;
        const f32x4 axv = {blo(axu.x), bhi(axu.x), blo(axu.y), bhi(axu.y)};
        f32x4 r4;
#pragma unroll
        for (int r = 0; r < 4; ++r) { float t = axv[r] * cx + am[r] * cm; r4[r] = t + dpp_f<0x128>(t); }
        *(LAS f32x4*)(lds + L_P + w * P_STR + n16 * 64 + g * 16) = r4;
        CS(5)
        __builtin_amdgcn_raw_buffer_store_b64(vpk, rvtm, st_vtm, 0, 16);
        wg_barrier();
        CS(6)
        if (w == 0) {
          LAS char* b = lds + L_P + (l & 1) * 4 * P_STR + (l >> 3) * 64 + gq * 16;
          f32x4 t0 = *(LAS f32x4*)b, t1 = *(LAS f32x4*)(b + P_STR), t2 = *(LAS f32x4*)(b + 2 * P_STR), t3 = *(LAS f32x4*)(b + 3 * P_STR);
          f32x4 t = (t0 + t1) + (t2 + t3);
#pragma unroll
          for (int r = 0; r < 4; ++r) t[r] += dpp_f<0xB1>(t[r]);
          unsigned p0 = min(cvt_pk(t[0], t[1]), 0xfffffffeu), p1 = min(cvt_pk(t[2], t[3]), 0xfffffffeu);
          unsigned q0 = dpp_u<0x4E>(p0), q1 = dpp_u<0x4E>(p1);
          __builtin_amdgcn_raw_buffer_store_b128((u32x4){p0, p1, q0, q1}, rmem, st_mem, 0, 16);
          st_mem += inc_mem;
          CS(7)
        }
      } else {
        if (!W7 && MP >= 0) asm volatile("" :: "v"(m0), "v"(m1), "v"(axu), "v"(ms));
        __builtin_amdgcn_raw_buffer_store_b64(vpk, rvtm, st_vtm, 0, 16);
      }
      st_vtm += KVM_B;
    } else {
      wg_barrier();
    }
  }
  drain_barrier();
  if (W7 && l == 0) flag_add(flag_ptr(p, F_CM, NSEG - 1));
}

constexpr int H_P = 0, HP_STR = 1056, H_E = 7 * HP_STR + 32;
DI void helper_wave(const Params& p, const int hid, LAS char* lds, const int w, const int l) {
  const int n16 = l & 15, g = l >> 4, hb = n16 >> 3, i8 = n16 & 7;
  const int h = hid >> 1, hf = hid & 1;
  const __amdgpu_buffer_rsrc_t rmem = __builtin_amdgcn_make_buffer_rsrc(p.ws + WS_MEMIMG, 0, (NSEG + 1) * MEMIMG_B, 0x00020000);
  const __amdgpu_buffer_rsrc_t rx = __builtin_amdgcn_make_buffer_rsrc(p.ws + WS_XCH, 0, NSEG * XCH_B, 0x00020000);
  const __amdgpu_buffer_rsrc_t rk = ws_rsrc(p, WS_KT, (size_t)SEQ * NKT * 2), ru = ws_rsrc(p, WS_UT, (size_t)SEQ * NKT * 2);
  const int wk = w < 7 ? w : 0;
  unsigned off_mem = (wk + 7 * hb) * 512 + g * 128 + i8 * 16;
  const int o0 = hf * 14 + 2 * wk;
  const unsigned kp = (unsigned)((h * 28 + wk) * 64 + l) * 16, up = (unsigned)((h * 28 + o0) * 64 + l) * 16;
  unsigned st_ax = n16 < 8 ? XA_OFF + (o0 * 8 + (h >> 1)) * 512 + (h & 1) * 256 + g * 64 + n16 * 8 : OOB;
  const unsigned inc_ax = n16 < 8 ? XCH_B : 0;
  unsigned st_ms = (l < 8 && hf == 0) ? XS_OFF + (h >> 1) * 128 + ((h & 1) * 8 + l) * 8 : OOB;
  const unsigned inc_ms = (l < 8 && hf == 0) ? XCH_B : 0;
  bf16x8 k00, k01, k10, k11, u[2];
  auto prefetch = [&](int s) {
    flag_wait(flag_ptr(p, F_XG, s), 56);
    const unsigned k = kp + (unsigned)s * (SEG * NKT * 2), q = up + (unsigned)s * (SEG * NKT * 2);
    k00 = LDF(rk, k); k01 = LDF(rk, k + 7 * 1024); k10 = LDF(rk, k + 14 * 1024); k11 = LDF(rk, k + 21 * 1024);
    u[0] = LDF(ru, q); u[1] = LDF(ru, q + 1024);
  };
  if (w < 7) prefetch(0);
  int budget = 1 << 20;
  unsigned tb1 = 0;
  for (int s = 0; s + 1 < NSEG; ++s) {
    if (w < 7) {
      if (s) wait_until(tb1, TH);
      CBAR();
      u32x4 v = LD16(rmem, off_mem);
      if (__builtin_expect(isbad(mx4(v)), 0)) { do { __builtin_amdgcn_s_sleep(1); CBAR(); v = LD16(rmem, off_mem); } while (isbad(mx4(v)) && --budget > 0); }
      off_mem += MEMIMG_B;
      bf16x8 f = __builtin_bit_cast(bf16x8, v);
      asm volatile("" : "+v"(u[0]), "+v"(u[1]));
      f32x4 z = {0, 0, 0, 0};
      f32x4 a0 = MFMA16(k00, f, z), a1 = MFMA16(k01, f, z), b0 = MFMA16(k10, f, z), b1 = MFMA16(k11, f, z);
      f32x4 r;
#pragma unroll
      for (int q = 0; q < 4; ++q) { float own = hb ? b1[q] : a0[q]; float oth = hb ? a1[q] : b0[q]; r[q] = own + dpp_f<0x128>(oth); }
      *(LAS f32x4*)(lds + H_P + w * HP_STR + l * 16) = r;
    }
    wg_barrier();
    tb1 = now32();
    if (w == 0 || w == 7) {
      f32x4 t[7];
#pragma unroll
      for (int k = 0; k < 7; ++k) t[k] = *(LAS f32x4*)(lds + H_P + k * HP_STR + l * 16);
      f32x4 sc = ((t[0] + t[1]) + (t[2] + t[3])) + ((t[4] + t[5]) + t[6]);
      float mx = fmaxf(fmax3(sc[0], sc[1], sc[2]), sc[3]);
      mx = fmaxf(mx, dpp_f<0x128>(mx)); mx = x32_max(mx); mx = x16_max(mx);
      float e0 = __builtin_amdgcn_exp2f(sc[0] - mx), e1 = __builtin_amdgcn_exp2f(sc[1] - mx), e2 = __builtin_amdgcn_exp2f(sc[2] - mx), e3 = __builtin_amdgcn_exp2f(sc[3] - mx);
      if (w == 0) {
        *(LAS u32x2*)(lds + H_E + (g * 16 + i8) * 16 + hb * 8) = (u32x2){cvt_pk(e0, e1), cvt_pk(e2, e3)};
        wg_barrier();
      } else {
        wg_barrier();
        float sm = (e0 + e1) + (e2 + e3);
        sm += dpp_f<0x128>(sm); sm = x32_sum(sm); sm = x16_sum(sm);
        ST8(((u32x2){min(f2u(mx), 0xfffffffeu), min(f2u(sm), 0xfffffffeu)}), rx, st_ms);
        st_ms += inc_ms;
      }
    } else {
      wg_barrier();
    }
    if (w < 7) {
      bf16x8 pe = *(LAS bf16x8*)(lds + H_E + (g * 16 + i8) * 16);
      f32x4 z = {0, 0, 0, 0};
      f32x4 d0 = MFMA16(u[0], pe, z), d1 = MFMA16(u[1], pe, z);
      u32x2 s0 = {min(cvt_pk(d0[0], d0[1]), 0xfffffffeu), min(cvt_pk(d0[2], d0[3]), 0xfffffffeu)};
      u32x2 s1 = {min(cvt_pk(d1[0], d1[1]), 0xfffffffeu), min(cvt_pk(d1[2], d1[3]), 0xfffffffeu)};
      ST8(s0, rx, st_ax);
      ST8(s1, rx, n16 < 8 ? st_ax + 4096 : OOB);
    }
    st_ax += inc_ax;
    CBAR();
    if (w < 7) prefetch(s + 1);
  }
}

DI void comb_row(const Params& p, int row, int l) {
  const bool act = l < 56;
  const __amdgpu_buffer_rsrc_t rpo = ws_rsrc(p, WS_PO, (size_t)4 * SEQ * DM * 4), rh = ws_rsrc(p, WS_H, (size_t)SEQ * DM * 4), rhn = ws_rsrc(p, WS_HN, (size_t)SEQ * DM * 2);
  float v[8];
  const unsigned off = act ? (unsigned)(row * DM + l * 8) * 4 : OOB;
  u32x4 t = act ? *(const u32x4*)(p.x + (size_t)row * DM + l * 8) : (u32x4){0, 0, 0, 0};
  f32x4 a = {0, 0, 0, 0}, b = a;
#pragma unroll
  for (int k = 0; k < 4; ++k) {
    a += __builtin_bit_cast(f32x4, LD16(rpo, act ? off + k * (SEQ * DM * 4) : OOB));
    b += __builtin_bit_cast(f32x4, LD16(rpo, act ? off + k * (SEQ * DM * 4) + 16 : OOB));
  }
#pragma unroll
  for (int j = 0; j < 2; ++j) { v[2 * j] = blo(t[j]) + a[2 * j]; v[2 * j + 1] = bhi(t[j]) + a[2 * j + 1]; v[4 + 2 * j] = blo(t[2 + j]) + b[2 * j]; v[5 + 2 * j] = bhi(t[2 + j]) + b[2 * j + 1]; }
  ST16(((u32x4){f2u(v[0]), f2u(v[1]), f2u(v[2]), f2u(v[3])}), rh, off);
  ST16(((u32x4){f2u(v[4]), f2u(v[5]), f2u(v[6]), f2u(v[7])}), rh, act ? off + 16 : OOB);
  u32x4 r = ln_row(v, act);
  ST16(r, rhn, act ? tiled_off(row, l) : OOB);
}

DI void att_wg(const Params& p, LAS char* lds, int s, int hg, int w, int l) {
  const int n16 = l & 15, g = l >> 4;
  const int hl = w >> 1, qt = w & 1, h = hg * 4 + hl;
  const __amdgpu_buffer_rsrc_t rk = ws_rsrc(p, WS_KT, (size_t)SEQ * NKT * 2), ru = ws_rsrc(p, WS_UT, (size_t)SEQ * NKT * 2);
  const __amdgpu_buffer_rsrc_t rkm = ws_rsrc(p, WS_KTM, (size_t)NSEG * KVM_B), rvm = ws_rsrc(p, WS_VTM, (size_t)NSEG * KVM_B);
  const __amdgpu_buffer_rsrc_t rpo = ws_rsrc(p, WS_PO, (size_t)4 * SEQ * DM * 4);
  const __amdgpu_buffer_rsrc_t rxf = ws_rsrc(p, WS_XF, (size_t)SEQ * DM * 2);
  bf16x8 q[14];
#pragma unroll
  for (int ks = 0; ks < 14; ++ks) q[ks] = LDF(rxf, (unsigned)(((s * 2 + qt) * 14 + ks) * 64 + l) * 16);
  flag_wait(flag_ptr(p, F_XG, s), 56);
  const unsigned ko = (unsigned)(((s * NH + h) * 28) * 64 + l) * 16;
  f32x4 s0 = {0, 0, 0, 0}, s1 = s0, sm = s0;
#pragma unroll
  for (int ks = 0; ks < 14; ++ks) s0 = MFMA16(LDF(rk, ko + ks * 1024), q[ks], s0);
  if (qt) {
#pragma unroll
    for (int ks = 0; ks < 14; ++ks) s1 = MFMA16(LDF(rk, ko + (14 + ks) * 1024), q[ks], s1);
  }
  bf16x8 uv[4][4];
  const unsigned uo = (unsigned)(((s * NH + hg * 4) * 28 + w) * 64 + l) * 16;
#pragma unroll
  for (int j = 0; j < 4; ++j)
#pragma unroll
    for (int k = 0; k < 4; ++k) uv[j][k] = LDF(ru, uo + (k * 28 + ((j < 3 || w < 4) ? j * 8 : 0)) * 1024);
  flag_wait(flag_ptr(p, F_CM, s), 32);
  DSTAMP(s == 63 && hg == 0 && w == 0 && l == 0, 3)
  const unsigned kmo = (unsigned)s * KVM_B + (unsigned)(((h >> 1) * 14) * 64 + l) * 16;
  bf16x8 vm[4];
#pragma unroll
  for (int j = 0; j < 4; ++j) {
    const unsigned vo = (unsigned)s * KVM_B + (unsigned)((2 * hg * 28 + w + ((j < 3 || w < 4) ? j * 8 : 0)) * 64 + l) * 8;
    const u32x2 a = LD8(rvm, vo), b = LD8(rvm, vo + 28 * 512);
    vm[j] = __builtin_bit_cast(bf16x8, (u32x4){a.x, a.y, b.x, b.y});
  }
#pragma unroll
  for (int ks = 0; ks < 14; ++ks) sm = MFMA16(LDF(rkm, kmo + ks * 1024), q[ks], sm);
  const float NEG = -3.0e38f;
  const bool ownm = (g >> 1) == (h & 1);
  float v[12];
#pragma unroll
  for (int r = 0; r < 4; ++r) {
    v[r] = (qt || (4 * g + r) <= n16) ? s0[r] : NEG;
    v[4 + r] = (qt && (4 * g + r) <= n16) ? s1[r] : NEG;
    v[8 + r] = ownm ? sm[r] : NEG;
  }
  float mx = NEG;
#pragma unroll
  for (int r = 0; r < 12; ++r) mx = fmaxf(mx, v[r]);
  mx = x16_max(mx); mx = x32_max(mx);
  float sum = 0.f;
#pragma unroll
  for (int r = 0; r < 12; ++r) { v[r] = v[r] > -1.0e38f ? __builtin_amdgcn_exp2f(v[r] - mx) : 0.f; sum += v[r]; }
  sum = x16_sum(sum); sum = x32_sum(sum);
  const float rinv = 1.0f / sum;
  u32x4 px = {cvt_pk(v[0] * rinv, v[1] * rinv), cvt_pk(v[2] * rinv, v[3] * rinv), cvt_pk(v[4] * rinv, v[5] * rinv), cvt_pk(v[6] * rinv, v[7] * rinv)};
  *(LAS u32x4*)(lds + (hl * 2 + qt) * 1024 + l * 16) = px;
  if (ownm) *(LAS u32x2*)(lds + 8192 + qt * 1024 + l * 16 + (hl >> 1) * 8) = (u32x2){cvt_pk(v[8] * rinv, v[9] * rinv), cvt_pk(v[10] * rinv, v[11] * rinv)};
  wg_barrier();
  bf16x8 P[2][5];
#pragma unroll
  for (int t = 0; t < 2; ++t) {
#pragma unroll
    for (int k = 0; k < 4; ++k) P[t][k] = *(LAS bf16x8*)(lds + (k * 2 + t) * 1024 + l * 16);
    P[t][4] = *(LAS bf16x8*)(lds + 8192 + t * 1024 + l * 16);
  }
  const unsigned po = (unsigned)((hg * SEQ + s * SEG + n16) * DM + 4 * g + w * 16) * 4;
#pragma unroll
  for (int j = 0; j < 4; ++j) {
    f32x4 d0 = {0, 0, 0, 0}, d1 = d0;
#pragma unroll
    for (int k = 0; k < 4; ++k) { d0 = MFMA16(uv[j][k], P[0][k], d0); d1 = MFMA16(uv[j][k], P[1][k], d1); }
    d0 = MFMA16(vm[j], P[0][4], d0); d1 = MFMA16(vm[j], P[1][4], d1);
    if (j < 3 || w < 4) { ST16(__builtin_bit_cast(u32x4, d0), rpo, po + j * 512); ST16(__builtin_bit_cast(u32x4, d1), rpo, po + j * 512 + 16 * DM * 4); }
  }
  drain_barrier();
  if (w == 0) {
    if (l == 0) flag_add(flag_ptr(p, F_AT, s));
    flag_wait(flag_ptr(p, F_AT, s), 4);
  }
  wg_barrier();
  DSTAMP(s == 63 && hg == 0 && w == 0 && l == 0, 4)
  comb_row(p, s * SEG + hg * 8 + w, l);
  drain_barrier();
  if (w == 0 && l == 0) flag_add(flag_ptr(p, F_HN, s));
}

constexpr int L_GW = 57344, G1_STR = 72, G2_STR = 40;
DI void glu1_stage(const Params& p, LAS char* lds, int fb, int tid) {
#pragma unroll
  for (int i = 0; i < 7; ++i) {
    const int c = i * 512 + tid, row = c >> 2, gu = row >= 448, d = row - 448 * gu;
    u32x4 v = *(const u32x4*)((gu ? p.wu : p.wg) + (size_t)d * FF + fb * 32 + (c & 3) * 8);
    LAS char* dst = lds + L_GW + row * G1_STR + (c & 3) * 16;
    *(LAS u32x2*)dst = (u32x2){v.x, v.y}; *(LAS u32x2*)(dst + 8) = (u32x2){v.z, v.w};
  }
}
DI void glu2_stage(const Params& p, LAS char* lds, int o, int tid) {
#pragma unroll
  for (int i = 0; i < 7; ++i) {
    const int c = i * 512 + tid, row = c >> 1;
    u32x4 v = *(const u32x4*)(p.wd + (size_t)row * DM + o * 16 + (c & 1) * 8);
    LAS char* dst = lds + L_GW + row * G2_STR + (c & 1) * 16;
    *(LAS u32x2*)dst = (u32x2){v.x, v.y}; *(LAS u32x2*)(dst + 8) = (u32x2){v.z, v.w};
  }
}
DI bf16x8 tr_frag(LAS char* b, int str) {
  s16x4 lo = __builtin_amdgcn_ds_read_tr16_b64_v4i16((LAS s16x4*)b);
  s16x4 hi = __builtin_amdgcn_ds_read_tr16_b64_v4i16((LAS s16x4*)(b + 4 * str));
  u32x2 l2 = __builtin_bit_cast(u32x2, lo), h2 = __builtin_bit_cast(u32x2, hi);
  return __builtin_bit_cast(bf16x8, (u32x4){l2.x, l2.y, h2.x, h2.y});
}
DI void glu1_wg(const Params& p, LAS char* lds, int fb, int w, int l) {
  const int ftl = w & 1, rt = (w >> 1) & 1, sp = w >> 2;
  const __amdgpu_buffer_rsrc_t rhn = ws_rsrc(p, WS_HN, (size_t)SEQ * DM * 2), rhg = ws_rsrc(p, WS_HG, (size_t)SEQ * FF * 2);
  bf16x8 wgt[14], wup[14];
#pragma unroll
  for (int ks = 0; ks < 14; ++ks) {
    LAS char* b = lds + L_GW + (32 * ks + 8 * (l >> 4) + ((l & 15) >> 2)) * G1_STR + ftl * 32 + (l & 3) * 8;
    wgt[ks] = tr_frag(b, G1_STR); wup[ks] = tr_frag(b + 448 * G1_STR, G1_STR);
  }
  const int n16 = l & 15, g = l >> 4;
  if (w == 0) *(LAS unsigned*)(lds + l * 4) = 0u;
  wg_barrier();
  for (int s = sp; s < NSEG; s += 2) {
    flag_wait(flag_ptr(p, F_HN, s), 4);
    DSTAMP(s == 63 && fb == 0 && w == 4 && l == 0, 5)
    const unsigned ho = (unsigned)(((s * 2 + rt) * 14) * 64 + l) * 16;
    bf16x8 x[14];
#pragma unroll
    for (int ks = 0; ks < 14; ++ks) x[ks] = LDF(rhn, ho + ks * 1024);
    f32x4 ag = {0, 0, 0, 0}, au = ag;
#pragma unroll
    for (int ks = 0; ks < 14; ++ks) { ag = MFMA16(wgt[ks], x[ks], ag); au = MFMA16(wup[ks], x[ks], au); }
    u32x2 o = {cvt_pk(ag[0] * au[0], ag[1] * au[1]), cvt_pk(ag[2] * au[2], ag[3] * au[3])};
    ST8(o, rhg, (unsigned)(((((s * 2 + rt) * 56 + fb) * 64 + (2 * ftl + (g >> 1)) * 16 + n16) * 8 + 4 * (g & 1)) * 2));
    asm volatile("s_waitcnt vmcnt(0)" ::: "memory");
    if (l == 0) {
      const unsigned t = __hip_atomic_fetch_add((LAS unsigned*)(lds + s * 4), 1u, __ATOMIC_RELAXED, __HIP_MEMORY_SCOPE_WORKGROUP);
      if (t == 3) flag_add(flag_ptr(p, F_G1, s));
    }
  }
}

DI void glu2_wg(const Params& p, LAS char* lds, int o, int s0, int w, int l) {
  const __amdgpu_buffer_rsrc_t rhg = ws_rsrc(p, WS_HG, (size_t)SEQ * FF * 2), rh = ws_rsrc(p, WS_H, (size_t)SEQ * DM * 4);
  bf16x8 wd[7];
#pragma unroll
  for (int k = 0; k < 7; ++k) wd[k] = tr_frag(lds + L_GW + (32 * (7 * w + k) + 8 * (l >> 4) + ((l & 15) >> 2)) * G2_STR + (l & 3) * 8, G2_STR);
  const int tid = w * 64 + l;
  int buf = 0;
  for (int s = s0; s < NSEG; s += 2) {
    if (w == 0) flag_wait(flag_ptr(p, F_G1, s), 56);
    wg_barrier();
    DSTAMP(s == 63 && o == 0 && tid == 0, 6)
    const unsigned ho = (unsigned)(((s * 2) * 56 + 7 * w) * 64 + l) * 16;
    bf16x8 a[7], b[7];
#pragma unroll
    for (int k = 0; k < 7; ++k) { a[k] = LDF(rhg, ho + k * 1024); b[k] = LDF(rhg, ho + (56 + k) * 1024); }
    const int row = s * SEG + ((tid >> 6) & 1) * 16 + (tid & 15), col = o * 16 + 4 * ((tid >> 4) & 3);
    u32x4 hv = {0, 0, 0, 0};
    if (tid < 128) hv = LD16(rh, (unsigned)(row * DM + col) * 4);
    f32x4 d0 = {0, 0, 0, 0}, d1 = d0;
#pragma unroll
    for (int k = 0; k < 7; ++k) { d0 = MFMA16(wd[k], a[k], d0); d1 = MFMA16(wd[k], b[k], d1); }
    LAS char* L = lds + buf * 16384;
    *(LAS f32x4*)(L + w * 2048 + l * 16) = d0;
    *(LAS f32x4*)(L + w * 2048 + 1024 + l * 16) = d1;
    wg_barrier();
    if (tid < 128) {
      f32x4 acc = __builtin_bit_cast(f32x4, hv);
#pragma unroll
      for (int k = 0; k < 8; ++k) acc += *(LAS f32x4*)(L + k * 2048 + tid * 16);
      *(u32x2*)(p.out + (size_t)row * DM + col) = (u32x2){cvt_pk(acc[0], acc[1]), cvt_pk(acc[2], acc[3])};
    }
    buf ^= 1;
  }
}

__global__ void __launch_bounds__(512, 2) k_main(Params p) {
  extern __shared__ __attribute__((aligned(16))) char smem[];
  LAS char* lds = (LAS char*)smem;
  const int xg = blockIdx.x & 7, r = blockIdx.x >> 3;
  const int w = __builtin_amdgcn_readfirstlane(threadIdx.x >> 6), l = threadIdx.x & 63;
  pre_phase(p, lds, blockIdx.x, w, l);
  DSTAMP(blockIdx.x == 40 && threadIdx.x == 0, 7)
  if (xg == 0) {
    const int cw = r;
    if (cw < 28) { if (w == 7) chain_wave<true, true>(p, cw, lds, w, l); else chain_wave<false, true>(p, cw, lds, w, l); }
    else { if (w == 7) chain_wave<true, false>(p, cw, lds, w, l); else chain_wave<false, false>(p, cw, lds, w, l); }
    return;
  }
  if (xg == 1) { helper_wave(p, r, lds, w, l); return; }
  const int wi = (xg - 2) * 32 + r;
  if (wi < 168) { const unsigned t0 = now32(); wait_until(t0, (wi / 56) * XSTAG); xgemm_wg(p, lds, wi % 56, wi / 56, 3, w, l); }
  if (wi < 56) glu1_stage(p, lds, wi, threadIdx.x); else if (wi < 112) glu2_stage(p, lds, (wi - 56) % 28, threadIdx.x);
  wg_barrier();
  if (wi < 56) { glu1_wg(p, lds, wi, w, l); return; }
  if (wi < 112) {
    glu2_wg(p, lds, (wi - 56) % 28, (wi - 56) / 28, w, l);
    return;
  }
  for (;;) {
    if (threadIdx.x == 0) *(LAS unsigned*)(lds + 10244) = __hip_atomic_fetch_add(flag_ptr(p, F_Q, 0), 1u, __ATOMIC_RELAXED, __HIP_MEMORY_SCOPE_AGENT);
    wg_barrier();
    const unsigned t = __builtin_amdgcn_readfirstlane(*(LAS unsigned*)(lds + 10244));
    if (t >= NSEG * 4) break;
    att_wg(p, lds, t >> 2, t & 3, w, l);
  }
}

extern "C" void kernel_launch(void* const* d_in, const int* in_sizes, int n_in,
                              void* d_out, int out_size, void* d_ws, size_t ws_size,
                              hipStream_t stream) {
  Params p;
  p.x = (const uint16_t*)d_in[0]; p.mem0 = (const uint16_t*)d_in[1];
  p.Wq = (const uint16_t*)d_in[2]; p.Wk = (const uint16_t*)d_in[3]; p.Wv = (const uint16_t*)d_in[4]; p.Wo = (const uint16_t*)d_in[5];
  p.wg = (const uint16_t*)d_in[8]; p.wu = (const uint16_t*)d_in[9]; p.wd = (const uint16_t*)d_in[10];
  p.out = (uint16_t*)d_out; p.ws = (char*)d_ws;
  (void)hipFuncSetAttribute((const void*)k_main, hipFuncAttributeMaxDynamicSharedMemorySize, CHAIN_LDS + 1024);
  (void)hipMemsetAsync(p.ws + WS_FLG, 0, WS_END - WS_FLG, stream);
  k_main<<<256, 512, CHAIN_LDS + 1024, stream>>>(p);
}
```

```cpp
#include <hip/hip_runtime.h>
#include <hip/hip_bf16.h>
#include <stdint.h>

typedef __bf16 bf16x8 __attribute__((ext_vector_type(8)));
typedef __bf16 bf16x16 __attribute__((ext_vector_type(16)));
typedef __bf16 bf16x2 __attribute__((ext_vector_type(2)));
typedef float f32x4 __attribute__((ext_vector_type(4)));
typedef unsigned u32x4 __attribute__((ext_vector_type(4)));
typedef unsigned u32x2 __attribute__((ext_vector_type(2)));
typedef unsigned u32x8 __attribute__((ext_vector_type(8)));
typedef short s16x4 __attribute__((ext_vector_type(4)));
#define LAS __attribute__((address_space(3)))
#define DI __device__ __forceinline__

constexpr int DM = 448, NH = 16, HD = 256, NSEG = 64, SEG = 32, SEQ = 2048, NTOT = 14336, NKT = 7168, FF = 1792;
constexpr int MEMIMG_B = 7168;
constexpr int XM_OFF = 0, XS_OFF = 8192, XA_OFF = 9216, XCH_B = 9216 + 28 * 4096;
constexpr int KVM_B = NH * 8 * DM * 2;

constexpr int KR = 5, XW = 1;
#define TUNE TM = 1900, MP = -1, T2 = 4300, TH = 4000, DB = 4, XSTAG = 8000, PRE0 = 3, PT = 0, RS = 0, XP = 0
constexpr int TUNE;

constexpr int L_M = 0, L_T = 7168, T_STR = 640, L_P = L_T + 8 * T_STR, P_STR = 1056, L_WB = L_P + 8 * P_STR + 64;
constexpr int L_WB_AL = (L_WB + 255) & ~255;
constexpr int CHAIN_LDS = L_WB_AL + 7 * (14 - KR) * 2048;

constexpr size_t WS_MEMIMG = 0;
constexpr size_t WS_XCH = WS_MEMIMG + (size_t)(NSEG + 1) * MEMIMG_B;
constexpr size_t WS_SENT_END = WS_XCH + (size_t)NSEG * XCH_B;
constexpr size_t WS_XF = (WS_SENT_END + 4095) & ~(size_t)4095;
constexpr size_t WS_WT = WS_XF + (size_t)SEQ * DM * 2;
constexpr size_t WS_KT = WS_WT + (size_t)NTOT * DM * 2;
constexpr size_t WS_UT = WS_KT + (size_t)SEQ * NKT * 2;
constexpr size_t WS_KTM = WS_UT + (size_t)SEQ * NKT * 2;
constexpr size_t WS_VTM = WS_KTM + (size_t)NSEG * KVM_B;
constexpr size_t WS_PO = WS_VTM + (size_t)NSEG * KVM_B;
constexpr size_t WS_H = WS_PO + (size_t)4 * SEQ * DM * 4;
constexpr size_t WS_HN = WS_H + (size_t)SEQ * DM * 4;
constexpr size_t WS_WG = WS_HN + (size_t)SEQ * DM * 2;
constexpr size_t WS_WD = WS_WG + (size_t)2 * FF * DM * 2;
constexpr size_t WS_HG = WS_WD + (size_t)FF * DM * 2;
constexpr size_t WS_FLG = WS_HG + (size_t)SEQ * FF * 2;
constexpr size_t WS_END = WS_FLG + 6 * NSEG * 128;
constexpr int F_XG = 0, F_CM = 1, F_AT = 2, F_HN = 3, F_G1 = 4, F_Q = 5;
#define DSTAMP(c, k)
#define CS(k)

struct Params {
  const uint16_t* x; const uint16_t* mem0; const uint16_t* Wq; const uint16_t* Wk; const uint16_t* Wv; const uint16_t* Wo;
  const uint16_t* wg; const uint16_t* wu; const uint16_t* wd; uint16_t* out; char* ws;
};

DI unsigned cvt_pk(float a, float b) { bf16x2 r = {(__bf16)a, (__bf16)b}; return __builtin_bit_cast(unsigned, r); }
DI void wg_barrier() { asm volatile("s_waitcnt lgkmcnt(0)\n\ts_barrier" ::: "memory"); }
DI unsigned umax3(unsigned a, unsigned b, unsigned c) { return max(max(a, b), c); }
DI float fmax3(float a, float b, float c) { return fmaxf(fmaxf(a, b), c); }
DI float u2f(unsigned u) { return __builtin_bit_cast(float, u); }
DI unsigned f2u(float f) { return __builtin_bit_cast(unsigned, f); }
DI float blo(unsigned u) { return u2f(u << 16); }
DI float bhi(unsigned u) { return u2f(u & 0xffff0000u); }
template<int CTRL> DI float dpp_f(float x) { return u2f((unsigned)__builtin_amdgcn_update_dpp(0, (int)f2u(x), CTRL, 0xf, 0xf, true)); }
template<int CTRL> DI unsigned dpp_u(unsigned x) { return (unsigned)__builtin_amdgcn_update_dpp(0, (int)x, CTRL, 0xf, 0xf, true); }
DI float x32_max(float x) { auto r = __builtin_amdgcn_permlane32_swap(f2u(x), f2u(x), false, false); return fmaxf(u2f(r[0]), u2f(r[1])); }
DI float x32_sum(float x) { auto r = __builtin_amdgcn_permlane32_swap(f2u(x), f2u(x), false, false); return u2f(r[0]) + u2f(r[1]); }
DI float x16_max(float x) { auto r = __builtin_amdgcn_permlane16_swap(f2u(x), f2u(x), false, false); return fmaxf(u2f(r[0]), u2f(r[1])); }
DI float x16_sum(float x) { auto r = __builtin_amdgcn_permlane16_swap(f2u(x), f2u(x), false, false); return u2f(r[0]) + u2f(r[1]); }
DI float wave_sum(float v) {
  v += dpp_f<0xB1>(v); v += dpp_f<0x4E>(v); v += dpp_f<0x124>(v); v += dpp_f<0x128>(v);
  v = x16_sum(v); v = x32_sum(v); return v;
}
#define SMFMAC(a, b, c, idx) __builtin_amdgcn_smfmac_f32_16x16x64_bf16((a), (b), (c), (idx), 0, 0)
#define MFMA16(a, b, c) __builtin_amdgcn_mfma_f32_16x16x32_bf16((a), (b), (c), 0, 0, 0)
constexpr unsigned OOB = 0x7fffffffu;
#define LD16(r, off) __builtin_amdgcn_raw_buffer_load_b128((r), (off), 0, 16)
#define LD4(r, off) __builtin_amdgcn_raw_buffer_load_b32((r), (off), 0, 16)
#define LD8(r, off) __builtin_amdgcn_raw_buffer_load_b64((r), (off), 0, 16)
#define CBAR() asm volatile("" ::: "memory")
#define SBAR() __builtin_amdgcn_sched_barrier(0)

DI unsigned* flag_ptr(const Params& p, int kind, int s) { return (unsigned*)(p.ws + WS_FLG + (size_t)(kind * NSEG + s) * 128); }
DI void flag_add(unsigned* f) { __hip_atomic_fetch_add(f, 1u, __ATOMIC_RELAXED, __HIP_MEMORY_SCOPE_AGENT); }
DI void flag_wait(const unsigned* f, unsigned want) {
  int budget = 1 << 20;
  while ((unsigned)__builtin_amdgcn_readfirstlane(__hip_atomic_load(f, __ATOMIC_RELAXED, __HIP_MEMORY_SCOPE_AGENT)) < want) {
    if (--budget < 0) break;
    __builtin_amdgcn_s_sleep(4);
  }
  asm volatile("" ::: "memory");
}
DI void drain_barrier() { asm volatile("s_waitcnt vmcnt(0) lgkmcnt(0)\n\ts_barrier" ::: "memory"); }
DI __amdgpu_buffer_rsrc_t ws_rsrc(const Params& p, size_t off, size_t bytes) { return __builtin_amdgcn_make_buffer_rsrc(p.ws + off, 0, (int)bytes, 0x00020000); }
#define LDF(r, off) __builtin_bit_cast(bf16x8, LD16((r), (off)))
#define ST16(v, r, off) __builtin_amdgcn_raw_buffer_store_b128((v), (r), (off), 0, 16)
#define ST8(v, r, off) __builtin_amdgcn_raw_buffer_store_b64((v), (r), (off), 0, 16)

DI u32x4 ln_row(float (&v)[8], bool act) {
  float s = 0.f;
#pragma unroll
  for (int j = 0; j < 8; ++j) s += v[j];
  s = wave_sum(act ? s : 0.f);
  const float mean = s * (1.0f / DM);
  float q = 0.f;
#pragma unroll
  for (int j = 0; j < 8; ++j) { float d = v[j] - mean; q += d * d; }
  q = wave_sum(act ? q : 0.f);
  const float rstd = 1.0f / sqrtf(q * (1.0f / DM) + 1e-5f);
  u32x4 r;
#pragma unroll
  for (int j = 0; j < 4; ++j) r[j] = cvt_pk((v[2 * j] - mean) * rstd, (v[2 * j + 1] - mean) * rstd);
  return r;
}
DI unsigned tiled_off(int row, int c8) { return (unsigned)((((row >> 4) * 14 + (c8 >> 2)) * 64 + (c8 & 3) * 16 + (row & 15)) * 16); }

constexpr int WQ_STR = 144, WP_OFF = 224 * WQ_STR, WO_STR = 240, WBUF = 336 * WQ_STR + 4608;
template<bool isB>
DI void wt_wg(const Params& p, LAS char* lds, int task, int w, int l) {
  const int n16 = l & 15, g = l >> 4, tid = w * 64 + l;
  const int mat = task >> 3, qe = (task >> 2) & 1, qn = task & 3;
  const int h = mat & 15;
  const uint16_t* Qg = (isB ? p.Wv : p.Wk) + (size_t)(qe * 224) * (NH * HD) + h * HD;
  const uint16_t* Pg = isB ? p.Wo + (size_t)h * HD * DM + qn * 112 : p.Wq + (size_t)(qn * 112) * (NH * HD) + h * HD;
  u32x4 r[4][6];
  const uint16_t* src[6]; int loff[6];
#pragma unroll
  for (int i = 0; i < 6; ++i) {
    const int c = min(i * 512 + tid, 2687), c2 = c - 1792;
    src[i] = c < 1792 ? Qg + (size_t)(c >> 3) * (NH * HD) + (c & 7) * 8
           : (isB ? Pg + (size_t)(c2 / 14) * DM + (c2 % 14) * 8 : Pg + (size_t)(c2 >> 3) * (NH * HD) + (c2 & 7) * 8);
    loff[i] = (c < 1792 || !isB) ? (c >> 3) * WQ_STR + (c & 7) * 16 : WP_OFF + (c2 / 14) * WO_STR + (c2 % 14) * 16;
  }
  const int strP = isB ? 64 * DM : 64, str3 = tid < 256 ? 64 : strP;
  const int rot = (qn + 2 * qe) & 3;
  auto gload = [&](int j) {
    const int kk = (j + rot) & 3;
#pragma unroll
    for (int i = 0; i < 6; ++i) r[j][i] = *(const u32x4*)(src[i] + kk * (i < 3 ? 64 : (i == 3 ? str3 : strP)));
  };
  gload(0);
  const int we = w >> 2, wn = w & 3;
  f32x4 acc[8][2];
#pragma unroll
  for (int a = 0; a < 8; ++a) { acc[a][0] = (f32x4){0, 0, 0, 0}; acc[a][1] = (f32x4){0, 0, 0, 0}; }
  const int qrow = 8 * (n16 >> 2) + (n16 & 3);
  gload(1); gload(2); gload(3);
  DSTAMP(blockIdx.x == 40 && tid == 0, 76)
#pragma unroll 1
  for (int it = 0; it < 2; ++it) {
    if (it) wg_barrier();
#pragma unroll
    for (int i = 0; i < 6; ++i) { *(LAS u32x4*)(lds + loff[i]) = r[0][i]; *(LAS u32x4*)(lds + WBUF + loff[i]) = r[1][i]; }
    wg_barrier();
    DSTAMP(blockIdx.x == 40 && tid == 0, 74 + it)
#pragma unroll 2
    for (int k4 = 0; k4 < 4; ++k4) {
      LAS char* L = lds + (k4 >> 1) * WBUF;
      const int k = k4 & 1;
      bf16x8 pb[2];
#pragma unroll
      for (int c = 0; c < 2; ++c) {
        const int nt = wn + 4 * c;
        if (isB) {
          LAS char* bb = L + WP_OFF + (k * 32 + 8 * g + (n16 >> 2)) * WO_STR + nt * 32 + (n16 & 3) * 8;
          s16x4 lo = __builtin_amdgcn_ds_read_tr16_b64_v4i16((LAS s16x4*)bb);
          s16x4 hi = __builtin_amdgcn_ds_read_tr16_b64_v4i16((LAS s16x4*)(bb + 4 * WO_STR));
          u32x2 l2 = __builtin_bit_cast(u32x2, lo), h2 = __builtin_bit_cast(u32x2, hi);
          pb[c] = __builtin_bit_cast(bf16x8, (u32x4){l2.x, l2.y, h2.x, h2.y});
        } else {
          pb[c] = *(LAS bf16x8*)(L + WP_OFF + (nt * 16 + n16) * WQ_STR + k * 64 + g * 16);
        }
      }
#pragma unroll
      for (int a = 0; a < 8; ++a) {
        const int ep = 4 * we + (a >> 1);
        bf16x8 qa = *(LAS bf16x8*)(L + (ep * 32 + (a & 1) * 4 + qrow) * WQ_STR + k * 64 + g * 16);
        acc[a][0] = MFMA16(qa, pb[0], acc[a][0]); acc[a][1] = MFMA16(qa, pb[1], acc[a][1]);
      }
    }
#pragma unroll
    for (int i = 0; i < 6; ++i) { r[0][i] = r[2][i]; r[1][i] = r[3][i]; }
  }
  DSTAMP(blockIdx.x == 40 && tid == 0, 77)
  const float sc = isB ? 1.0f : 0.0625f * 1.4426950408889634f;
  const __amdgpu_buffer_rsrc_t rw = ws_rsrc(p, WS_WT, (size_t)NTOT * DM * 2);
#pragma unroll
  for (int c = 0; c < 2; ++c)
#pragma unroll
    for (int m = 0; m < 4; ++m) {
      if ((c < 1 || wn < 3) && (m < 3 || we == 0)) {
        const int nt = ((isB ? NKT : 0) + h * DM + qn * 112) / 16 + wn + 4 * c, ks = qe * 7 + 4 * we + m;
        const f32x4 x = acc[2 * m][c] * sc, y = acc[2 * m + 1][c] * sc;
        ST16(((u32x4){cvt_pk(x[0], x[1]), cvt_pk(x[2], x[3]), cvt_pk(y[0], y[1]), cvt_pk(y[2], y[3])}), rw, (unsigned)((nt * 14 + ks) * 64 + l) * 16);
      }
    }
}

DI void pre_phase(const Params& p, LAS char* lds, int b, int w, int l) {
  const int tid = w * 64 + l;
  const __amdgpu_buffer_rsrc_t rs = ws_rsrc(p, 0, WS_XF + (size_t)SEQ * DM * 2);
  const bool act = l < 56;
  const int row = b * 8 + w;
  u32x4 xt = act ? *(const u32x4*)(p.x + (size_t)row * DM + l * 8) : (u32x4){0, 0, 0, 0};
  const int task = (b & 7) * 32 + (b >> 3);
  if (task >= 128) wt_wg<true>(p, lds, task, w, l); else wt_wg<false>(p, lds, task, w, l);
  {
    float v[8];
#pragma unroll
    for (int j = 0; j < 4; ++j) { v[2 * j] = blo(xt[j]); v[2 * j + 1] = bhi(xt[j]); }
    u32x4 r = ln_row(v, act);
    ST16(r, rs, act ? (unsigned)WS_XF + tiled_off(row, l) : OOB);
  }
  if (tid < 32) {
    const int i = b * 32 + tid;
    ST16(((u32x4){~0u, ~0u, ~0u, ~0u}), rs, i < XCH_B / 16 ? (unsigned)WS_XCH + i * 16 : (unsigned)MEMIMG_B + (i - XCH_B / 16) * 16);
  }
  if (b == 9 && tid < 448) {
    u32x4 t = *(const u32x4*)(p.mem0 + (tid & 7) * DM + (tid >> 3) * 8);
    t.x = min(t.x, 0xfffffffeu); t.y = min(t.y, 0xfffffffeu); t.z = min(t.z, 0xfffffffeu); t.w = min(t.w, 0xfffffffeu);
    ST16(t, rs, tid * 16);
  }
  DSTAMP(b == 40 && tid == 0, 72)
  drain_barrier();
  DSTAMP(b == 40 && tid == 0, 73)
  if (w == 0) {
    if (l == 0) {
      const unsigned t = __hip_atomic_fetch_add(flag_ptr(p, F_Q, 8 + (b & 7)), 1u, __ATOMIC_RELAXED, __HIP_MEMORY_SCOPE_AGENT);
      if (t == 31) flag_add(flag_ptr(p, F_Q, 16));
    }
    int budget = 1 << 20;
    while ((unsigned)__builtin_amdgcn_readfirstlane(__hip_atomic_load(flag_ptr(p, F_Q, 16), __ATOMIC_RELAXED, __HIP_MEMORY_SCOPE_AGENT)) < 8u) {
      if (--budget < 0) break;
      __builtin_amdgcn_s_sleep(2);
    }
  }
  wg_barrier();
}

DI void xgemm_wg(const Params& p, LAS char* lds, int nb, int seg0, int step, int w, int l) {
  const int n16 = l & 15, g = l >> 4;
  const int n0 = nb * 256 + w * 32;
  const bool isU = n0 >= NKT;
  const int r0 = isU ? n0 + n16 : n0 + 8 * (n16 >> 2) + (n16 & 3);
  const int r1 = isU ? r0 + 16 : r0 + 4;
  const __amdgpu_buffer_rsrc_t rw = ws_rsrc(p, WS_WT, (size_t)NTOT * DM * 2), rxf = ws_rsrc(p, WS_XF, (size_t)SEQ * DM * 2);
  const unsigned W0 = (unsigned)((r0 >> 4) * 14 * 64 + g * 16 + (r0 & 15)) * 16, W1 = (unsigned)((r1 >> 4) * 14 * 64 + g * 16 + (r1 & 15)) * 16;
  bf16x8 w0[14], w1[14];
#pragma unroll
  for (int ks = 0; ks < 14; ++ks) { w0[ks] = LDF(rw, W0 + ks * 1024); w1[ks] = LDF(rw, W1 + ks * 1024); }
  const int nn = isU ? n0 - NKT : n0;
  const int hh = nn / DM, cc = nn % DM;
  const int tid = w * 64 + l;
  u32x4 pre[4];
  auto gload = [&](int s) {
#pragma unroll
    for (int i = 0; i < 4; ++i) { const int c = i * 512 + tid; pre[i] = LD16(rxf, c < 1792 ? (unsigned)(s * 1792 + c) * 16 : OOB); }
  };
  gload(seg0);
  int buf = 0, prev = -1;
  const __amdgpu_buffer_rsrc_t rout = ws_rsrc(p, isU ? WS_UT : WS_KT, (size_t)SEQ * NKT * 2);
  const __amdgpu_buffer_rsrc_t rsent = ws_rsrc(p, 0, WS_SENT_END);
  const unsigned tx0 = (unsigned)__builtin_amdgcn_s_memtime();
  unsigned txd = 0;
  for (int s = seg0; s < NSEG; s += step) {
    while ((unsigned)__builtin_amdgcn_s_memtime() - tx0 < txd) __builtin_amdgcn_s_sleep(8);
    txd += XP;
    LAS char* L = lds + buf * 28672;
#pragma unroll
    for (int i = 0; i < 4; ++i) { const int c = i * 512 + tid; if (c < 1792) *(LAS u32x4*)(L + c * 16) = pre[i]; }
    asm volatile("s_waitcnt vmcnt(0)" ::: "memory");
    if (s + step < NSEG) gload(s + step);
    wg_barrier();
    if (tid == 0 && prev >= 0) flag_add(flag_ptr(p, F_XG, prev));
    f32x4 a00 = {0, 0, 0, 0}, a01 = a00, a10 = a00, a11 = a00;
#pragma unroll
    for (int ks = 0; ks < 14; ++ks) {
      bf16x8 x0 = *(LAS bf16x8*)(L + ks * 1024 + l * 16), x1 = *(LAS bf16x8*)(L + (14 + ks) * 1024 + l * 16);
      if (isU) { a00 = MFMA16(x0, w0[ks], a00); a01 = MFMA16(x1, w0[ks], a01); a10 = MFMA16(x0, w1[ks], a10); a11 = MFMA16(x1, w1[ks], a11); }
      else     { a00 = MFMA16(w0[ks], x0, a00); a01 = MFMA16(w1[ks], x0, a01); a10 = MFMA16(w0[ks], x1, a10); a11 = MFMA16(w1[ks], x1, a11); }
    }
    u32x4 o0 = {cvt_pk(a00[0], a00[1]), cvt_pk(a00[2], a00[3]), cvt_pk(a01[0], a01[1]), cvt_pk(a01[2], a01[3])};
    u32x4 o1 = {cvt_pk(a10[0], a10[1]), cvt_pk(a10[2], a10[3]), cvt_pk(a11[0], a11[1]), cvt_pk(a11[2], a11[3])};
    {
      const unsigned off = (unsigned)((((s * NH + hh) * 28 + (isU ? (cc >> 4) : (cc >> 5))) * 64 + l) * 16);
      ST16(o0, rout, off); ST16(o1, rout, off + (isU ? 1024 : 14 * 1024));
    }
    if (s + 1 < NSEG) {
      const int i = nb * 147 + tid;
      if (tid < 147 && i < (XCH_B + MEMIMG_B) / 16) {
        const unsigned off = i < XCH_B / 16 ? (unsigned)(WS_XCH + (size_t)(s + 1) * XCH_B) + i * 16 : (unsigned)((s + 2) * MEMIMG_B) + (i - XCH_B / 16) * 16;
        ST16(((u32x4){~0u, ~0u, ~0u, ~0u}), rsent, off);
      }
    }
    prev = s;
    buf ^= 1;
  }
  drain_barrier();
  if (tid == 0 && prev >= 0) flag_add(flag_ptr(p, F_XG, prev));
}
DI bf16x16 load_wfrag(__amdgpu_buffer_rsrc_t rw, unsigned base, int k) {
  u32x2 alo = LD8(rw, base + k * 1024), ahi = LD8(rw, base + k * 1024 + 512);
  u32x2 blo_ = LD8(rw, base + (k + 7) * 1024), bhi_ = LD8(rw, base + (k + 7) * 1024 + 512);
  u32x8 r = {alo.x, blo_.x, alo.y, blo_.y, ahi.x, bhi_.x, ahi.y, bhi_.y};
  return __builtin_bit_cast(bf16x16, r);
}
DI f32x4 comb32(f32x4 a, f32x4 b) {
  f32x4 r;
#pragma unroll
  for (int i = 0; i < 4; ++i) { float fa = a[i], fb = b[i]; auto t = __builtin_amdgcn_permlane32_swap(f2u(fa), f2u(fb), false, false); r[i] = u2f(t[0]) + u2f(t[1]); }
  return r;
}
DI bf16x16 lds_wfrag(LAS char* d) { u32x4 t0 = *(LAS u32x4*)d, t1 = *(LAS u32x4*)(d + 1024);
  u32x8 t = {t0.x, t0.y, t0.z, t0.w, t1.x, t1.y, t1.z, t1.w}; return __builtin_bit_cast(bf16x16, t); }
DI unsigned now32() { return (unsigned)__builtin_amdgcn_s_memtime(); }
DI void wait_until(unsigned t0, unsigned d) { while (now32() - t0 < d) { } }
DI void wait_until_asm(unsigned t0, unsigned d) {
  asm volatile("1:\n\ts_memtime vcc\n\ts_waitcnt lgkmcnt(0)\n\ts_sub_u32 vcc_lo, vcc_lo, %0\n\ts_cmp_lt_u32 vcc_lo, %1\n\ts_cbranch_scc1 1b"
               :: "s"(t0), "s"(d) : "vcc", "scc", "memory");
}
DI unsigned mx4(u32x4 v) { return max(umax3(v.x, v.y, v.z), v.w); }
DI bool isbad(unsigned m) { return __builtin_amdgcn_ballot_w64(m == 0xffffffffu) != 0; }

template<bool W7, bool OUT>
DI void chain_wave(const Params& p, const int cw, LAS char* lds, const int w, const int l) {
  constexpr int KRW = W7 ? 14 : KR;
  constexpr int NL = 14 - KRW;
  constexpr int PRE = W7 ? 0 : PRE0;
  const int n16 = l & 15, g = l >> 4, tid = w * 64 + l;
  const int h = cw >> 1, c = cw & 1, o = cw;
  const int idx = n16 < 8 ? 0x4444 : 0xEEEE;
  const __amdgpu_buffer_rsrc_t rw = ws_rsrc(p, WS_WT, (size_t)NTOT * DM * 2);
  const unsigned WX = ((g >> 1) * 16 + n16) * 16 + (g & 1) * 8;
  const __amdgpu_buffer_rsrc_t rmem = __builtin_amdgcn_make_buffer_rsrc(p.ws + WS_MEMIMG, 0, (NSEG + 1) * MEMIMG_B, 0x00020000);
  const __amdgpu_buffer_rsrc_t rx = __builtin_amdgcn_make_buffer_rsrc(p.ws + WS_XCH, 0, NSEG * XCH_B, 0x00020000);
  const __amdgpu_buffer_rsrc_t rktm = __builtin_amdgcn_make_buffer_rsrc(p.ws + WS_KTM, 0, NSEG * KVM_B, 0x00020000);
  const __amdgpu_buffer_rsrc_t rvtm = __builtin_amdgcn_make_buffer_rsrc(p.ws + WS_VTM, 0, NSEG * KVM_B, 0x00020000);
  bf16x16 wa[W7 ? 1 : 14];
  bf16x16 wb[OUT ? KRW : 1];
  if (!W7) {
    const unsigned ra = WX + (unsigned)(h * 28 + 14 * c + 2 * w) * 14336;
#pragma unroll
    for (int u = 0; u < 14; ++u) wa[u] = load_wfrag(rw, ra + (u & 1) * 14336, u >> 1);
  }
  LAS char* wl = lds + L_WB_AL + w * (14 - KR) * 2048 + l * 16;
  if (OUT) {
    const unsigned ra = WX + (unsigned)(448 + (2 * w) * 28 + o) * 14336;
#pragma unroll
    for (int u = 0; u < 14; ++u) {
      bf16x16 f = load_wfrag(rw, ra + (u & 1) * 28 * 14336, u >> 1);
      if (u < KRW) wb[u] = f;
      else { u32x8 t = __builtin_bit_cast(u32x8, f); LAS char* d = wl + (u - KRW) * 2048;
        *(LAS u32x4*)d = (u32x4){t[0], t[1], t[2], t[3]}; *(LAS u32x4*)(d + 1024) = (u32x4){t[4], t[5], t[6], t[7]}; }
    }
  }
  if (W7) { if (l < T_STR / 16) *(LAS u32x4*)(lds + L_T + 7 * T_STR + l * 16) = (u32x4){0, 0, 0, 0}; }
  unsigned off_mem = tid * 16;
  const int hb = n16 >> 3, i8 = n16 & 7;
  unsigned off_mm = XM_OFF + w * 1024 + (hb * 16 + (g & 1) * 8 + i8) * 16;
  unsigned off_ms = XS_OFF + w * 128 + n16 * 8;
  unsigned off_ax = XA_OFF + (o * 8 + w) * 512 + hb * 256 + g * 64 + i8 * 8;
  const bool own_m = hb == (g >> 1);
  const bool st_m = n16 < 8 && g < 2;
  unsigned st_mm = st_m ? XM_OFF + (h >> 1) * 1024 + c * 512 + ((h & 1) * 16 + g * 8 + i8) * 16 : OOB;
  const unsigned inc_mm = st_m ? XCH_B : 0;
  const int gq = (l >> 1) & 3;
  unsigned st_mem = ((l & 3) == 0) ? MEMIMG_B + o * 256 + (gq >> 1) * 128 + (l >> 3) * 16 : OOB;
  const unsigned inc_mem = ((l & 3) == 0) ? MEMIMG_B : 0;
  unsigned st_vtm = ((w * 28 + o) * 64 + l) * 8;
  unsigned st_ktm = ((((h >> 1) * 14 + 7 * c + (g >> 1)) * 4 + 2 * hb + (g & 1)) * 16 + (h & 1) * 8 + i8) * 16;
  LAS char* a_rd = lds + L_M + hb * 7 * 512 + g * 128 + i8 * 16;
  int budget = 1 << 20;
  unsigned tb1 = 0;
  wg_barrier();
  for (int s = 0; s < NSEG; ++s) {
    bf16x16 tw[NL > 0 ? NL : 1];
    if (OUT) {
#pragma unroll
      for (int j = 0; j < PRE; ++j) tw[j] = lds_wfrag(wl + j * 2048);
      SBAR();
    }
    if (!W7) {
      if (s) wait_until(tb1, T2);
      CBAR();
      u32x4 v = LD16(rmem, off_mem);
      if (__builtin_expect(isbad(mx4(v)), 0)) { do { if (RS) __builtin_amdgcn_s_sleep(RS); CBAR(); v = LD16(rmem, off_mem); } while (isbad(mx4(v)) && --budget > 0); }
      CS(9)
      *(LAS u32x4*)(lds + L_M + tid * 16) = v;
      off_mem += MEMIMG_B;
    }
    if (W7) asm volatile("s_waitcnt vmcnt(0)" ::: "memory");
    wg_barrier();
    tb1 = now32();
    if (W7 && l == 0 && s > 0) flag_add(flag_ptr(p, F_CM, s - 1));
    unsigned pt = 0;
    if (PT && !W7) pt = LD4(rmem, off_mem);
    bf16x8 af[7];
#pragma unroll
    for (int k = 0; k < 7; ++k) af[k] = *(LAS bf16x8*)(a_rd + k * 512);
    f32x4 vt1 = {0, 0, 0, 0}, vt2 = {0, 0, 0, 0};
    if (!W7) {
      f32x4 acc1 = {0, 0, 0, 0}, acc2 = {0, 0, 0, 0};
#pragma unroll
      for (int k = 0; k < 7; ++k) { acc1 = SMFMAC(af[k], wa[2 * k], acc1, idx); acc2 = SMFMAC(af[k], wa[2 * k + 1], acc2, idx); }
      f32x4 acc = comb32(acc1, acc2);
      u32x2 pk = {cvt_pk(acc[0], acc[1]), cvt_pk(acc[2], acc[3])};
      *(LAS u32x2*)(lds + L_T + w * T_STR + n16 * 40 + g * 8) = pk;
      wg_barrier();
      CS(0)
    } else {
      bf16x8 mb[4];
#pragma unroll
      for (int q = 0; q < 4; ++q) { int ks = min(7 * c + 2 * q + (g >> 1), 13); mb[q] = *(LAS bf16x8*)(lds + L_M + ks * 512 + (2 * hb + (g & 1)) * 128 + i8 * 16); }
      if (OUT) {
#pragma unroll
        for (int k = 0; k < 7; ++k) { vt1 = SMFMAC(af[k], wb[2 * k], vt1, idx); vt2 = SMFMAC(af[k], wb[2 * k + 1], vt2, idx); }
      }
      asm volatile("" : "+v"(vt1), "+v"(vt2));
      SBAR();
      __builtin_amdgcn_s_setprio(3);
      wg_barrier();
      f32x4 d2 = {0, 0, 0, 0}, d3 = {0, 0, 0, 0};
      bf16x8 kt[4];
#pragma unroll
      for (int q = 0; q < 4; ++q) {
        LAS char* b = lds + L_T + (2 * q + (g >> 1)) * T_STR + (8 * (g & 1) + (n16 >> 2)) * 40 + (n16 & 3) * 8;
        s16x4 lo = __builtin_amdgcn_ds_read_tr16_b64_v4i16((LAS s16x4*)b);
        s16x4 hi = __builtin_amdgcn_ds_read_tr16_b64_v4i16((LAS s16x4*)(b + 160));
        u32x2 l2 = __builtin_bit_cast(u32x2, lo), h2 = __builtin_bit_cast(u32x2, hi);
        kt[q] = __builtin_bit_cast(bf16x8, (u32x4){l2.x, l2.y, h2.x, h2.y});
      }
      d2 = MFMA16(kt[0], mb[0], d2); d3 = MFMA16(kt[1], mb[1], d3); d2 = MFMA16(kt[2], mb[2], d2); d3 = MFMA16(kt[3], mb[3], d3);
      u32x4 s2;
#pragma unroll
      for (int r = 0; r < 4; ++r) { float f = d2[r] + d3[r]; auto t = __builtin_amdgcn_permlane32_swap(f2u(f), f2u(f), false, false);
        s2[r] = min(f2u(f + dpp_f<0x128>(u2f(t[1]))), 0xfffffffeu); }
      __builtin_amdgcn_raw_buffer_store_b128(s2, rx, st_mm, 0, 16);
      CS(0)
      __builtin_amdgcn_s_setprio(0);
      CBAR();
#pragma unroll
      for (int q = 0; q < 4; ++q) __builtin_amdgcn_raw_buffer_store_b128(__builtin_bit_cast(u32x4, kt[q]), rktm, (q == 3 && g >= 2) ? OOB : st_ktm + q * 2048, 0, 16);
      st_mm += inc_mm; st_ktm += KVM_B;
    }
    if (OUT) {
      u32x4 m0, m1; u32x2 axu, ms;
#define XLOADS() { SBAR(); wait_until_asm(tb1, TM); CBAR(); m0 = LD16(rx, off_mm); m1 = LD16(rx, off_mm + 512); axu = LD8(rx, off_ax); ms = LD8(rx, off_ms); SBAR(); }
      if (!W7) {
        __builtin_amdgcn_s_sleep(DB);
#pragma unroll
        for (int u = 0; u < 14; ++u) {
          if (u < KRW) { if (u & 1) vt2 = SMFMAC(af[u >> 1], wb[u], vt2, idx); else vt1 = SMFMAC(af[u >> 1], wb[u], vt1, idx);
            if (u >= KRW - XW && PRE + (u - (KRW - XW)) < NL) tw[PRE + (u - (KRW - XW))] = lds_wfrag(wl + (PRE + (u - (KRW - XW))) * 2048); }
          else { if (u & 1) vt2 = SMFMAC(af[u >> 1], tw[u - KRW], vt2, idx); else vt1 = SMFMAC(af[u >> 1], tw[u - KRW], vt1, idx);
            if (u - KRW + PRE + XW < NL) tw[u - KRW + PRE + XW] = lds_wfrag(wl + (u - KRW + PRE + XW) * 2048); }
          SBAR();
          if (u == MP) XLOADS()
        }
      }
      if (PT && !W7) asm volatile("" :: "v"(pt));
      f32x4 vt = comb32(vt1, vt2);
      u32x2 vpk = {cvt_pk(vt[0], vt[1]), cvt_pk(vt[2], vt[3])};
      if (__builtin_expect(s + 1 < NSEG, 1)) {
        bf16x8 va = __builtin_bit_cast(bf16x8, (u32x4){vpk.x, vpk.y, 0u, 0u});
        if (W7 || MP < 0) XLOADS()
        CS(1)
        if (__builtin_expect(isbad(max(mx4(m0), mx4(m1))), 0)) { do { if (RS) __builtin_amdgcn_s_sleep(RS); CBAR(); m0 = LD16(rx, off_mm); m1 = LD16(rx, off_mm + 512); } while (isbad(max(mx4(m0), mx4(m1))) && --budget > 0); }
        CS(2)
        f32x4 mv = __builtin_bit_cast(f32x4, m0) + __builtin_bit_cast(f32x4, m1);
        float mm = fmaxf(fmax3(mv[0], mv[1], mv[2]), mv[3]);
        mm = x16_max(mm);
        float em[4];
#pragma unroll
        for (int r = 0; r < 4; ++r) em[r] = __builtin_amdgcn_exp2f(mv[r] - mm);
        unsigned pm0 = cvt_pk(em[0], em[1]), pm1 = cvt_pk(em[2], em[3]);
        bf16x8 pm = __builtin_bit_cast(bf16x8, (u32x4){own_m ? pm0 : 0u, own_m ? pm1 : 0u, 0u, 0u});
        f32x4 am = MFMA16(va, pm, ((f32x4){0, 0, 0, 0}));
        float summ = x16_sum((em[0] + em[1]) + (em[2] + em[3]));
        asm volatile("" : "+v"(am), "+v"(summ), "+v"(mm));
        SBAR();
        CS(3)
        if (__builtin_expect(isbad(max(umax3(axu.x, axu.y, ms.x), ms.y)), 0)) { do { __builtin_amdgcn_s_sleep(1); CBAR(); axu = LD8(rx, off_ax); ms = LD8(rx, off_ms); } while (isbad(max(umax3(axu.x, axu.y, ms.x), ms.y)) && --budget > 0); }
        CS(4)
        off_mm += XCH_B; off_ms += XCH_B; off_ax += XCH_B;
        const float mx = u2f(ms.x), sumx = u2f(ms.y);
        const float mt = fmaxf(mx, mm);
        const float fx = __builtin_amdgcn_exp2f(mx - mt), fm = __builtin_amdgcn_exp2f(mm - mt);
        const float rinv = __builtin_amdgcn_rcpf(fx * sumx + fm * summ);
        const float cx = fx * rinv, cm = fm * rinv;
        const f32x4 axv = {blo(axu.x), bhi(axu.x), blo(axu.y), bhi(axu.y)};
        f32x4 r4;
#pragma unroll
        for (int r = 0; r < 4; ++r) { float t = axv[r] * cx + am[r] * cm; r4[r] = t + dpp_f<0x128>(t); }
        *(LAS f32x4*)(lds + L_P + w * P_STR + n16 * 64 + g * 16) = r4;
        CS(5)
        __builtin_amdgcn_raw_buffer_store_b64(vpk, rvtm, st_vtm, 0, 16);
        wg_barrier();
        CS(6)
        if (w == 0) {
          LAS char* b = lds + L_P + (l & 1) * 4 * P_STR + (l >> 3) * 64 + gq * 16;
          f32x4 t0 = *(LAS f32x4*)b, t1 = *(LAS f32x4*)(b + P_STR), t2 = *(LAS f32x4*)(b + 2 * P_STR), t3 = *(LAS f32x4*)(b + 3 * P_STR);
          f32x4 t = (t0 + t1) + (t2 + t3);
#pragma unroll
          for (int r = 0; r < 4; ++r) t[r] += dpp_f<0xB1>(t[r]);
          unsigned p0 = min(cvt_pk(t[0], t[1]), 0xfffffffeu), p1 = min(cvt_pk(t[2], t[3]), 0xfffffffeu);
          unsigned q0 = dpp_u<0x4E>(p0), q1 = dpp_u<0x4E>(p1);
          __builtin_amdgcn_raw_buffer_store_b128((u32x4){p0, p1, q0, q1}, rmem, st_mem, 0, 16);
          st_mem += inc_mem;
          CS(7)
        }
        if (PT) { CBAR(); unsigned a = LD4(rx, off_mm), b = LD4(rx, off_mm + 512), c2 = LD4(rx, off_ax), d = LD4(rx, off_ms); asm volatile("" :: "v"(a), "v"(b), "v"(c2), "v"(d)); }
      } else {
        if (!W7 && MP >= 0) asm volatile("" :: "v"(m0), "v"(m1), "v"(axu), "v"(ms));
        __builtin_amdgcn_raw_buffer_store_b64(vpk, rvtm, st_vtm, 0, 16);
      }
      st_vtm += KVM_B;
    } else {
      if (PT && !W7) asm volatile("" :: "v"(pt));
      wg_barrier();
    }
  }
  drain_barrier();
  if (W7 && l == 0) flag_add(flag_ptr(p, F_CM, NSEG - 1));
}

constexpr int H_P = 0, HP_STR = 1056, H_E = 7 * HP_STR + 32;
DI void helper_wave(const Params& p, const int hid, LAS char* lds, const int w, const int l) {
  const int n16 = l & 15, g = l >> 4, hb = n16 >> 3, i8 = n16 & 7;
  const int h = hid >> 1, hf = hid & 1;
  const __amdgpu_buffer_rsrc_t rmem = __builtin_amdgcn_make_buffer_rsrc(p.ws + WS_MEMIMG, 0, (NSEG + 1) * MEMIMG_B, 0x00020000);
  const __amdgpu_buffer_rsrc_t rx = __builtin_amdgcn_make_buffer_rsrc(p.ws + WS_XCH, 0, NSEG * XCH_B, 0x00020000);
  const __amdgpu_buffer_rsrc_t rk = ws_rsrc(p, WS_KT, (size_t)SEQ * NKT * 2), ru = ws_rsrc(p, WS_UT, (size_t)SEQ * NKT * 2);
  const int wk = w < 7 ? w : 0;
  unsigned off_mem = (wk + 7 * hb) * 512 + g * 128 + i8 * 16;
  const int o0 = hf * 14 + 2 * wk;
  const unsigned kp = (unsigned)((h * 28 + wk) * 64 + l) * 16, up = (unsigned)((h * 28 + o0) * 64 + l) * 16;
  unsigned st_ax = n16 < 8 ? XA_OFF + (o0 * 8 + (h >> 1)) * 512 + (h & 1) * 256 + g * 64 + n16 * 8 : OOB;
  const unsigned inc_ax = n16 < 8 ? XCH_B : 0;
  unsigned st_ms = (l < 8 && hf == 0) ? XS_OFF + (h >> 1) * 128 + ((h & 1) * 8 + l) * 8 : OOB;
  const unsigned inc_ms = (l < 8 && hf == 0) ? XCH_B : 0;
  bf16x8 k00, k01, k10, k11, u[2];
  auto prefetch = [&](int s) {
    flag_wait(flag_ptr(p, F_XG, s), 56);
    const unsigned k = kp + (unsigned)s * (SEG * NKT * 2), q = up + (unsigned)s * (SEG * NKT * 2);
    k00 = LDF(rk, k); k01 = LDF(rk, k + 7 * 1024); k10 = LDF(rk, k + 14 * 1024); k11 = LDF(rk, k + 21 * 1024);
    u[0] = LDF(ru, q); u[1] = LDF(ru, q + 1024);
  };
  if (w < 7) prefetch(0);
  int budget = 1 << 20;
  unsigned tb1 = 0;
  for (int s = 0; s + 1 < NSEG; ++s) {
    if (w < 7) {
      if (s) wait_until(tb1, TH);
      CBAR();
      u32x4 v = LD16(rmem, off_mem);
      if (__builtin_expect(isbad(mx4(v)), 0)) { do { __builtin_amdgcn_s_sleep(1); CBAR(); v = LD16(rmem, off_mem); } while (isbad(mx4(v)) && --budget > 0); }
      off_mem += MEMIMG_B;
      bf16x8 f = __builtin_bit_cast(bf16x8, v);
      asm volatile("" : "+v"(u[0]), "+v"(u[1]));
      f32x4 z = {0, 0, 0, 0};
      f32x4 a0 = MFMA16(k00, f, z), a1 = MFMA16(k01, f, z), b0 = MFMA16(k10, f, z), b1 = MFMA16(k11, f, z);
      f32x4 r;
#pragma unroll
      for (int q = 0; q < 4; ++q) { float own = hb ? b1[q] : a0[q]; float oth = hb ? a1[q] : b0[q]; r[q] = own + dpp_f<0x128>(oth); }
      *(LAS f32x4*)(lds + H_P + w * HP_STR + l * 16) = r;
    }
    wg_barrier();
    tb1 = now32();
    if (w == 0 || w == 7) {
      f32x4 t[7];
#pragma unroll
      for (int k = 0; k < 7; ++k) t[k] = *(LAS f32x4*)(lds + H_P + k * HP_STR + l * 16);
      f32x4 sc = ((t[0] + t[1]) + (t[2] + t[3])) + ((t[4] + t[5]) + t[6]);
      float mx = fmaxf(fmax3(sc[0], sc[1], sc[2]), sc[3]);
      mx = fmaxf(mx, dpp_f<0x128>(mx)); mx = x32_max(mx); mx = x16_max(mx);
      float e0 = __builtin_amdgcn_exp2f(sc[0] - mx), e1 = __builtin_amdgcn_exp2f(sc[1] - mx), e2 = __builtin_amdgcn_exp2f(sc[2] - mx), e3 = __builtin_amdgcn_exp2f(sc[3] - mx);
      if (w == 0) {
        *(LAS u32x2*)(lds + H_E + (g * 16 + i8) * 16 + hb * 8) = (u32x2){cvt_pk(e0, e1), cvt_pk(e2, e3)};
        wg_barrier();
      } else {
        wg_barrier();
        float sm = (e0 + e1) + (e2 + e3);
        sm += dpp_f<0x128>(sm); sm = x32_sum(sm); sm = x16_sum(sm);
        ST8(((u32x2){min(f2u(mx), 0xfffffffeu), min(f2u(sm), 0xfffffffeu)}), rx, st_ms);
        st_ms += inc_ms;
      }
    } else {
      wg_barrier();
    }
    if (w < 7) {
      bf16x8 pe = *(LAS bf16x8*)(lds + H_E + (g * 16 + i8) * 16);
      f32x4 z = {0, 0, 0, 0};
      f32x4 d0 = MFMA16(u[0], pe, z), d1 = MFMA16(u[1], pe, z);
      u32x2 s0 = {min(cvt_pk(d0[0], d0[1]), 0xfffffffeu), min(cvt_pk(d0[2], d0[3]), 0xfffffffeu)};
      u32x2 s1 = {min(cvt_pk(d1[0], d1[1]), 0xfffffffeu), min(cvt_pk(d1[2], d1[3]), 0xfffffffeu)};
      ST8(s0, rx, st_ax);
      ST8(s1, rx, n16 < 8 ? st_ax + 4096 : OOB);
    }
    st_ax += inc_ax;
    CBAR();
    if (w < 7) prefetch(s + 1);
  }
}

constexpr int HS_STR = 452 * 4, L_HS = 12288, L_ST = L_HS + 8 * HS_STR;
DI void comb_wg(const Params& p, LAS char* lds, int s, int hg, int w, int l) {
  const int tid = w * 64 + l, rt = hg >> 1, half = hg & 1;
  const __amdgpu_buffer_rsrc_t rpo = ws_rsrc(p, WS_PO, (size_t)4 * SEQ * DM * 4), rh = ws_rsrc(p, WS_H, (size_t)SEQ * DM * 4), rhn = ws_rsrc(p, WS_HN, (size_t)SEQ * DM * 2);
  u32x4 pv[2][4]; u32x2 xv[2]; unsigned off[2];
#pragma unroll
  for (int j = 0; j < 2; ++j) {
    const int idx = j * 512 + tid, o = idx >> 5, g = (idx >> 3) & 3, i8 = idx & 7;
    const bool ok = idx < 896;
    off[j] = ok ? (unsigned)((((s * 2 + rt) * 28 + o) * 64 + g * 16 + half * 8 + i8) * 16) : OOB;
#pragma unroll
    for (int k = 0; k < 4; ++k) pv[j][k] = LD16(rpo, ok ? off[j] + k * (SEQ * DM * 4) : OOB);
    xv[j] = ok ? *(const u32x2*)(p.x + (size_t)(s * SEG + hg * 8 + i8) * DM + o * 16 + g * 4) : (u32x2){0, 0};
  }
#pragma unroll
  for (int j = 0; j < 2; ++j) {
    const int idx = j * 512 + tid, o = idx >> 5, g = (idx >> 3) & 3, i8 = idx & 7;
    f32x4 a = (__builtin_bit_cast(f32x4, pv[j][0]) + __builtin_bit_cast(f32x4, pv[j][1])) + (__builtin_bit_cast(f32x4, pv[j][2]) + __builtin_bit_cast(f32x4, pv[j][3]));
    a += (f32x4){blo(xv[j].x), bhi(xv[j].x), blo(xv[j].y), bhi(xv[j].y)};
    ST16(__builtin_bit_cast(u32x4, a), rh, off[j]);
    if (idx < 896) *(LAS f32x4*)(lds + L_HS + i8 * HS_STR + (o * 16 + g * 4) * 4) = a;
  }
  wg_barrier();
  {
    float v[7], sm = 0.f;
#pragma unroll
    for (int j = 0; j < 7; ++j) { v[j] = *(LAS float*)(lds + L_HS + w * HS_STR + (j * 64 + l) * 4); sm += v[j]; }
    const float mean = wave_sum(sm) * (1.0f / DM);
    float q = 0.f;
#pragma unroll
    for (int j = 0; j < 7; ++j) { float d = v[j] - mean; q += d * d; }
    const float rstd = 1.0f / sqrtf(wave_sum(q) * (1.0f / DM) + 1e-5f);
    if (l == 0) { *(LAS float*)(lds + L_ST + w * 8) = mean; *(LAS float*)(lds + L_ST + w * 8 + 4) = rstd; }
  }
  wg_barrier();
  if (tid < 448) {
    const int c8 = tid >> 3, i8 = tid & 7;
    const float mean = *(LAS float*)(lds + L_ST + i8 * 8), rstd = *(LAS float*)(lds + L_ST + i8 * 8 + 4);
    const f32x4 a = *(LAS f32x4*)(lds + L_HS + i8 * HS_STR + c8 * 32), b = *(LAS f32x4*)(lds + L_HS + i8 * HS_STR + c8 * 32 + 16);
    ST16(((u32x4){cvt_pk((a[0] - mean) * rstd, (a[1] - mean) * rstd), cvt_pk((a[2] - mean) * rstd, (a[3] - mean) * rstd),
                  cvt_pk((b[0] - mean) * rstd, (b[1] - mean) * rstd), cvt_pk((b[2] - mean) * rstd, (b[3] - mean) * rstd)}), rhn, tiled_off(s * SEG + hg * 8 + i8, c8));
  }
}

DI void att_wg(const Params& p, LAS char* lds, int s, int hg, int w, int l) {
  const int n16 = l & 15, g = l >> 4;
  const int hl = w >> 1, qt = w & 1, h = hg * 4 + hl;
  const __amdgpu_buffer_rsrc_t rk = ws_rsrc(p, WS_KT, (size_t)SEQ * NKT * 2), ru = ws_rsrc(p, WS_UT, (size_t)SEQ * NKT * 2);
  const __amdgpu_buffer_rsrc_t rkm = ws_rsrc(p, WS_KTM, (size_t)NSEG * KVM_B), rvm = ws_rsrc(p, WS_VTM, (size_t)NSEG * KVM_B);
  const __amdgpu_buffer_rsrc_t rpo = ws_rsrc(p, WS_PO, (size_t)4 * SEQ * DM * 4);
  const __amdgpu_buffer_rsrc_t rxf = ws_rsrc(p, WS_XF, (size_t)SEQ * DM * 2);
  bf16x8 q[14];
#pragma unroll
  for (int ks = 0; ks < 14; ++ks) q[ks] = LDF(rxf, (unsigned)(((s * 2 + qt) * 14 + ks) * 64 + l) * 16);
  flag_wait(flag_ptr(p, F_XG, s), 56);
  const unsigned ko = (unsigned)(((s * NH + h) * 28) * 64 + l) * 16;
  f32x4 s0 = {0, 0, 0, 0}, s1 = s0, sm = s0;
  {
    bf16x8 k0[14], k1[14];
#pragma unroll
    for (int ks = 0; ks < 14; ++ks) { k0[ks] = LDF(rk, ko + ks * 1024); k1[ks] = LDF(rk, qt ? ko + (14 + ks) * 1024 : OOB); }
    SBAR();
#pragma unroll
    for (int ks = 0; ks < 14; ++ks) { s0 = MFMA16(k0[ks], q[ks], s0); s1 = MFMA16(k1[ks], q[ks], s1); }
  }
  bf16x8 uv[4][4];
  const unsigned uo = (unsigned)(((s * NH + hg * 4) * 28 + w) * 64 + l) * 16;
#pragma unroll
  for (int j = 0; j < 4; ++j)
#pragma unroll
    for (int k = 0; k < 4; ++k) uv[j][k] = LDF(ru, uo + (k * 28 + ((j < 3 || w < 4) ? j * 8 : 0)) * 1024);
  flag_wait(flag_ptr(p, F_CM, s), 32);
  DSTAMP(s == 63 && hg == 0 && w == 0 && l == 0, 3)
  const unsigned kmo = (unsigned)s * KVM_B + (unsigned)(((h >> 1) * 14) * 64 + l) * 16;
  bf16x8 vm[4];
#pragma unroll
  for (int j = 0; j < 4; ++j) {
    const unsigned vo = (unsigned)s * KVM_B + (unsigned)((2 * hg * 28 + w + ((j < 3 || w < 4) ? j * 8 : 0)) * 64 + l) * 8;
    const u32x2 a = LD8(rvm, vo), b = LD8(rvm, vo + 28 * 512);
    vm[j] = __builtin_bit_cast(bf16x8, (u32x4){a.x, a.y, b.x, b.y});
  }
  {
    bf16x8 km[14];
#pragma unroll
    for (int ks = 0; ks < 14; ++ks) km[ks] = LDF(rkm, kmo + ks * 1024);
    SBAR();
#pragma unroll
    for (int ks = 0; ks < 14; ++ks) sm = MFMA16(km[ks], q[ks], sm);
  }
  asm volatile("" : "+v"(sm));
  DSTAMP(s == 63 && hg == 0 && w == 0 && l == 0, 80)
  const float NEG = -3.0e38f;
  const bool ownm = (g >> 1) == (h & 1);
  float v[12];
#pragma unroll
  for (int r = 0; r < 4; ++r) {
    v[r] = (qt || (4 * g + r) <= n16) ? s0[r] : NEG;
    v[4 + r] = (qt && (4 * g + r) <= n16) ? s1[r] : NEG;
    v[8 + r] = ownm ? sm[r] : NEG;
  }
  float mx = NEG;
#pragma unroll
  for (int r = 0; r < 12; ++r) mx = fmaxf(mx, v[r]);
  mx = x16_max(mx); mx = x32_max(mx);
  float sum = 0.f;
#pragma unroll
  for (int r = 0; r < 12; ++r) { v[r] = v[r] > -1.0e38f ? __builtin_amdgcn_exp2f(v[r] - mx) : 0.f; sum += v[r]; }
  sum = x16_sum(sum); sum = x32_sum(sum);
  const float rinv = 1.0f / sum;
  u32x4 px = {cvt_pk(v[0] * rinv, v[1] * rinv), cvt_pk(v[2] * rinv, v[3] * rinv), cvt_pk(v[4] * rinv, v[5] * rinv), cvt_pk(v[6] * rinv, v[7] * rinv)};
  *(LAS u32x4*)(lds + (hl * 2 + qt) * 1024 + l * 16) = px;
  if (ownm) *(LAS u32x2*)(lds + 8192 + qt * 1024 + l * 16 + (hl >> 1) * 8) = (u32x2){cvt_pk(v[8] * rinv, v[9] * rinv), cvt_pk(v[10] * rinv, v[11] * rinv)};
  wg_barrier();
  DSTAMP(s == 63 && hg == 0 && w == 0 && l == 0, 81)
  bf16x8 P[2][5];
#pragma unroll
  for (int t = 0; t < 2; ++t) {
#pragma unroll
    for (int k = 0; k < 4; ++k) P[t][k] = *(LAS bf16x8*)(lds + (k * 2 + t) * 1024 + l * 16);
    P[t][4] = *(LAS bf16x8*)(lds + 8192 + t * 1024 + l * 16);
  }
  const unsigned po = (unsigned)((((hg * NSEG + s) * 2) * 28 + w) * 64 + l) * 16;
#pragma unroll
  for (int j = 0; j < 4; ++j) {
    f32x4 d0 = {0, 0, 0, 0}, d1 = d0;
#pragma unroll
    for (int k = 0; k < 4; ++k) { d0 = MFMA16(uv[j][k], P[0][k], d0); d1 = MFMA16(uv[j][k], P[1][k], d1); }
    d0 = MFMA16(vm[j], P[0][4], d0); d1 = MFMA16(vm[j], P[1][4], d1);
    if (j < 3 || w < 4) { ST16(__builtin_bit_cast(u32x4, d0), rpo, po + j * 8192); ST16(__builtin_bit_cast(u32x4, d1), rpo, po + j * 8192 + 28 * 1024); }
  }
  DSTAMP(s == 63 && hg == 0 && w == 0 && l == 0, 82)
  drain_barrier();
  DSTAMP(s == 63 && hg == 0 && w == 0 && l == 0, 83)
  if (w == 0) {
    if (l == 0) flag_add(flag_ptr(p, F_AT, s));
    flag_wait(flag_ptr(p, F_AT, s), 4);
  }
  wg_barrier();
  DSTAMP(s == 63 && hg == 0 && w == 0 && l == 0, 4)
  comb_wg(p, lds, s, hg, w, l);
  drain_barrier();
  if (w == 0 && l == 0) flag_add(flag_ptr(p, F_HN, s));
}

constexpr int L_GW = 57344, G1_STR = 72, G2_STR = 40;
DI void glu1_stage(const Params& p, LAS char* lds, int fb, int tid) {
#pragma unroll
  for (int i = 0; i < 7; ++i) {
    const int c = i * 512 + tid, row = c >> 2, gu = row >= 448, d = row - 448 * gu;
    u32x4 v = *(const u32x4*)((gu ? p.wu : p.wg) + (size_t)d * FF + fb * 32 + (c & 3) * 8);
    LAS char* dst = lds + L_GW + row * G1_STR + (c & 3) * 16;
    *(LAS u32x2*)dst = (u32x2){v.x, v.y}; *(LAS u32x2*)(dst + 8) = (u32x2){v.z, v.w};
  }
}
DI void glu2_stage(const Params& p, LAS char* lds, int o, int tid) {
#pragma unroll
  for (int i = 0; i < 7; ++i) {
    const int c = i * 512 + tid, row = c >> 1;
    u32x4 v = *(const u32x4*)(p.wd + (size_t)row * DM + o * 16 + (c & 1) * 8);
    LAS char* dst = lds + L_GW + row * G2_STR + (c & 1) * 16;
    *(LAS u32x2*)dst = (u32x2){v.x, v.y}; *(LAS u32x2*)(dst + 8) = (u32x2){v.z, v.w};
  }
}
DI bf16x8 tr_frag(LAS char* b, int str) {
  s16x4 lo = __builtin_amdgcn_ds_read_tr16_b64_v4i16((LAS s16x4*)b);
  s16x4 hi = __builtin_amdgcn_ds_read_tr16_b64_v4i16((LAS s16x4*)(b + 4 * str));
  u32x2 l2 = __builtin_bit_cast(u32x2, lo), h2 = __builtin_bit_cast(u32x2, hi);
  return __builtin_bit_cast(bf16x8, (u32x4){l2.x, l2.y, h2.x, h2.y});
}
DI void glu1_wg(const Params& p, LAS char* lds, int fb, int w, int l) {
  const int ftl = w & 1, rt = (w >> 1) & 1, sp = w >> 2;
  const __amdgpu_buffer_rsrc_t rhn = ws_rsrc(p, WS_HN, (size_t)SEQ * DM * 2), rhg = ws_rsrc(p, WS_HG, (size_t)SEQ * FF * 2);
  bf16x8 wgt[14], wup[14];
#pragma unroll
  for (int ks = 0; ks < 14; ++ks) {
    LAS char* b = lds + L_GW + (32 * ks + 8 * (l >> 4) + ((l & 15) >> 2)) * G1_STR + ftl * 32 + (l & 3) * 8;
    wgt[ks] = tr_frag(b, G1_STR); wup[ks] = tr_frag(b + 448 * G1_STR, G1_STR);
  }
  const int n16 = l & 15, g = l >> 4;
  if (w == 0) *(LAS unsigned*)(lds + l * 4) = 0u;
  wg_barrier();
  for (int s = sp; s < NSEG; s += 2) {
    flag_wait(flag_ptr(p, F_HN, s), 4);
    DSTAMP(s == 63 && fb == 0 && w == 4 && l == 0, 5)
    const unsigned ho = (unsigned)(((s * 2 + rt) * 14) * 64 + l) * 16;
    bf16x8 x[14];
#pragma unroll
    for (int ks = 0; ks < 14; ++ks) x[ks] = LDF(rhn, ho + ks * 1024);
    SBAR();
    f32x4 ag = {0, 0, 0, 0}, au = ag;
#pragma unroll
    for (int ks = 0; ks < 14; ++ks) { ag = MFMA16(wgt[ks], x[ks], ag); au = MFMA16(wup[ks], x[ks], au); }
    u32x2 o = {cvt_pk(ag[0] * au[0], ag[1] * au[1]), cvt_pk(ag[2] * au[2], ag[3] * au[3])};
    ST8(o, rhg, (unsigned)(((((s * 2 + rt) * 56 + fb) * 64 + (2 * ftl + (g >> 1)) * 16 + n16) * 8 + 4 * (g & 1)) * 2));
    asm volatile("s_waitcnt vmcnt(0)" ::: "memory");
    if (l == 0) {
      const unsigned t = __hip_atomic_fetch_add((LAS unsigned*)(lds + s * 4), 1u, __ATOMIC_RELAXED, __HIP_MEMORY_SCOPE_WORKGROUP);
      if (t == 3) flag_add(flag_ptr(p, F_G1, s));
    }
  }
}

DI void glu2_wg(const Params& p, LAS char* lds, int o, int s0, int w, int l) {
  const __amdgpu_buffer_rsrc_t rhg = ws_rsrc(p, WS_HG, (size_t)SEQ * FF * 2), rh = ws_rsrc(p, WS_H, (size_t)SEQ * DM * 4);
  bf16x8 wd[7];
#pragma unroll
  for (int k = 0; k < 7; ++k) wd[k] = tr_frag(lds + L_GW + (32 * (7 * w + k) + 8 * (l >> 4) + ((l & 15) >> 2)) * G2_STR + (l & 3) * 8, G2_STR);
  const int tid = w * 64 + l;
  int buf = 0;
  for (int s = s0; s < NSEG; s += 2) {
    if (w == 0) flag_wait(flag_ptr(p, F_G1, s), 56);
    wg_barrier();
    DSTAMP(s == 63 && o == 0 && tid == 0, 6)
    const unsigned ho = (unsigned)(((s * 2) * 56 + 7 * w) * 64 + l) * 16;
    bf16x8 a[7], b[7];
#pragma unroll
    for (int k = 0; k < 7; ++k) { a[k] = LDF(rhg, ho + k * 1024); b[k] = LDF(rhg, ho + (56 + k) * 1024); }
    const int row = s * SEG + ((tid >> 6) & 1) * 16 + (tid & 15), col = o * 16 + 4 * ((tid >> 4) & 3);
    SBAR();
    u32x4 hv = {0, 0, 0, 0};
    if (tid < 128) hv = LD16(rh, (unsigned)(((s * 2 + (tid >> 6)) * 28 + o) * 64 + (tid & 63)) * 16);
    f32x4 d0 = {0, 0, 0, 0}, d1 = d0;
#pragma unroll
    for (int k = 0; k < 7; ++k) { d0 = MFMA16(wd[k], a[k], d0); d1 = MFMA16(wd[k], b[k], d1); }
    LAS char* L = lds + buf * 16384;
    *(LAS f32x4*)(L + w * 2048 + l * 16) = d0;
    *(LAS f32x4*)(L + w * 2048 + 1024 + l * 16) = d1;
    wg_barrier();
    if (tid < 128) {
      f32x4 acc = __builtin_bit_cast(f32x4, hv);
#pragma unroll
      for (int k = 0; k < 8; ++k) acc += *(LAS f32x4*)(L + k * 2048 + tid * 16);
      *(u32x2*)(p.out + (size_t)row * DM + col) = (u32x2){cvt_pk(acc[0], acc[1]), cvt_pk(acc[2], acc[3])};
    }
    buf ^= 1;
  }
}

__global__ void __launch_bounds__(512, 2) k_main(Params p) {
  extern __shared__ __attribute__((aligned(16))) char smem[];
  LAS char* lds = (LAS char*)smem;
  const int xg = blockIdx.x & 7, r = blockIdx.x >> 3;
  const int w = __builtin_amdgcn_readfirstlane(threadIdx.x >> 6), l = threadIdx.x & 63;
  pre_phase(p, lds, blockIdx.x, w, l);
  DSTAMP(blockIdx.x == 40 && threadIdx.x == 0, 7)
  if (xg == 0) {
    const int cw = r;
    if (cw < 28) { if (w == 7) chain_wave<true, true>(p, cw, lds, w, l); else chain_wave<false, true>(p, cw, lds, w, l); }
    else { if (w == 7) chain_wave<true, false>(p, cw, lds, w, l); else chain_wave<false, false>(p, cw, lds, w, l); }
    return;
  }
  if (xg == 1) { helper_wave(p, r, lds, w, l); return; }
  const int wi = (xg - 2) * 32 + r;
  if (wi < 168) { const unsigned t0 = now32(); wait_until(t0, (wi / 56) * XSTAG); xgemm_wg(p, lds, wi % 56, wi / 56, 3, w, l); }
  if (wi < 56) glu1_stage(p, lds, wi, threadIdx.x); else if (wi < 112) glu2_stage(p, lds, (wi - 56) % 28, threadIdx.x);
  wg_barrier();
  if (wi < 56) { glu1_wg(p, lds, wi, w, l); return; }
  if (wi < 112) {
    glu2_wg(p, lds, (wi - 56) % 28, (wi - 56) / 28, w, l);
    return;
  }
  for (;;) {
    if (threadIdx.x == 0) *(LAS unsigned*)(lds + 10244) = __hip_atomic_fetch_add(flag_ptr(p, F_Q, 0), 1u, __ATOMIC_RELAXED, __HIP_MEMORY_SCOPE_AGENT);
    wg_barrier();
    const unsigned t = __builtin_amdgcn_readfirstlane(*(LAS unsigned*)(lds + 10244));
    if (t >= NSEG * 4) break;
    att_wg(p, lds, t >> 2, t & 3, w, l);
  }
}

extern "C" void kernel_launch(void* const* d_in, const int* in_sizes, int n_in,
                              void* d_out, int out_size, void* d_ws, size_t ws_size,
                              hipStream_t stream) {
  Params p;
  p.x = (const uint16_t*)d_in[0]; p.mem0 = (const uint16_t*)d_in[1];
  p.Wq = (const uint16_t*)d_in[2]; p.Wk = (const uint16_t*)d_in[3]; p.Wv = (const uint16_t*)d_in[4]; p.Wo = (const uint16_t*)d_in[5];
  p.wg = (const uint16_t*)d_in[8]; p.wu = (const uint16_t*)d_in[9]; p.wd = (const uint16_t*)d_in[10];
  p.out = (uint16_t*)d_out; p.ws = (char*)d_ws;
  (void)hipFuncSetAttribute((const void*)k_main, hipFuncAttributeMaxDynamicSharedMemorySize, CHAIN_LDS + 1024);
  (void)hipMemsetAsync(p.ws + WS_FLG, 0, WS_END - WS_FLG, stream);
  k_main<<<256, 512, CHAIN_LDS + 1024, stream>>>(p);
}
```

```cpp
#include <hip/hip_runtime.h>
#include <hip/hip_bf16.h>
#include <stdint.h>

typedef __bf16 bf16x8 __attribute__((ext_vector_type(8)));
typedef __bf16 bf16x16 __attribute__((ext_vector_type(16)));
typedef __bf16 bf16x2 __attribute__((ext_vector_type(2)));
typedef float f32x4 __attribute__((ext_vector_type(4)));
typedef unsigned u32x4 __attribute__((ext_vector_type(4)));
typedef unsigned u32x2 __attribute__((ext_vector_type(2)));
typedef unsigned u32x8 __attribute__((ext_vector_type(8)));
typedef short s16x4 __attribute__((ext_vector_type(4)));
#define LAS __attribute__((address_space(3)))
#define DI __device__ __forceinline__

constexpr int DM = 448, NH = 16, HD = 256, NSEG = 64, SEG = 32, SEQ = 2048, NTOT = 14336, NKT = 7168, FF = 1792;
constexpr int MEMIMG_B = 7168;
constexpr int XM_OFF = 0, XS_OFF = 8192, XA_OFF = 9216, XCH_B = 9216 + 28 * 4096;
constexpr int KVM_B = NH * 8 * DM * 2;

constexpr int KR = 5, XW = 1;
#define TUNE TM = 1800, MP = -1, T2 = 4100, TH = 3800, DB = 4, XSTAG = 8000, PRE0 = 3, PT = 0, RS = 0, XP = 0, ADAPT = 1, VLATE = 1
constexpr int TUNE;

constexpr int L_M = 0, L_T = 7168, T_STR = 640, L_P = L_T + 8 * T_STR, P_STR = 1056, L_WB = L_P + 8 * P_STR + 64;
constexpr int L_WB_AL = (L_WB + 255) & ~255;
constexpr int CHAIN_LDS = L_WB_AL + 7 * (14 - KR) * 2048;

constexpr size_t WS_MEMIMG = 0;
constexpr size_t WS_XCH = WS_MEMIMG + (size_t)(NSEG + 1) * MEMIMG_B;
constexpr size_t WS_SENT_END = WS_XCH + (size_t)NSEG * XCH_B;
constexpr size_t WS_XF = (WS_SENT_END + 4095) & ~(size_t)4095;
constexpr size_t WS_WT = WS_XF + (size_t)SEQ * DM * 2;
constexpr size_t WS_KT = WS_WT + (size_t)NTOT * DM * 2;
constexpr size_t WS_UT = WS_KT + (size_t)SEQ * NKT * 2;
constexpr size_t WS_KTM = WS_UT + (size_t)SEQ * NKT * 2;
constexpr size_t WS_VTM = WS_KTM + (size_t)NSEG * KVM_B;
constexpr size_t WS_PO = WS_VTM + (size_t)NSEG * KVM_B;
constexpr size_t WS_H = WS_PO + (size_t)4 * SEQ * DM * 4;
constexpr size_t WS_HN = WS_H + (size_t)SEQ * DM * 4;
constexpr size_t WS_WG = WS_HN + (size_t)SEQ * DM * 2;
constexpr size_t WS_WD = WS_WG + (size_t)2 * FF * DM * 2;
constexpr size_t WS_HG = WS_WD + (size_t)FF * DM * 2;
constexpr size_t WS_FLG = WS_HG + (size_t)SEQ * FF * 2;
constexpr size_t WS_END = WS_FLG + 6 * NSEG * 128;
constexpr int F_XG = 0, F_CM = 1, F_AT = 2, F_HN = 3, F_G1 = 4, F_Q = 5;
#define DSTAMP(c, k)
#define CS(k)

struct Params {
  const uint16_t* x; const uint16_t* mem0; const uint16_t* Wq; const uint16_t* Wk; const uint16_t* Wv; const uint16_t* Wo;
  const uint16_t* wg; const uint16_t* wu; const uint16_t* wd; uint16_t* out; char* ws;
};

DI unsigned cvt_pk(float a, float b) { bf16x2 r = {(__bf16)a, (__bf16)b}; return __builtin_bit_cast(unsigned, r); }
DI void wg_barrier() { asm volatile("s_waitcnt lgkmcnt(0)\n\ts_barrier" ::: "memory"); }
DI unsigned umax3(unsigned a, unsigned b, unsigned c) { return max(max(a, b), c); }
DI float fmax3(float a, float b, float c) { return fmaxf(fmaxf(a, b), c); }
DI float u2f(unsigned u) { return __builtin_bit_cast(float, u); }
DI unsigned f2u(float f) { return __builtin_bit_cast(unsigned, f); }
DI float blo(unsigned u) { return u2f(u << 16); }
DI float bhi(unsigned u) { return u2f(u & 0xffff0000u); }
template<int CTRL> DI float dpp_f(float x) { return u2f((unsigned)__builtin_amdgcn_update_dpp(0, (int)f2u(x), CTRL, 0xf, 0xf, true)); }
template<int CTRL> DI unsigned dpp_u(unsigned x) { return (unsigned)__builtin_amdgcn_update_dpp(0, (int)x, CTRL, 0xf, 0xf, true); }
DI float x32_max(float x) { auto r = __builtin_amdgcn_permlane32_swap(f2u(x), f2u(x), false, false); return fmaxf(u2f(r[0]), u2f(r[1])); }
DI float x32_sum(float x) { auto r = __builtin_amdgcn_permlane32_swap(f2u(x), f2u(x), false, false); return u2f(r[0]) + u2f(r[1]); }
DI float x16_max(float x) { auto r = __builtin_amdgcn_permlane16_swap(f2u(x), f2u(x), false, false); return fmaxf(u2f(r[0]), u2f(r[1])); }
DI float x16_sum(float x) { auto r = __builtin_amdgcn_permlane16_swap(f2u(x), f2u(x), false, false); return u2f(r[0]) + u2f(r[1]); }
DI float wave_sum(float v) {
  v += dpp_f<0xB1>(v); v += dpp_f<0x4E>(v); v += dpp_f<0x124>(v); v += dpp_f<0x128>(v);
  v = x16_sum(v); v = x32_sum(v); return v;
}
#define SMFMAC(a, b, c, idx) __builtin_amdgcn_smfmac_f32_16x16x64_bf16((a), (b), (c), (idx), 0, 0)
#define MFMA16(a, b, c) __builtin_amdgcn_mfma_f32_16x16x32_bf16((a), (b), (c), 0, 0, 0)
constexpr unsigned OOB = 0x7fffffffu;
#define LD16(r, off) __builtin_amdgcn_raw_buffer_load_b128((r), (off), 0, 16)
#define LD4(r, off) __builtin_amdgcn_raw_buffer_load_b32((r), (off), 0, 16)
#define LD8(r, off) __builtin_amdgcn_raw_buffer_load_b64((r), (off), 0, 16)
#define CBAR() asm volatile("" ::: "memory")
#define SBAR() __builtin_amdgcn_sched_barrier(0)

DI unsigned* flag_ptr(const Params& p, int kind, int s) { return (unsigned*)(p.ws + WS_FLG + (size_t)(kind * NSEG + s) * 128); }
DI void flag_add(unsigned* f) { __hip_atomic_fetch_add(f, 1u, __ATOMIC_RELAXED, __HIP_MEMORY_SCOPE_AGENT); }
DI void flag_wait(const unsigned* f, unsigned want) {
  int budget = 1 << 20;
  while ((unsigned)__builtin_amdgcn_readfirstlane(__hip_atomic_load(f, __ATOMIC_RELAXED, __HIP_MEMORY_SCOPE_AGENT)) < want) {
    if (--budget < 0) break;
    __builtin_amdgcn_s_sleep(4);
  }
  asm volatile("" ::: "memory");
}
DI void drain_barrier() { asm volatile("s_waitcnt vmcnt(0) lgkmcnt(0)\n\ts_barrier" ::: "memory"); }
DI __amdgpu_buffer_rsrc_t ws_rsrc(const Params& p, size_t off, size_t bytes) { return __builtin_amdgcn_make_buffer_rsrc(p.ws + off, 0, (int)bytes, 0x00020000); }
#define LDF(r, off) __builtin_bit_cast(bf16x8, LD16((r), (off)))
#define ST16(v, r, off) __builtin_amdgcn_raw_buffer_store_b128((v), (r), (off), 0, 16)
#define ST8(v, r, off) __builtin_amdgcn_raw_buffer_store_b64((v), (r), (off), 0, 16)

DI u32x4 ln_row(float (&v)[8], bool act) {
  float s = 0.f;
#pragma unroll
  for (int j = 0; j < 8; ++j) s += v[j];
  s = wave_sum(act ? s : 0.f);
  const float mean = s * (1.0f / DM);
  float q = 0.f;
#pragma unroll
  for (int j = 0; j < 8; ++j) { float d = v[j] - mean; q += d * d; }
  q = wave_sum(act ? q : 0.f);
  const float rstd = 1.0f / sqrtf(q * (1.0f / DM) + 1e-5f);
  u32x4 r;
#pragma unroll
  for (int j = 0; j < 4; ++j) r[j] = cvt_pk((v[2 * j] - mean) * rstd, (v[2 * j + 1] - mean) * rstd);
  return r;
}
DI unsigned tiled_off(int row, int c8) { return (unsigned)((((row >> 4) * 14 + (c8 >> 2)) * 64 + (c8 & 3) * 16 + (row & 15)) * 16); }

constexpr int WQ_STR = 144, WP_OFF = 224 * WQ_STR, WO_STR = 240, WBUF = 336 * WQ_STR + 4608;
template<bool isB>
DI void wt_wg(const Params& p, LAS char* lds, int task, int w, int l) {
  const int n16 = l & 15, g = l >> 4, tid = w * 64 + l;
  const int mat = task >> 3, qe = (task >> 2) & 1, qn = task & 3;
  const int h = mat & 15;
  const uint16_t* Qg = (isB ? p.Wv : p.Wk) + (size_t)(qe * 224) * (NH * HD) + h * HD;
  const uint16_t* Pg = isB ? p.Wo + (size_t)h * HD * DM + qn * 112 : p.Wq + (size_t)(qn * 112) * (NH * HD) + h * HD;
  u32x4 r[4][6];
  const uint16_t* src[6]; int loff[6];
#pragma unroll
  for (int i = 0; i < 6; ++i) {
    const int c = min(i * 512 + tid, 2687), c2 = c - 1792;
    src[i] = c < 1792 ? Qg + (size_t)(c >> 3) * (NH * HD) + (c & 7) * 8
           : (isB ? Pg + (size_t)(c2 / 14) * DM + (c2 % 14) * 8 : Pg + (size_t)(c2 >> 3) * (NH * HD) + (c2 & 7) * 8);
    const int qr = c >> 3, qp = (qr & ~31) + ((qr >> 2) & 1) * 16 + ((qr >> 3) & 3) * 4 + (qr & 3);
    loff[i] = c < 1792 ? qp * WQ_STR + (c & 7) * 16 : (!isB ? qr * WQ_STR + (c & 7) * 16 : WP_OFF + (c2 / 14) * WO_STR + (c2 % 14) * 16);
  }
  const int strP = isB ? 64 * DM : 64, str3 = tid < 256 ? 64 : strP;
  const int rot = (qn + 2 * qe) & 3;
  auto gload = [&](int j) {
    const int kk = (j + rot) & 3;
#pragma unroll
    for (int i = 0; i < 6; ++i) r[j][i] = *(const u32x4*)(src[i] + kk * (i < 3 ? 64 : (i == 3 ? str3 : strP)));
  };
  gload(0);
  const int we = w >> 2, wn = w & 3;
  f32x4 acc[8][2];
#pragma unroll
  for (int a = 0; a < 8; ++a) { acc[a][0] = (f32x4){0, 0, 0, 0}; acc[a][1] = (f32x4){0, 0, 0, 0}; }
  gload(1); gload(2); gload(3);
  DSTAMP(blockIdx.x == 40 && tid == 0, 76)
#pragma unroll 1
  for (int it = 0; it < 2; ++it) {
    if (it) wg_barrier();
#pragma unroll
    for (int i = 0; i < 6; ++i) { *(LAS u32x4*)(lds + loff[i]) = r[0][i]; *(LAS u32x4*)(lds + WBUF + loff[i]) = r[1][i]; }
    wg_barrier();
    DSTAMP(blockIdx.x == 40 && tid == 0, 74 + it)
#pragma unroll 2
    for (int k4 = 0; k4 < 4; ++k4) {
      LAS char* L = lds + (k4 >> 1) * WBUF;
      const int k = k4 & 1;
      bf16x8 pb[2];
#pragma unroll
      for (int c = 0; c < 2; ++c) {
        const int nt = wn + 4 * c;
        if (isB) {
          LAS char* bb = L + WP_OFF + (k * 32 + 8 * g + (n16 >> 2)) * WO_STR + nt * 32 + (n16 & 3) * 8;
          s16x4 lo = __builtin_amdgcn_ds_read_tr16_b64_v4i16((LAS s16x4*)bb);
          s16x4 hi = __builtin_amdgcn_ds_read_tr16_b64_v4i16((LAS s16x4*)(bb + 4 * WO_STR));
          u32x2 l2 = __builtin_bit_cast(u32x2, lo), h2 = __builtin_bit_cast(u32x2, hi);
          pb[c] = __builtin_bit_cast(bf16x8, (u32x4){l2.x, l2.y, h2.x, h2.y});
        } else {
          pb[c] = *(LAS bf16x8*)(L + WP_OFF + (nt * 16 + n16) * WQ_STR + k * 64 + g * 16);
        }
      }
#pragma unroll
      for (int a = 0; a < 8; ++a) {
        const int ep = 4 * we + (a >> 1);
        bf16x8 qa = *(LAS bf16x8*)(L + (ep * 32 + (a & 1) * 16 + n16) * WQ_STR + k * 64 + g * 16);
        acc[a][0] = MFMA16(qa, pb[0], acc[a][0]); acc[a][1] = MFMA16(qa, pb[1], acc[a][1]);
      }
    }
#pragma unroll
    for (int i = 0; i < 6; ++i) { r[0][i] = r[2][i]; r[1][i] = r[3][i]; }
  }
  DSTAMP(blockIdx.x == 40 && tid == 0, 77)
  const float sc = isB ? 1.0f : 0.0625f * 1.4426950408889634f;
  const __amdgpu_buffer_rsrc_t rw = ws_rsrc(p, WS_WT, (size_t)NTOT * DM * 2);
#pragma unroll
  for (int c = 0; c < 2; ++c)
#pragma unroll
    for (int m = 0; m < 4; ++m) {
      if ((c < 1 || wn < 3) && (m < 3 || we == 0)) {
        const int nt = ((isB ? NKT : 0) + h * DM + qn * 112) / 16 + wn + 4 * c, ks = qe * 7 + 4 * we + m;
        const f32x4 x = acc[2 * m][c] * sc, y = acc[2 * m + 1][c] * sc;
        ST16(((u32x4){cvt_pk(x[0], x[1]), cvt_pk(x[2], x[3]), cvt_pk(y[0], y[1]), cvt_pk(y[2], y[3])}), rw, (unsigned)((nt * 14 + ks) * 64 + l) * 16);
      }
    }
}

DI void pre_phase(const Params& p, LAS char* lds, int b, int w, int l) {
  const int tid = w * 64 + l;
  const __amdgpu_buffer_rsrc_t rs = ws_rsrc(p, 0, WS_XF + (size_t)SEQ * DM * 2);
  const bool act = l < 56;
  const int row = b * 8 + w;
  u32x4 xt = act ? *(const u32x4*)(p.x + (size_t)row * DM + l * 8) : (u32x4){0, 0, 0, 0};
  const int task = (b & 7) * 32 + (b >> 3);
  if (task >= 128) wt_wg<true>(p, lds, task, w, l); else wt_wg<false>(p, lds, task, w, l);
  {
    float v[8];
#pragma unroll
    for (int j = 0; j < 4; ++j) { v[2 * j] = blo(xt[j]); v[2 * j + 1] = bhi(xt[j]); }
    u32x4 r = ln_row(v, act);
    ST16(r, rs, act ? (unsigned)WS_XF + tiled_off(row, l) : OOB);
  }
  if (tid < 32) {
    const int i = b * 32 + tid;
    ST16(((u32x4){~0u, ~0u, ~0u, ~0u}), rs, i < XCH_B / 16 ? (unsigned)WS_XCH + i * 16 : (unsigned)MEMIMG_B + (i - XCH_B / 16) * 16);
  }
  if (b == 9 && tid < 448) {
    u32x4 t = *(const u32x4*)(p.mem0 + (tid & 7) * DM + (tid >> 3) * 8);
    t.x = min(t.x, 0xfffffffeu); t.y = min(t.y, 0xfffffffeu); t.z = min(t.z, 0xfffffffeu); t.w = min(t.w, 0xfffffffeu);
    ST16(t, rs, tid * 16);
  }
  DSTAMP(b == 40 && tid == 0, 72)
  drain_barrier();
  DSTAMP(b == 40 && tid == 0, 73)
  if (w == 0) {
    if (l == 0) {
      const unsigned t = __hip_atomic_fetch_add(flag_ptr(p, F_Q, 8 + (b & 7)), 1u, __ATOMIC_RELAXED, __HIP_MEMORY_SCOPE_AGENT);
      if (t == 31) flag_add(flag_ptr(p, F_Q, 16));
    }
    int budget = 1 << 20;
    while ((unsigned)__builtin_amdgcn_readfirstlane(__hip_atomic_load(flag_ptr(p, F_Q, 16), __ATOMIC_RELAXED, __HIP_MEMORY_SCOPE_AGENT)) < 8u) {
      if (--budget < 0) break;
      __builtin_amdgcn_s_sleep(2);
    }
  }
  wg_barrier();
}

DI void xgemm_wg(const Params& p, LAS char* lds, int nb, int seg0, int step, int w, int l) {
  const int n16 = l & 15, g = l >> 4;
  const int n0 = nb * 256 + w * 32;
  const bool isU = n0 >= NKT;
  const int r0 = isU ? n0 + n16 : n0 + 8 * (n16 >> 2) + (n16 & 3);
  const int r1 = isU ? r0 + 16 : r0 + 4;
  const __amdgpu_buffer_rsrc_t rw = ws_rsrc(p, WS_WT, (size_t)NTOT * DM * 2), rxf = ws_rsrc(p, WS_XF, (size_t)SEQ * DM * 2);
  const unsigned W0 = (unsigned)((r0 >> 4) * 14 * 64 + g * 16 + (r0 & 15)) * 16, W1 = (unsigned)((r1 >> 4) * 14 * 64 + g * 16 + (r1 & 15)) * 16;
  bf16x8 w0[14], w1[14];
#pragma unroll
  for (int ks = 0; ks < 14; ++ks) { w0[ks] = LDF(rw, W0 + ks * 1024); w1[ks] = LDF(rw, W1 + ks * 1024); }
  const int nn = isU ? n0 - NKT : n0;
  const int hh = nn / DM, cc = nn % DM;
  const int tid = w * 64 + l;
  u32x4 pre[4];
  auto gload = [&](int s) {
#pragma unroll
    for (int i = 0; i < 4; ++i) { const int c = i * 512 + tid; pre[i] = LD16(rxf, c < 1792 ? (unsigned)(s * 1792 + c) * 16 : OOB); }
  };
  gload(seg0);
  int buf = 0, prev = -1;
  const __amdgpu_buffer_rsrc_t rout = ws_rsrc(p, isU ? WS_UT : WS_KT, (size_t)SEQ * NKT * 2);
  const __amdgpu_buffer_rsrc_t rsent = ws_rsrc(p, 0, WS_SENT_END);
  const unsigned tx0 = (unsigned)__builtin_amdgcn_s_memtime();
  unsigned txd = 0;
  for (int s = seg0; s < NSEG; s += step) {
    while ((unsigned)__builtin_amdgcn_s_memtime() - tx0 < txd) __builtin_amdgcn_s_sleep(8);
    txd += XP;
    LAS char* L = lds + buf * 28672;
#pragma unroll
    for (int i = 0; i < 4; ++i) { const int c = i * 512 + tid; if (c < 1792) *(LAS u32x4*)(L + c * 16) = pre[i]; }
    asm volatile("s_waitcnt vmcnt(0)" ::: "memory");
    if (s + step < NSEG) gload(s + step);
    wg_barrier();
    if (tid == 0 && prev >= 0) flag_add(flag_ptr(p, F_XG, prev));
    f32x4 a00 = {0, 0, 0, 0}, a01 = a00, a10 = a00, a11 = a00;
#pragma unroll
    for (int ks = 0; ks < 14; ++ks) {
      bf16x8 x0 = *(LAS bf16x8*)(L + ks * 1024 + l * 16), x1 = *(LAS bf16x8*)(L + (14 + ks) * 1024 + l * 16);
      if (isU) { a00 = MFMA16(x0, w0[ks], a00); a01 = MFMA16(x1, w0[ks], a01); a10 = MFMA16(x0, w1[ks], a10); a11 = MFMA16(x1, w1[ks], a11); }
      else     { a00 = MFMA16(w0[ks], x0, a00); a01 = MFMA16(w1[ks], x0, a01); a10 = MFMA16(w0[ks], x1, a10); a11 = MFMA16(w1[ks], x1, a11); }
    }
    u32x4 o0 = {cvt_pk(a00[0], a00[1]), cvt_pk(a00[2], a00[3]), cvt_pk(a01[0], a01[1]), cvt_pk(a01[2], a01[3])};
    u32x4 o1 = {cvt_pk(a10[0], a10[1]), cvt_pk(a10[2], a10[3]), cvt_pk(a11[0], a11[1]), cvt_pk(a11[2], a11[3])};
    {
      const unsigned off = (unsigned)((((s * NH + hh) * 28 + (isU ? (cc >> 4) : (cc >> 5))) * 64 + l) * 16);
      ST16(o0, rout, off); ST16(o1, rout, off + (isU ? 1024 : 14 * 1024));
    }
    if (s + 1 < NSEG) {
      const int i = nb * 147 + tid;
      if (tid < 147 && i < (XCH_B + MEMIMG_B) / 16) {
        const unsigned off = i < XCH_B / 16 ? (unsigned)(WS_XCH + (size_t)(s + 1) * XCH_B) + i * 16 : (unsigned)((s + 2) * MEMIMG_B) + (i - XCH_B / 16) * 16;
        ST16(((u32x4){~0u, ~0u, ~0u, ~0u}), rsent, off);
      }
    }
    prev = s;
    buf ^= 1;
  }
  drain_barrier();
  if (tid == 0 && prev >= 0) flag_add(flag_ptr(p, F_XG, prev));
}
DI bf16x16 load_wfrag(__amdgpu_buffer_rsrc_t rw, unsigned base, int k) {
  u32x2 alo = LD8(rw, base + k * 1024), ahi = LD8(rw, base + k * 1024 + 512);
  u32x2 blo_ = LD8(rw, base + (k + 7) * 1024), bhi_ = LD8(rw, base + (k + 7) * 1024 + 512);
  u32x8 r = {alo.x, blo_.x, alo.y, blo_.y, ahi.x, bhi_.x, ahi.y, bhi_.y};
  return __builtin_bit_cast(bf16x16, r);
}
DI f32x4 comb32(f32x4 a, f32x4 b) {
  f32x4 r;
#pragma unroll
  for (int i = 0; i < 4; ++i) { float fa = a[i], fb = b[i]; auto t = __builtin_amdgcn_permlane32_swap(f2u(fa), f2u(fb), false, false); r[i] = u2f(t[0]) + u2f(t[1]); }
  return r;
}
DI bf16x16 lds_wfrag(LAS char* d) { u32x4 t0 = *(LAS u32x4*)d, t1 = *(LAS u32x4*)(d + 1024);
  u32x8 t = {t0.x, t0.y, t0.z, t0.w, t1.x, t1.y, t1.z, t1.w}; return __builtin_bit_cast(bf16x16, t); }
DI unsigned now32() { return (unsigned)__builtin_amdgcn_s_memtime(); }
DI void wait_until(unsigned t0, unsigned d) { while (now32() - t0 < d) { } }
DI void wait_until_asm(unsigned t0, unsigned d) {
  asm volatile("1:\n\ts_memtime vcc\n\ts_waitcnt lgkmcnt(0)\n\ts_sub_u32 vcc_lo, vcc_lo, %0\n\ts_cmp_lt_u32 vcc_lo, %1\n\ts_cbranch_scc1 1b"
               :: "s"(t0), "s"(d) : "vcc", "scc", "memory");
}
DI unsigned mx4(u32x4 v) { return max(umax3(v.x, v.y, v.z), v.w); }
DI bool isbad(unsigned m) { return __builtin_amdgcn_ballot_w64(m == 0xffffffffu) != 0; }

template<bool W7, bool OUT>
DI void chain_wave(const Params& p, const int cw, LAS char* lds, const int w, const int l) {
  constexpr int KRW = W7 ? 14 : KR;
  constexpr int NL = 14 - KRW;
  constexpr int PRE = W7 ? 0 : PRE0;
  const int n16 = l & 15, g = l >> 4, tid = w * 64 + l;
  const int h = cw >> 1, c = cw & 1, o = cw;
  const int idx = n16 < 8 ? 0x4444 : 0xEEEE;
  const __amdgpu_buffer_rsrc_t rw = ws_rsrc(p, WS_WT, (size_t)NTOT * DM * 2);
  const unsigned WX = ((g >> 1) * 16 + n16) * 16 + (g & 1) * 8;
  const __amdgpu_buffer_rsrc_t rmem = __builtin_amdgcn_make_buffer_rsrc(p.ws + WS_MEMIMG, 0, (NSEG + 1) * MEMIMG_B, 0x00020000);
  const __amdgpu_buffer_rsrc_t rx = __builtin_amdgcn_make_buffer_rsrc(p.ws + WS_XCH, 0, NSEG * XCH_B, 0x00020000);
  const __amdgpu_buffer_rsrc_t rktm = __builtin_amdgcn_make_buffer_rsrc(p.ws + WS_KTM, 0, NSEG * KVM_B, 0x00020000);
  const __amdgpu_buffer_rsrc_t rvtm = __builtin_amdgcn_make_buffer_rsrc(p.ws + WS_VTM, 0, NSEG * KVM_B, 0x00020000);
  bf16x16 wa[W7 ? 1 : 14];
  bf16x16 wb[OUT ? KRW : 1];
  if (!W7) {
    const unsigned ra = WX + (unsigned)(h * 28 + 14 * c + 2 * w) * 14336;
#pragma unroll
    for (int u = 0; u < 14; ++u) wa[u] = load_wfrag(rw, ra + (u & 1) * 14336, u >> 1);
  }
  LAS char* wl = lds + L_WB_AL + w * (14 - KR) * 2048 + l * 16;
  if (OUT) {
    const unsigned ra = WX + (unsigned)(448 + (2 * w) * 28 + o) * 14336;
#pragma unroll
    for (int u = 0; u < 14; ++u) {
      bf16x16 f = load_wfrag(rw, ra + (u & 1) * 28 * 14336, u >> 1);
      if (u < KRW) wb[u] = f;
      else { u32x8 t = __builtin_bit_cast(u32x8, f); LAS char* d = wl + (u - KRW) * 2048;
        *(LAS u32x4*)d = (u32x4){t[0], t[1], t[2], t[3]}; *(LAS u32x4*)(d + 1024) = (u32x4){t[4], t[5], t[6], t[7]}; }
    }
  }
  if (W7) { if (l < T_STR / 16) *(LAS u32x4*)(lds + L_T + 7 * T_STR + l * 16) = (u32x4){0, 0, 0, 0}; }
  unsigned off_mem = tid * 16;
  const int hb = n16 >> 3, i8 = n16 & 7;
  unsigned off_mm = XM_OFF + w * 1024 + (hb * 16 + (g & 1) * 8 + i8) * 16;
  unsigned off_ms = XS_OFF + w * 128 + n16 * 8;
  unsigned off_ax = XA_OFF + (o * 8 + w) * 512 + hb * 256 + g * 64 + i8 * 8;
  const bool own_m = hb == (g >> 1);
  const bool st_m = n16 < 8 && g < 2;
  unsigned st_mm = st_m ? XM_OFF + (h >> 1) * 1024 + c * 512 + ((h & 1) * 16 + g * 8 + i8) * 16 : OOB;
  const unsigned inc_mm = st_m ? XCH_B : 0;
  const int gq = (l >> 1) & 3;
  unsigned st_mem = ((l & 3) == 0) ? MEMIMG_B + o * 256 + (gq >> 1) * 128 + (l >> 3) * 16 : OOB;
  const unsigned inc_mem = ((l & 3) == 0) ? MEMIMG_B : 0;
  unsigned st_vtm = ((w * 28 + o) * 64 + l) * 8;
  unsigned st_ktm = ((((h >> 1) * 14 + 7 * c + (g >> 1)) * 4 + 2 * hb + (g & 1)) * 16 + (h & 1) * 8 + i8) * 16;
  LAS char* a_rd = lds + L_M + hb * 7 * 512 + g * 128 + i8 * 16;
  int budget = 1 << 20;
  unsigned tb1 = 0, adj = 0, clean = 0;
  wg_barrier();
  for (int s = 0; s < NSEG; ++s) {
    bf16x16 tw[NL > 0 ? NL : 1];
    if (OUT) {
#pragma unroll
      for (int j = 0; j < PRE; ++j) tw[j] = lds_wfrag(wl + j * 2048);
      SBAR();
    }
    if (!W7) {
      if (s) wait_until(tb1, T2 + 2 * adj);
      CBAR();
      u32x4 v = LD16(rmem, off_mem);
      if (__builtin_expect(isbad(mx4(v)), 0)) { do { if (RS) __builtin_amdgcn_s_sleep(RS); CBAR(); v = LD16(rmem, off_mem); } while (isbad(mx4(v)) && --budget > 0); }
      CS(9)
      *(LAS u32x4*)(lds + L_M + tid * 16) = v;
      off_mem += MEMIMG_B;
    }
    if (W7) asm volatile("s_waitcnt vmcnt(0)" ::: "memory");
    wg_barrier();
    const unsigned tprev = tb1;
    tb1 = now32();
    if (W7 && l == 0 && s > 0) flag_add(flag_ptr(p, F_CM, s - 1));
    unsigned pt = 0;
    if (PT && !W7) pt = LD4(rmem, off_mem);
    bf16x8 af[7];
#pragma unroll
    for (int k = 0; k < 7; ++k) af[k] = *(LAS bf16x8*)(a_rd + k * 512);
    f32x4 vt1 = {0, 0, 0, 0}, vt2 = {0, 0, 0, 0};
    if (!W7) {
      f32x4 acc1 = {0, 0, 0, 0}, acc2 = {0, 0, 0, 0};
#pragma unroll
      for (int k = 0; k < 7; ++k) { acc1 = SMFMAC(af[k], wa[2 * k], acc1, idx); acc2 = SMFMAC(af[k], wa[2 * k + 1], acc2, idx); }
      f32x4 acc = comb32(acc1, acc2);
      u32x2 pk = {cvt_pk(acc[0], acc[1]), cvt_pk(acc[2], acc[3])};
      *(LAS u32x2*)(lds + L_T + w * T_STR + n16 * 40 + g * 8) = pk;
      wg_barrier();
      CS(0)
    } else {
      bf16x8 mb[4];
#pragma unroll
      for (int q = 0; q < 4; ++q) { int ks = min(7 * c + 2 * q + (g >> 1), 13); mb[q] = *(LAS bf16x8*)(lds + L_M + ks * 512 + (2 * hb + (g & 1)) * 128 + i8 * 16); }
      if (OUT) {
#pragma unroll
        for (int k = 0; k < 7; ++k) { vt1 = SMFMAC(af[k], wb[2 * k], vt1, idx); vt2 = SMFMAC(af[k], wb[2 * k + 1], vt2, idx); }
      }
      asm volatile("" : "+v"(vt1), "+v"(vt2));
      SBAR();
      __builtin_amdgcn_s_setprio(3);
      wg_barrier();
      f32x4 d2 = {0, 0, 0, 0}, d3 = {0, 0, 0, 0};
      bf16x8 kt[4];
#pragma unroll
      for (int q = 0; q < 4; ++q) {
        LAS char* b = lds + L_T + (2 * q + (g >> 1)) * T_STR + (8 * (g & 1) + (n16 >> 2)) * 40 + (n16 & 3) * 8;
        s16x4 lo = __builtin_amdgcn_ds_read_tr16_b64_v4i16((LAS s16x4*)b);
        s16x4 hi = __builtin_amdgcn_ds_read_tr16_b64_v4i16((LAS s16x4*)(b + 160));
        u32x2 l2 = __builtin_bit_cast(u32x2, lo), h2 = __builtin_bit_cast(u32x2, hi);
        kt[q] = __builtin_bit_cast(bf16x8, (u32x4){l2.x, l2.y, h2.x, h2.y});
      }
      d2 = MFMA16(kt[0], mb[0], d2); d3 = MFMA16(kt[1], mb[1], d3); d2 = MFMA16(kt[2], mb[2], d2); d3 = MFMA16(kt[3], mb[3], d3);
      u32x4 s2;
#pragma unroll
      for (int r = 0; r < 4; ++r) { float f = d2[r] + d3[r]; auto t = __builtin_amdgcn_permlane32_swap(f2u(f), f2u(f), false, false);
        s2[r] = min(f2u(f + dpp_f<0x128>(u2f(t[1]))), 0xfffffffeu); }
      __builtin_amdgcn_raw_buffer_store_b128(s2, rx, st_mm, 0, 16);
      CS(0)
      __builtin_amdgcn_s_setprio(0);
      CBAR();
#pragma unroll
      for (int q = 0; q < 4; ++q) __builtin_amdgcn_raw_buffer_store_b128(__builtin_bit_cast(u32x4, kt[q]), rktm, (q == 3 && g >= 2) ? OOB : st_ktm + q * 2048, 0, 16);
      st_mm += inc_mm; st_ktm += KVM_B;
    }
    if (ADAPT) {
      if (s > 1 && tb1 - tprev > (unsigned)(T2 + 1300) + 2 * adj) { adj = min(adj + 64u, 256u); clean = 0; }
      else if (++clean >= 4 && adj) { adj -= 32; clean = 0; }
    }
    if (OUT) {
      u32x4 m0, m1; u32x2 axu, ms;
#define XLOADS() { SBAR(); wait_until_asm(tb1, TM + adj); CBAR(); m0 = LD16(rx, off_mm); m1 = LD16(rx, off_mm + 512); axu = LD8(rx, off_ax); ms = LD8(rx, off_ms); SBAR(); }
      if (!W7) {
        __builtin_amdgcn_s_sleep(DB);
#pragma unroll
        for (int u = 0; u < 14; ++u) {
          if (u < KRW) { if (u & 1) vt2 = SMFMAC(af[u >> 1], wb[u], vt2, idx); else vt1 = SMFMAC(af[u >> 1], wb[u], vt1, idx);
            if (u >= KRW - XW && PRE + (u - (KRW - XW)) < NL) tw[PRE + (u - (KRW - XW))] = lds_wfrag(wl + (PRE + (u - (KRW - XW))) * 2048); }
          else { if (u & 1) vt2 = SMFMAC(af[u >> 1], tw[u - KRW], vt2, idx); else vt1 = SMFMAC(af[u >> 1], tw[u - KRW], vt1, idx);
            if (u - KRW + PRE + XW < NL) tw[u - KRW + PRE + XW] = lds_wfrag(wl + (u - KRW + PRE + XW) * 2048); }
          SBAR();
          if (u == MP) XLOADS()
        }
      }
      if (PT && !W7) asm volatile("" :: "v"(pt));
      f32x4 vt = comb32(vt1, vt2);
      u32x2 vpk = {cvt_pk(vt[0], vt[1]), cvt_pk(vt[2], vt[3])};
      if (__builtin_expect(s + 1 < NSEG, 1)) {
        bf16x8 va = __builtin_bit_cast(bf16x8, (u32x4){vpk.x, vpk.y, 0u, 0u});
        if (VLATE == 2) __builtin_amdgcn_raw_buffer_store_b64(vpk, rvtm, st_vtm, 0, 16);
        if (W7 || MP < 0) XLOADS()
        CS(1)
        if (__builtin_expect(isbad(max(mx4(m0), mx4(m1))), 0)) { do { if (RS) __builtin_amdgcn_s_sleep(RS); CBAR(); m0 = LD16(rx, off_mm); m1 = LD16(rx, off_mm + 512); } while (isbad(max(mx4(m0), mx4(m1))) && --budget > 0); }
        CS(2)
        f32x4 mv = __builtin_bit_cast(f32x4, m0) + __builtin_bit_cast(f32x4, m1);
        float mm = fmaxf(fmax3(mv[0], mv[1], mv[2]), mv[3]);
        mm = x16_max(mm);
        float em[4];
#pragma unroll
        for (int r = 0; r < 4; ++r) em[r] = __builtin_amdgcn_exp2f(mv[r] - mm);
        unsigned pm0 = cvt_pk(em[0], em[1]), pm1 = cvt_pk(em[2], em[3]);
        bf16x8 pm = __builtin_bit_cast(bf16x8, (u32x4){own_m ? pm0 : 0u, own_m ? pm1 : 0u, 0u, 0u});
        f32x4 am = MFMA16(va, pm, ((f32x4){0, 0, 0, 0}));
        float summ = x16_sum((em[0] + em[1]) + (em[2] + em[3]));
        asm volatile("" : "+v"(am), "+v"(summ), "+v"(mm));
        SBAR();
        CS(3)
        if (__builtin_expect(isbad(max(umax3(axu.x, axu.y, ms.x), ms.y)), 0)) { do { __builtin_amdgcn_s_sleep(1); CBAR(); axu = LD8(rx, off_ax); ms = LD8(rx, off_ms); } while (isbad(max(umax3(axu.x, axu.y, ms.x), ms.y)) && --budget > 0); }
        CS(4)
        off_mm += XCH_B; off_ms += XCH_B; off_ax += XCH_B;
        const float mx = u2f(ms.x), sumx = u2f(ms.y);
        const float mt = fmaxf(mx, mm);
        const float fx = __builtin_amdgcn_exp2f(mx - mt), fm = __builtin_amdgcn_exp2f(mm - mt);
        const float rinv = __builtin_amdgcn_rcpf(fx * sumx + fm * summ);
        const float cx = fx * rinv, cm = fm * rinv;
        const f32x4 axv = {blo(axu.x), bhi(axu.x), blo(axu.y), bhi(axu.y)};
        f32x4 r4;
#pragma unroll
        for (int r = 0; r < 4; ++r) { float t = axv[r] * cx + am[r] * cm; r4[r] = t + dpp_f<0x128>(t); }
        *(LAS f32x4*)(lds + L_P + w * P_STR + n16 * 64 + g * 16) = r4;
        CS(5)
        if (VLATE == 0) __builtin_amdgcn_raw_buffer_store_b64(vpk, rvtm, st_vtm, 0, 16);
        wg_barrier();
        CS(6)
        if (w == 0) {
          LAS char* b = lds + L_P + (l & 1) * 4 * P_STR + (l >> 3) * 64 + gq * 16;
          f32x4 t0 = *(LAS f32x4*)b, t1 = *(LAS f32x4*)(b + P_STR), t2 = *(LAS f32x4*)(b + 2 * P_STR), t3 = *(LAS f32x4*)(b + 3 * P_STR);
          f32x4 t = (t0 + t1) + (t2 + t3);
#pragma unroll
          for (int r = 0; r < 4; ++r) t[r] += dpp_f<0xB1>(t[r]);
          unsigned p0 = min(cvt_pk(t[0], t[1]), 0xfffffffeu), p1 = min(cvt_pk(t[2], t[3]), 0xfffffffeu);
          unsigned q0 = dpp_u<0x4E>(p0), q1 = dpp_u<0x4E>(p1);
          __builtin_amdgcn_raw_buffer_store_b128((u32x4){p0, p1, q0, q1}, rmem, st_mem, 0, 16);
          st_mem += inc_mem;
          CS(7)
        }
        if (VLATE == 1) { CBAR(); __builtin_amdgcn_raw_buffer_store_b64(vpk, rvtm, st_vtm, 0, 16); }
        if (PT) { CBAR(); unsigned a = LD4(rx, off_mm), b = LD4(rx, off_mm + 512), c2 = LD4(rx, off_ax), d = LD4(rx, off_ms); asm volatile("" :: "v"(a), "v"(b), "v"(c2), "v"(d)); }
      } else {
        if (!W7 && MP >= 0) asm volatile("" :: "v"(m0), "v"(m1), "v"(axu), "v"(ms));
        __builtin_amdgcn_raw_buffer_store_b64(vpk, rvtm, st_vtm, 0, 16);
      }
      st_vtm += KVM_B;
    } else {
      if (PT && !W7) asm volatile("" :: "v"(pt));
      wg_barrier();
    }
  }
  drain_barrier();
  if (W7 && l == 0) flag_add(flag_ptr(p, F_CM, NSEG - 1));
}

constexpr int H_P = 0, HP_STR = 1056, H_E = 7 * HP_STR + 32;
DI void helper_wave(const Params& p, const int hid, LAS char* lds, const int w, const int l) {
  const int n16 = l & 15, g = l >> 4, hb = n16 >> 3, i8 = n16 & 7;
  const int h = hid >> 1, hf = hid & 1;
  const __amdgpu_buffer_rsrc_t rmem = __builtin_amdgcn_make_buffer_rsrc(p.ws + WS_MEMIMG, 0, (NSEG + 1) * MEMIMG_B, 0x00020000);
  const __amdgpu_buffer_rsrc_t rx = __builtin_amdgcn_make_buffer_rsrc(p.ws + WS_XCH, 0, NSEG * XCH_B, 0x00020000);
  const __amdgpu_buffer_rsrc_t rk = ws_rsrc(p, WS_KT, (size_t)SEQ * NKT * 2), ru = ws_rsrc(p, WS_UT, (size_t)SEQ * NKT * 2);
  const int wk = w < 7 ? w : 0;
  unsigned off_mem = (wk + 7 * hb) * 512 + g * 128 + i8 * 16;
  const int o0 = hf * 14 + 2 * wk;
  const unsigned kp = (unsigned)((h * 28 + wk) * 64 + l) * 16, up = (unsigned)((h * 28 + o0) * 64 + l) * 16;
  unsigned st_ax = n16 < 8 ? XA_OFF + (o0 * 8 + (h >> 1)) * 512 + (h & 1) * 256 + g * 64 + n16 * 8 : OOB;
  const unsigned inc_ax = n16 < 8 ? XCH_B : 0;
  unsigned st_ms = (l < 8 && hf == 0) ? XS_OFF + (h >> 1) * 128 + ((h & 1) * 8 + l) * 8 : OOB;
  const unsigned inc_ms = (l < 8 && hf == 0) ? XCH_B : 0;
  bf16x8 k00, k01, k10, k11, u[2];
  auto prefetch = [&](int s) {
    flag_wait(flag_ptr(p, F_XG, s), 56);
    const unsigned k = kp + (unsigned)s * (SEG * NKT * 2), q = up + (unsigned)s * (SEG * NKT * 2);
    k00 = LDF(rk, k); k01 = LDF(rk, k + 7 * 1024); k10 = LDF(rk, k + 14 * 1024); k11 = LDF(rk, k + 21 * 1024);
    u[0] = LDF(ru, q); u[1] = LDF(ru, q + 1024);
  };
  if (w < 7) prefetch(0);
  int budget = 1 << 20;
  unsigned tb1 = 0;
  for (int s = 0; s + 1 < NSEG; ++s) {
    if (w < 7) {
      if (s) wait_until(tb1, TH);
      CBAR();
      u32x4 v = LD16(rmem, off_mem);
      if (__builtin_expect(isbad(mx4(v)), 0)) { do { __builtin_amdgcn_s_sleep(1); CBAR(); v = LD16(rmem, off_mem); } while (isbad(mx4(v)) && --budget > 0); }
      off_mem += MEMIMG_B;
      bf16x8 f = __builtin_bit_cast(bf16x8, v);
      asm volatile("" : "+v"(u[0]), "+v"(u[1]));
      f32x4 z = {0, 0, 0, 0};
      f32x4 a0 = MFMA16(k00, f, z), a1 = MFMA16(k01, f, z), b0 = MFMA16(k10, f, z), b1 = MFMA16(k11, f, z);
      f32x4 r;
#pragma unroll
      for (int q = 0; q < 4; ++q) { float own = hb ? b1[q] : a0[q]; float oth = hb ? a1[q] : b0[q]; r[q] = own + dpp_f<0x128>(oth); }
      *(LAS f32x4*)(lds + H_P + w * HP_STR + l * 16) = r;
    }
    wg_barrier();
    tb1 = now32();
    if (w == 0 || w == 7) {
      f32x4 t[7];
#pragma unroll
      for (int k = 0; k < 7; ++k) t[k] = *(LAS f32x4*)(lds + H_P + k * HP_STR + l * 16);
      f32x4 sc = ((t[0] + t[1]) + (t[2] + t[3])) + ((t[4] + t[5]) + t[6]);
      float mx = fmaxf(fmax3(sc[0], sc[1], sc[2]), sc[3]);
      mx = fmaxf(mx, dpp_f<0x128>(mx)); mx = x32_max(mx); mx = x16_max(mx);
      float e0 = __builtin_amdgcn_exp2f(sc[0] - mx), e1 = __builtin_amdgcn_exp2f(sc[1] - mx), e2 = __builtin_amdgcn_exp2f(sc[2] - mx), e3 = __builtin_amdgcn_exp2f(sc[3] - mx);
      if (w == 0) {
        *(LAS u32x2*)(lds + H_E + (g * 16 + i8) * 16 + hb * 8) = (u32x2){cvt_pk(e0, e1), cvt_pk(e2, e3)};
        wg_barrier();
      } else {
        wg_barrier();
        float sm = (e0 + e1) + (e2 + e3);
        sm += dpp_f<0x128>(sm); sm = x32_sum(sm); sm = x16_sum(sm);
        ST8(((u32x2){min(f2u(mx), 0xfffffffeu), min(f2u(sm), 0xfffffffeu)}), rx, st_ms);
        st_ms += inc_ms;
      }
    } else {
      wg_barrier();
    }
    if (w < 7) {
      bf16x8 pe = *(LAS bf16x8*)(lds + H_E + (g * 16 + i8) * 16);
      f32x4 z = {0, 0, 0, 0};
      f32x4 d0 = MFMA16(u[0], pe, z), d1 = MFMA16(u[1], pe, z);
      u32x2 s0 = {min(cvt_pk(d0[0], d0[1]), 0xfffffffeu), min(cvt_pk(d0[2], d0[3]), 0xfffffffeu)};
      u32x2 s1 = {min(cvt_pk(d1[0], d1[1]), 0xfffffffeu), min(cvt_pk(d1[2], d1[3]), 0xfffffffeu)};
      ST8(s0, rx, st_ax);
      ST8(s1, rx, n16 < 8 ? st_ax + 4096 : OOB);
    }
    st_ax += inc_ax;
    CBAR();
    if (w < 7) prefetch(s + 1);
  }
}

constexpr int HS_STR = 452 * 4, L_HS = 12288, L_ST = L_HS + 8 * HS_STR;
DI void comb_wg(const Params& p, LAS char* lds, int s, int hg, int w, int l) {
  const int tid = w * 64 + l, rt = hg >> 1, half = hg & 1;
  const __amdgpu_buffer_rsrc_t rpo = ws_rsrc(p, WS_PO, (size_t)4 * SEQ * DM * 4), rh = ws_rsrc(p, WS_H, (size_t)SEQ * DM * 4), rhn = ws_rsrc(p, WS_HN, (size_t)SEQ * DM * 2);
  u32x4 pv[2][4]; u32x2 xv[2]; unsigned off[2];
#pragma unroll
  for (int j = 0; j < 2; ++j) {
    const int idx = j * 512 + tid, o = idx >> 5, g = (idx >> 3) & 3, i8 = idx & 7;
    const bool ok = idx < 896;
    off[j] = ok ? (unsigned)((((s * 2 + rt) * 28 + o) * 64 + g * 16 + half * 8 + i8) * 16) : OOB;
#pragma unroll
    for (int k = 0; k < 4; ++k) pv[j][k] = LD16(rpo, ok ? off[j] + k * (SEQ * DM * 4) : OOB);
    xv[j] = ok ? *(const u32x2*)(p.x + (size_t)(s * SEG + hg * 8 + i8) * DM + o * 16 + g * 4) : (u32x2){0, 0};
  }
#pragma unroll
  for (int j = 0; j < 2; ++j) {
    const int idx = j * 512 + tid, o = idx >> 5, g = (idx >> 3) & 3, i8 = idx & 7;
    f32x4 a = (__builtin_bit_cast(f32x4, pv[j][0]) + __builtin_bit_cast(f32x4, pv[j][1])) + (__builtin_bit_cast(f32x4, pv[j][2]) + __builtin_bit_cast(f32x4, pv[j][3]));
    a += (f32x4){blo(xv[j].x), bhi(xv[j].x), blo(xv[j].y), bhi(xv[j].y)};
    ST16(__builtin_bit_cast(u32x4, a), rh, off[j]);
    if (idx < 896) *(LAS f32x4*)(lds + L_HS + i8 * HS_STR + (o * 16 + g * 4) * 4) = a;
  }
  wg_barrier();
  {
    float v[7], sm = 0.f;
#pragma unroll
    for (int j = 0; j < 7; ++j) { v[j] = *(LAS float*)(lds + L_HS + w * HS_STR + (j * 64 + l) * 4); sm += v[j]; }
    const float mean = wave_sum(sm) * (1.0f / DM);
    float q = 0.f;
#pragma unroll
    for (int j = 0; j < 7; ++j) { float d = v[j] - mean; q += d * d; }
    const float rstd = 1.0f / sqrtf(wave_sum(q) * (1.0f / DM) + 1e-5f);
    if (l == 0) { *(LAS float*)(lds + L_ST + w * 8) = mean; *(LAS float*)(lds + L_ST + w * 8 + 4) = rstd; }
  }
  wg_barrier();
  if (tid < 448) {
    const int c8 = tid >> 3, i8 = tid & 7;
    const float mean = *(LAS float*)(lds + L_ST + i8 * 8), rstd = *(LAS float*)(lds + L_ST + i8 * 8 + 4);
    const f32x4 a = *(LAS f32x4*)(lds + L_HS + i8 * HS_STR + c8 * 32), b = *(LAS f32x4*)(lds + L_HS + i8 * HS_STR + c8 * 32 + 16);
    ST16(((u32x4){cvt_pk((a[0] - mean) * rstd, (a[1] - mean) * rstd), cvt_pk((a[2] - mean) * rstd, (a[3] - mean) * rstd),
                  cvt_pk((b[0] - mean) * rstd, (b[1] - mean) * rstd), cvt_pk((b[2] - mean) * rstd, (b[3] - mean) * rstd)}), rhn, tiled_off(s * SEG + hg * 8 + i8, c8));
  }
}

DI void att_wg(const Params& p, LAS char* lds, int s, int hg, int w, int l) {
  const int n16 = l & 15, g = l >> 4;
  const int hl = w >> 1, qt = w & 1, h = hg * 4 + hl;
  const __amdgpu_buffer_rsrc_t rk = ws_rsrc(p, WS_KT, (size_t)SEQ * NKT * 2), ru = ws_rsrc(p, WS_UT, (size_t)SEQ * NKT * 2);
  const __amdgpu_buffer_rsrc_t rkm = ws_rsrc(p, WS_KTM, (size_t)NSEG * KVM_B), rvm = ws_rsrc(p, WS_VTM, (size_t)NSEG * KVM_B);
  const __amdgpu_buffer_rsrc_t rpo = ws_rsrc(p, WS_PO, (size_t)4 * SEQ * DM * 4);
  const __amdgpu_buffer_rsrc_t rxf = ws_rsrc(p, WS_XF, (size_t)SEQ * DM * 2);
  bf16x8 q[14];
#pragma unroll
  for (int ks = 0; ks < 14; ++ks) q[ks] = LDF(rxf, (unsigned)(((s * 2 + qt) * 14 + ks) * 64 + l) * 16);
  flag_wait(flag_ptr(p, F_XG, s), 56);
  const unsigned ko = (unsigned)(((s * NH + h) * 28) * 64 + l) * 16;
  f32x4 s0 = {0, 0, 0, 0}, s1 = s0, sm = s0;
  {
    bf16x8 k0[14], k1[14];
#pragma unroll
    for (int ks = 0; ks < 14; ++ks) { k0[ks] = LDF(rk, ko + ks * 1024); k1[ks] = LDF(rk, qt ? ko + (14 + ks) * 1024 : OOB); }
    SBAR();
#pragma unroll
    for (int ks = 0; ks < 14; ++ks) { s0 = MFMA16(k0[ks], q[ks], s0); s1 = MFMA16(k1[ks], q[ks], s1); }
  }
  bf16x8 uv[4][4];
  const unsigned uo = (unsigned)(((s * NH + hg * 4) * 28 + w) * 64 + l) * 16;
#pragma unroll
  for (int j = 0; j < 4; ++j)
#pragma unroll
    for (int k = 0; k < 4; ++k) uv[j][k] = LDF(ru, uo + (k * 28 + ((j < 3 || w < 4) ? j * 8 : 0)) * 1024);
  flag_wait(flag_ptr(p, F_CM, s), 32);
  DSTAMP(s == 63 && hg == 0 && w == 0 && l == 0, 3)
  const unsigned kmo = (unsigned)s * KVM_B + (unsigned)(((h >> 1) * 14) * 64 + l) * 16;
  bf16x8 vm[4];
#pragma unroll
  for (int j = 0; j < 4; ++j) {
    const unsigned vo = (unsigned)s * KVM_B + (unsigned)((2 * hg * 28 + w + ((j < 3 || w < 4) ? j * 8 : 0)) * 64 + l) * 8;
    const u32x2 a = LD8(rvm, vo), b = LD8(rvm, vo + 28 * 512);
    vm[j] = __builtin_bit_cast(bf16x8, (u32x4){a.x, a.y, b.x, b.y});
  }
  {
    bf16x8 km[14];
#pragma unroll
    for (int ks = 0; ks < 14; ++ks) km[ks] = LDF(rkm, kmo + ks * 1024);
    SBAR();
#pragma unroll
    for (int ks = 0; ks < 14; ++ks) sm = MFMA16(km[ks], q[ks], sm);
  }
  asm volatile("" : "+v"(sm));
  DSTAMP(s == 63 && hg == 0 && w == 0 && l == 0, 80)
  const float NEG = -3.0e38f;
  const bool ownm = (g >> 1) == (h & 1);
  float v[12];
#pragma unroll
  for (int r = 0; r < 4; ++r) {
    v[r] = (qt || (4 * g + r) <= n16) ? s0[r] : NEG;
    v[4 + r] = (qt && (4 * g + r) <= n16) ? s1[r] : NEG;
    v[8 + r] = ownm ? sm[r] : NEG;
  }
  float mx = NEG;
#pragma unroll
  for (int r = 0; r < 12; ++r) mx = fmaxf(mx, v[r]);
  mx = x16_max(mx); mx = x32_max(mx);
  float sum = 0.f;
#pragma unroll
  for (int r = 0; r < 12; ++r) { v[r] = v[r] > -1.0e38f ? __builtin_amdgcn_exp2f(v[r] - mx) : 0.f; sum += v[r]; }
  sum = x16_sum(sum); sum = x32_sum(sum);
  const float rinv = 1.0f / sum;
  u32x4 px = {cvt_pk(v[0] * rinv, v[1] * rinv), cvt_pk(v[2] * rinv, v[3] * rinv), cvt_pk(v[4] * rinv, v[5] * rinv), cvt_pk(v[6] * rinv, v[7] * rinv)};
  *(LAS u32x4*)(lds + (hl * 2 + qt) * 1024 + l * 16) = px;
  if (ownm) *(LAS u32x2*)(lds + 8192 + qt * 1024 + l * 16 + (hl >> 1) * 8) = (u32x2){cvt_pk(v[8] * rinv, v[9] * rinv), cvt_pk(v[10] * rinv, v[11] * rinv)};
  wg_barrier();
  DSTAMP(s == 63 && hg == 0 && w == 0 && l == 0, 81)
  bf16x8 P[2][5];
#pragma unroll
  for (int t = 0; t < 2; ++t) {
#pragma unroll
    for (int k = 0; k < 4; ++k) P[t][k] = *(LAS bf16x8*)(lds + (k * 2 + t) * 1024 + l * 16);
    P[t][4] = *(LAS bf16x8*)(lds + 8192 + t * 1024 + l * 16);
  }
  const unsigned po = (unsigned)((((hg * NSEG + s) * 2) * 28 + w) * 64 + l) * 16;
#pragma unroll
  for (int j = 0; j < 4; ++j) {
    f32x4 d0 = {0, 0, 0, 0}, d1 = d0;
#pragma unroll
    for (int k = 0; k < 4; ++k) { d0 = MFMA16(uv[j][k], P[0][k], d0); d1 = MFMA16(uv[j][k], P[1][k], d1); }
    d0 = MFMA16(vm[j], P[0][4], d0); d1 = MFMA16(vm[j], P[1][4], d1);
    if (j < 3 || w < 4) { ST16(__builtin_bit_cast(u32x4, d0), rpo, po + j * 8192); ST16(__builtin_bit_cast(u32x4, d1), rpo, po + j * 8192 + 28 * 1024); }
  }
  DSTAMP(s == 63 && hg == 0 && w == 0 && l == 0, 82)
  drain_barrier();
  DSTAMP(s == 63 && hg == 0 && w == 0 && l == 0, 83)
  if (w == 0) {
    if (l == 0) flag_add(flag_ptr(p, F_AT, s));
    flag_wait(flag_ptr(p, F_AT, s), 4);
  }
  wg_barrier();
  DSTAMP(s == 63 && hg == 0 && w == 0 && l == 0, 4)
  comb_wg(p, lds, s, hg, w, l);
  drain_barrier();
  if (w == 0 && l == 0) flag_add(flag_ptr(p, F_HN, s));
}

constexpr int L_GW = 57344, G1_STR = 72, G2_STR = 40;
DI void glu1_stage(const Params& p, LAS char* lds, int fb, int tid) {
#pragma unroll
  for (int i = 0; i < 7; ++i) {
    const int c = i * 512 + tid, row = c >> 2, gu = row >= 448, d = row - 448 * gu;
    u32x4 v = *(const u32x4*)((gu ? p.wu : p.wg) + (size_t)d * FF + fb * 32 + (c & 3) * 8);
    LAS char* dst = lds + L_GW + row * G1_STR + (c & 3) * 16;
    *(LAS u32x2*)dst = (u32x2){v.x, v.y}; *(LAS u32x2*)(dst + 8) = (u32x2){v.z, v.w};
  }
}
DI void glu2_stage(const Params& p, LAS char* lds, int o, int tid) {
#pragma unroll
  for (int i = 0; i < 7; ++i) {
    const int c = i * 512 + tid, row = c >> 1;
    u32x4 v = *(const u32x4*)(p.wd + (size_t)row * DM + o * 16 + (c & 1) * 8);
    LAS char* dst = lds + L_GW + row * G2_STR + (c & 1) * 16;
    *(LAS u32x2*)dst = (u32x2){v.x, v.y}; *(LAS u32x2*)(dst + 8) = (u32x2){v.z, v.w};
  }
}
DI bf16x8 tr_frag(LAS char* b, int str) {
  s16x4 lo = __builtin_amdgcn_ds_read_tr16_b64_v4i16((LAS s16x4*)b);
  s16x4 hi = __builtin_amdgcn_ds_read_tr16_b64_v4i16((LAS s16x4*)(b + 4 * str));
  u32x2 l2 = __builtin_bit_cast(u32x2, lo), h2 = __builtin_bit_cast(u32x2, hi);
  return __builtin_bit_cast(bf16x8, (u32x4){l2.x, l2.y, h2.x, h2.y});
}
DI void glu1_wg(const Params& p, LAS char* lds, int fb, int w, int l) {
  const int ftl = w & 1, rt = (w >> 1) & 1, sp = w >> 2;
  const __amdgpu_buffer_rsrc_t rhn = ws_rsrc(p, WS_HN, (size_t)SEQ * DM * 2), rhg = ws_rsrc(p, WS_HG, (size_t)SEQ * FF * 2);
  bf16x8 wgt[14], wup[14];
#pragma unroll
  for (int ks = 0; ks < 14; ++ks) {
    LAS char* b = lds + L_GW + (32 * ks + 8 * (l >> 4) + ((l & 15) >> 2)) * G1_STR + ftl * 32 + (l & 3) * 8;
    wgt[ks] = tr_frag(b, G1_STR); wup[ks] = tr_frag(b + 448 * G1_STR, G1_STR);
  }
  const int n16 = l & 15, g = l >> 4;
  if (w == 0) *(LAS unsigned*)(lds + l * 4) = 0u;
  wg_barrier();
  for (int s = sp; s < NSEG; s += 2) {
    flag_wait(flag_ptr(p, F_HN, s), 4);
    DSTAMP(s == 63 && fb == 0 && w == 4 && l == 0, 5)
    const unsigned ho = (unsigned)(((s * 2 + rt) * 14) * 64 + l) * 16;
    bf16x8 x[14];
#pragma unroll
    for (int ks = 0; ks < 14; ++ks) x[ks] = LDF(rhn, ho + ks * 1024);
    SBAR();
    f32x4 ag = {0, 0, 0, 0}, au = ag;
#pragma unroll
    for (int ks = 0; ks < 14; ++ks) { ag = MFMA16(wgt[ks], x[ks], ag); au = MFMA16(wup[ks], x[ks], au); }
    u32x2 o = {cvt_pk(ag[0] * au[0], ag[1] * au[1]), cvt_pk(ag[2] * au[2], ag[3] * au[3])};
    ST8(o, rhg, (unsigned)(((((s * 2 + rt) * 56 + fb) * 64 + (2 * ftl + (g >> 1)) * 16 + n16) * 8 + 4 * (g & 1)) * 2));
    asm volatile("s_waitcnt vmcnt(0)" ::: "memory");
    if (l == 0) {
      const unsigned t = __hip_atomic_fetch_add((LAS unsigned*)(lds + s * 4), 1u, __ATOMIC_RELAXED, __HIP_MEMORY_SCOPE_WORKGROUP);
      if (t == 3) flag_add(flag_ptr(p, F_G1, s));
    }
  }
}

DI void glu2_wg(const Params& p, LAS char* lds, int o, int s0, int w, int l) {
  const __amdgpu_buffer_rsrc_t rhg = ws_rsrc(p, WS_HG, (size_t)SEQ * FF * 2), rh = ws_rsrc(p, WS_H, (size_t)SEQ * DM * 4);
  bf16x8 wd[7];
#pragma unroll
  for (int k = 0; k < 7; ++k) wd[k] = tr_frag(lds + L_GW + (32 * (7 * w + k) + 8 * (l >> 4) + ((l & 15) >> 2)) * G2_STR + (l & 3) * 8, G2_STR);
  const int tid = w * 64 + l;
  int buf = 0;
  for (int s = s0; s < NSEG; s += 2) {
    if (w == 0) flag_wait(flag_ptr(p, F_G1, s), 56);
    wg_barrier();
    DSTAMP(s == 63 && o == 0 && tid == 0, 6)
    const unsigned ho = (unsigned)(((s * 2) * 56 + 7 * w) * 64 + l) * 16;
    bf16x8 a[7], b[7];
#pragma unroll
    for (int k = 0; k < 7; ++k) { a[k] = LDF(rhg, ho + k * 1024); b[k] = LDF(rhg, ho + (56 + k) * 1024); }
    const int row = s * SEG + ((tid >> 6) & 1) * 16 + (tid & 15), col = o * 16 + 4 * ((tid >> 4) & 3);
    SBAR();
    u32x4 hv = {0, 0, 0, 0};
    if (tid < 128) hv = LD16(rh, (unsigned)(((s * 2 + (tid >> 6)) * 28 + o) * 64 + (tid & 63)) * 16);
    f32x4 d0 = {0, 0, 0, 0}, d1 = d0;
#pragma unroll
    for (int k = 0; k < 7; ++k) { d0 = MFMA16(wd[k], a[k], d0); d1 = MFMA16(wd[k], b[k], d1); }
    LAS char* L = lds + buf * 16384;
    *(LAS f32x4*)(L + w * 2048 + l * 16) = d0;
    *(LAS f32x4*)(L + w * 2048 + 1024 + l * 16) = d1;
    wg_barrier();
    if (tid < 128) {
      f32x4 acc = __builtin_bit_cast(f32x4, hv);
#pragma unroll
      for (int k = 0; k < 8; ++k) acc += *(LAS f32x4*)(L + k * 2048 + tid * 16);
      *(u32x2*)(p.out + (size_t)row * DM + col) = (u32x2){cvt_pk(acc[0], acc[1]), cvt_pk(acc[2], acc[3])};
    }
    buf ^= 1;
  }
}

__global__ void __launch_bounds__(512, 2) k_main(Params p) {
  extern __shared__ __attribute__((aligned(16))) char smem[];
  LAS char* lds = (LAS char*)smem;
  const int xg = blockIdx.x & 7, r = blockIdx.x >> 3;
  const int w = __builtin_amdgcn_readfirstlane(threadIdx.x >> 6), l = threadIdx.x & 63;
  pre_phase(p, lds, blockIdx.x, w, l);
  DSTAMP(blockIdx.x == 40 && threadIdx.x == 0, 7)
  if (xg == 0) {
    const int cw = r;
    if (cw < 28) { if (w == 7) chain_wave<true, true>(p, cw, lds, w, l); else chain_wave<false, true>(p, cw, lds, w, l); }
    else { if (w == 7) chain_wave<true, false>(p, cw, lds, w, l); else chain_wave<false, false>(p, cw, lds, w, l); }
    return;
  }
  if (xg == 1) { helper_wave(p, r, lds, w, l); return; }
  const int wi = (xg - 2) * 32 + r;
  if (wi < 168) { const unsigned t0 = now32(); wait_until(t0, (wi / 56) * XSTAG); xgemm_wg(p, lds, wi % 56, wi / 56, 3, w, l); }
  if (wi < 56) glu1_stage(p, lds, wi, threadIdx.x); else if (wi < 112) glu2_stage(p, lds, (wi - 56) % 28, threadIdx.x);
  wg_barrier();
  if (wi < 56) { glu1_wg(p, lds, wi, w, l); return; }
  if (wi < 112) {
    glu2_wg(p, lds, (wi - 56) % 28, (wi - 56) / 28, w, l);
    return;
  }
  for (;;) {
    if (threadIdx.x == 0) *(LAS unsigned*)(lds + 10244) = __hip_atomic_fetch_add(flag_ptr(p, F_Q, 0), 1u, __ATOMIC_RELAXED, __HIP_MEMORY_SCOPE_AGENT);
    wg_barrier();
    const unsigned t = __builtin_amdgcn_readfirstlane(*(LAS unsigned*)(lds + 10244));
    if (t >= NSEG * 4) break;
    att_wg(p, lds, t >> 2, t & 3, w, l);
  }
}

extern "C" void kernel_launch(void* const* d_in, const int* in_sizes, int n_in,
                              void* d_out, int out_size, void* d_ws, size_t ws_size,
                              hipStream_t stream) {
  Params p;
  p.x = (const uint16_t*)d_in[0]; p.mem0 = (const uint16_t*)d_in[1];
  p.Wq = (const uint16_t*)d_in[2]; p.Wk = (const uint16_t*)d_in[3]; p.Wv = (const uint16_t*)d_in[4]; p.Wo = (const uint16_t*)d_in[5];
  p.wg = (const uint16_t*)d_in[8]; p.wu = (const uint16_t*)d_in[9]; p.wd = (const uint16_t*)d_in[10];
  p.out = (uint16_t*)d_out; p.ws = (char*)d_ws;
  (void)hipFuncSetAttribute((const void*)k_main, hipFuncAttributeMaxDynamicSharedMemorySize, CHAIN_LDS + 1024);
  (void)hipMemsetAsync(p.ws + WS_FLG, 0, WS_END - WS_FLG, stream);
  k_main<<<256, 512, CHAIN_LDS + 1024, stream>>>(p);
}
```

```cpp
#include <hip/hip_runtime.h>
#include <hip/hip_bf16.h>
#include <stdint.h>

typedef __bf16 bf16x8 __attribute__((ext_vector_type(8)));
typedef __bf16 bf16x16 __attribute__((ext_vector_type(16)));
typedef __bf16 bf16x2 __attribute__((ext_vector_type(2)));
typedef float f32x4 __attribute__((ext_vector_type(4)));
typedef unsigned u32x4 __attribute__((ext_vector_type(4)));
typedef unsigned u32x2 __attribute__((ext_vector_type(2)));
typedef unsigned u32x8 __attribute__((ext_vector_type(8)));
typedef short s16x4 __attribute__((ext_vector_type(4)));
#define LAS __attribute__((address_space(3)))
#define DI __device__ __forceinline__

constexpr int DM = 448, NH = 16, HD = 256, NSEG = 64, SEG = 32, SEQ = 2048, NTOT = 14336, NKT = 7168, FF = 1792;
constexpr int MEMIMG_B = 7168;
constexpr int XM_OFF = 0, XS_OFF = 8192, XA_OFF = 9216, XCH_B = 9216 + 28 * 4096;
constexpr int KVM_B = NH * 8 * DM * 2;

constexpr int KR = 5, XW = 1;
#define TUNE TM = 1800, MP = -1, T2 = 4100, TH = 3800, DB = 4, XSTAG = 0, PRE0 = 3, PT = 0, RS = 0, XP = 0, ADAPT = 1, VLATE = 1
constexpr int TUNE;

constexpr int L_M = 0, L_T = 7168, T_STR = 640, L_P = L_T + 8 * T_STR, P_STR = 1056, L_WB = L_P + 8 * P_STR + 64;
constexpr int L_WB_AL = (L_WB + 255) & ~255;
constexpr int CHAIN_LDS = L_WB_AL + 7 * (14 - KR) * 2048;

constexpr size_t WS_MEMIMG = 0;
constexpr size_t WS_XCH = WS_MEMIMG + (size_t)(NSEG + 1) * MEMIMG_B;
constexpr size_t WS_SENT_END = WS_XCH + (size_t)NSEG * XCH_B;
constexpr size_t WS_XF = (WS_SENT_END + 4095) & ~(size_t)4095;
constexpr size_t WS_WT = WS_XF + (size_t)SEQ * DM * 2;
constexpr size_t WS_KT = WS_WT + (size_t)NTOT * DM * 2;
constexpr size_t WS_UT = WS_KT + (size_t)SEQ * NKT * 2;
constexpr size_t WS_KTM = WS_UT + (size_t)SEQ * NKT * 2;
constexpr size_t WS_VTM = WS_KTM + (size_t)NSEG * KVM_B;
constexpr size_t WS_PO = WS_VTM + (size_t)NSEG * KVM_B;
constexpr size_t WS_H = WS_PO + (size_t)4 * SEQ * DM * 4;
constexpr size_t WS_HN = WS_H + (size_t)SEQ * DM * 4;
constexpr size_t WS_WG = WS_HN + (size_t)SEQ * DM * 2;
constexpr size_t WS_WD = WS_WG + (size_t)2 * FF * DM * 2;
constexpr size_t WS_HG = WS_WD + (size_t)FF * DM * 2;
constexpr size_t WS_FLG = WS_HG + (size_t)SEQ * FF * 2;
constexpr size_t WS_END = WS_FLG + 6 * NSEG * 128;
constexpr int F_XG = 0, F_CM = 1, F_AT = 2, F_HN = 3, F_G1 = 4, F_Q = 5;

struct Params {
  const uint16_t* x; const uint16_t* mem0; const uint16_t* Wq; const uint16_t* Wk; const uint16_t* Wv; const uint16_t* Wo;
  const uint16_t* wg; const uint16_t* wu; const uint16_t* wd; uint16_t* out; char* ws;
};

DI unsigned cvt_pk(float a, float b) { bf16x2 r = {(__bf16)a, (__bf16)b}; return __builtin_bit_cast(unsigned, r); }
DI void wg_barrier() { asm volatile("s_waitcnt lgkmcnt(0)\n\ts_barrier" ::: "memory"); }
DI unsigned umax3(unsigned a, unsigned b, unsigned c) { return max(max(a, b), c); }
DI float fmax3(float a, float b, float c) { return fmaxf(fmaxf(a, b), c); }
DI float u2f(unsigned u) { return __builtin_bit_cast(float, u); }
DI unsigned f2u(float f) { return __builtin_bit_cast(unsigned, f); }
DI float blo(unsigned u) { return u2f(u << 16); }
DI float bhi(unsigned u) { return u2f(u & 0xffff0000u); }
template<int CTRL> DI float dpp_f(float x) { return u2f((unsigned)__builtin_amdgcn_update_dpp(0, (int)f2u(x), CTRL, 0xf, 0xf, true)); }
template<int CTRL> DI unsigned dpp_u(unsigned x) { return (unsigned)__builtin_amdgcn_update_dpp(0, (int)x, CTRL, 0xf, 0xf, true); }
DI float x32_max(float x) { auto r = __builtin_amdgcn_permlane32_swap(f2u(x), f2u(x), false, false); return fmaxf(u2f(r[0]), u2f(r[1])); }
DI float x32_sum(float x) { auto r = __builtin_amdgcn_permlane32_swap(f2u(x), f2u(x), false, false); return u2f(r[0]) + u2f(r[1]); }
DI float x16_max(float x) { auto r = __builtin_amdgcn_permlane16_swap(f2u(x), f2u(x), false, false); return fmaxf(u2f(r[0]), u2f(r[1])); }
DI float x16_sum(float x) { auto r = __builtin_amdgcn_permlane16_swap(f2u(x), f2u(x), false, false); return u2f(r[0]) + u2f(r[1]); }
DI float wave_sum(float v) {
  v += dpp_f<0xB1>(v); v += dpp_f<0x4E>(v); v += dpp_f<0x124>(v); v += dpp_f<0x128>(v);
  v = x16_sum(v); v = x32_sum(v); return v;
}
#define SMFMAC(a, b, c, idx) __builtin_amdgcn_smfmac_f32_16x16x64_bf16((a), (b), (c), (idx), 0, 0)
#define MFMA16(a, b, c) __builtin_amdgcn_mfma_f32_16x16x32_bf16((a), (b), (c), 0, 0, 0)
constexpr unsigned OOB = 0x7fffffffu;
#define LD16(r, off) __builtin_amdgcn_raw_buffer_load_b128((r), (off), 0, 16)
#define LD4(r, off) __builtin_amdgcn_raw_buffer_load_b32((r), (off), 0, 16)
#define LD8(r, off) __builtin_amdgcn_raw_buffer_load_b64((r), (off), 0, 16)
#define CBAR() asm volatile("" ::: "memory")
#define SBAR() __builtin_amdgcn_sched_barrier(0)

DI unsigned* flag_ptr(const Params& p, int kind, int s) { return (unsigned*)(p.ws + WS_FLG + (size_t)(kind * NSEG + s) * 128); }
DI void flag_add(unsigned* f) { __hip_atomic_fetch_add(f, 1u, __ATOMIC_RELAXED, __HIP_MEMORY_SCOPE_AGENT); }
DI void flag_wait(const unsigned* f, unsigned want) {
  int budget = 1 << 20;
  while ((unsigned)__builtin_amdgcn_readfirstlane(__hip_atomic_load(f, __ATOMIC_RELAXED, __HIP_MEMORY_SCOPE_AGENT)) < want) {
    if (--budget < 0) break;
    __builtin_amdgcn_s_sleep(4);
  }
  asm volatile("" ::: "memory");
}
DI void drain_barrier() { asm volatile("s_waitcnt vmcnt(0) lgkmcnt(0)\n\ts_barrier" ::: "memory"); }
DI __amdgpu_buffer_rsrc_t ws_rsrc(const Params& p, size_t off, size_t bytes) { return __builtin_amdgcn_make_buffer_rsrc(p.ws + off, 0, (int)bytes, 0x00020000); }
#define LDF(r, off) __builtin_bit_cast(bf16x8, LD16((r), (off)))
#define ST16(v, r, off) __builtin_amdgcn_raw_buffer_store_b128((v), (r), (off), 0, 16)
#define ST8(v, r, off) __builtin_amdgcn_raw_buffer_store_b64((v), (r), (off), 0, 16)

DI u32x4 ln_row(float (&v)[8], bool act) {
  float s = 0.f;
#pragma unroll
  for (int j = 0; j < 8; ++j) s += v[j];
  s = wave_sum(act ? s : 0.f);
  const float mean = s * (1.0f / DM);
  float q = 0.f;
#pragma unroll
  for (int j = 0; j < 8; ++j) { float d = v[j] - mean; q += d * d; }
  q = wave_sum(act ? q : 0.f);
  const float rstd = 1.0f / sqrtf(q * (1.0f / DM) + 1e-5f);
  u32x4 r;
#pragma unroll
  for (int j = 0; j < 4; ++j) r[j] = cvt_pk((v[2 * j] - mean) * rstd, (v[2 * j + 1] - mean) * rstd);
  return r;
}
DI unsigned tiled_off(int row, int c8) { return (unsigned)((((row >> 4) * 14 + (c8 >> 2)) * 64 + (c8 & 3) * 16 + (row & 15)) * 16); }

constexpr int WQ_STR = 144, WP_OFF = 224 * WQ_STR, WO_STR = 240, WBUF = 336 * WQ_STR + 4608;
template<bool isB>
DI void wt_wg(const Params& p, LAS char* lds, int task, int w, int l) {
  const int n16 = l & 15, g = l >> 4, tid = w * 64 + l;
  const int mat = task >> 3, qe = (task >> 2) & 1, qn = task & 3;
  const int h = mat & 15;
  const uint16_t* Qg = (isB ? p.Wv : p.Wk) + (size_t)(qe * 224) * (NH * HD) + h * HD;
  const uint16_t* Pg = isB ? p.Wo + (size_t)h * HD * DM + qn * 112 : p.Wq + (size_t)(qn * 112) * (NH * HD) + h * HD;
  u32x4 r[4][6];
  const uint16_t* src[6]; int loff[6];
#pragma unroll
  for (int i = 0; i < 6; ++i) {
    const int c = min(i * 512 + tid, 2687), c2 = c - 1792;
    src[i] = c < 1792 ? Qg + (size_t)(c >> 3) * (NH * HD) + (c & 7) * 8
           : (isB ? Pg + (size_t)(c2 / 14) * DM + (c2 % 14) * 8 : Pg + (size_t)(c2 >> 3) * (NH * HD) + (c2 & 7) * 8);
    const int qr = c >> 3, qp = (qr & ~31) + ((qr >> 2) & 1) * 16 + ((qr >> 3) & 3) * 4 + (qr & 3);
    loff[i] = c < 1792 ? qp * WQ_STR + (c & 7) * 16 : (!isB ? qr * WQ_STR + (c & 7) * 16 : WP_OFF + (c2 / 14) * WO_STR + (c2 % 14) * 16);
  }
  const int strP = isB ? 64 * DM : 64, str3 = tid < 256 ? 64 : strP;
  const int rot = (qn + 2 * qe) & 3;
  auto gload = [&](int j) {
    const int kk = (j + rot) & 3;
#pragma unroll
    for (int i = 0; i < 6; ++i) r[j][i] = *(const u32x4*)(src[i] + kk * (i < 3 ? 64 : (i == 3 ? str3 : strP)));
  };
  gload(0);
  const int we = w >> 2, wn = w & 3;
  f32x4 acc[8][2];
#pragma unroll
  for (int a = 0; a < 8; ++a) { acc[a][0] = (f32x4){0, 0, 0, 0}; acc[a][1] = (f32x4){0, 0, 0, 0}; }
  gload(1); gload(2); gload(3);
#pragma unroll 1
  for (int it = 0; it < 2; ++it) {
    if (it) wg_barrier();
#pragma unroll
    for (int i = 0; i < 6; ++i) { *(LAS u32x4*)(lds + loff[i]) = r[0][i]; *(LAS u32x4*)(lds + WBUF + loff[i]) = r[1][i]; }
    wg_barrier();
#pragma unroll 2
    for (int k4 = 0; k4 < 4; ++k4) {
      LAS char* L = lds + (k4 >> 1) * WBUF;
      const int k = k4 & 1;
      bf16x8 pb[2];
#pragma unroll
      for (int c = 0; c < 2; ++c) {
        const int nt = wn + 4 * c;
        if (isB) {
          LAS char* bb = L + WP_OFF + (k * 32 + 8 * g + (n16 >> 2)) * WO_STR + nt * 32 + (n16 & 3) * 8;
          s16x4 lo = __builtin_amdgcn_ds_read_tr16_b64_v4i16((LAS s16x4*)bb);
          s16x4 hi = __builtin_amdgcn_ds_read_tr16_b64_v4i16((LAS s16x4*)(bb + 4 * WO_STR));
          u32x2 l2 = __builtin_bit_cast(u32x2, lo), h2 = __builtin_bit_cast(u32x2, hi);
          pb[c] = __builtin_bit_cast(bf16x8, (u32x4){l2.x, l2.y, h2.x, h2.y});
        } else {
          pb[c] = *(LAS bf16x8*)(L + WP_OFF + (nt * 16 + n16) * WQ_STR + k * 64 + g * 16);
        }
      }
#pragma unroll
      for (int a = 0; a < 8; ++a) {
        const int ep = 4 * we + (a >> 1);
        bf16x8 qa = *(LAS bf16x8*)(L + (ep * 32 + (a & 1) * 16 + n16) * WQ_STR + k * 64 + g * 16);
        acc[a][0] = MFMA16(qa, pb[0], acc[a][0]); acc[a][1] = MFMA16(qa, pb[1], acc[a][1]);
      }
    }
#pragma unroll
    for (int i = 0; i < 6; ++i) { r[0][i] = r[2][i]; r[1][i] = r[3][i]; }
  }
  const float sc = isB ? 1.0f : 0.0625f * 1.4426950408889634f;
  const __amdgpu_buffer_rsrc_t rw = ws_rsrc(p, WS_WT, (size_t)NTOT * DM * 2);
#pragma unroll
  for (int c = 0; c < 2; ++c)
#pragma unroll
    for (int m = 0; m < 4; ++m) {
      if ((c < 1 || wn < 3) && (m < 3 || we == 0)) {
        const int nt = ((isB ? NKT : 0) + h * DM + qn * 112) / 16 + wn + 4 * c, ks = qe * 7 + 4 * we + m;
        const f32x4 x = acc[2 * m][c] * sc, y = acc[2 * m + 1][c] * sc;
        ST16(((u32x4){cvt_pk(x[0], x[1]), cvt_pk(x[2], x[3]), cvt_pk(y[0], y[1]), cvt_pk(y[2], y[3])}), rw, (unsigned)((nt * 14 + ks) * 64 + l) * 16);
      }
    }
}

DI void pre_phase(const Params& p, LAS char* lds, int b, int w, int l) {
  const int tid = w * 64 + l;
  const __amdgpu_buffer_rsrc_t rs = ws_rsrc(p, 0, WS_XF + (size_t)SEQ * DM * 2);
  const bool act = l < 56;
  const int row = b * 8 + w;
  u32x4 xt = act ? *(const u32x4*)(p.x + (size_t)row * DM + l * 8) : (u32x4){0, 0, 0, 0};
  const int task = (b & 7) * 32 + (b >> 3);
  if (task >= 128) wt_wg<true>(p, lds, task, w, l); else wt_wg<false>(p, lds, task, w, l);
  {
    float v[8];
#pragma unroll
    for (int j = 0; j < 4; ++j) { v[2 * j] = blo(xt[j]); v[2 * j + 1] = bhi(xt[j]); }
    u32x4 r = ln_row(v, act);
    ST16(r, rs, act ? (unsigned)WS_XF + tiled_off(row, l) : OOB);
  }
  if (tid < 32) {
    const int i = b * 32 + tid;
    ST16(((u32x4){~0u, ~0u, ~0u, ~0u}), rs, i < XCH_B / 16 ? (unsigned)WS_XCH + i * 16 : (unsigned)MEMIMG_B + (i - XCH_B / 16) * 16);
  }
  if (b == 9 && tid < 448) {
    u32x4 t = *(const u32x4*)(p.mem0 + (tid & 7) * DM + (tid >> 3) * 8);
    t.x = min(t.x, 0xfffffffeu); t.y = min(t.y, 0xfffffffeu); t.z = min(t.z, 0xfffffffeu); t.w = min(t.w, 0xfffffffeu);
    ST16(t, rs, tid * 16);
  }
  drain_barrier();
  if (w == 0) {
    if (l == 0) {
      const unsigned t = __hip_atomic_fetch_add(flag_ptr(p, F_Q, 8 + (b & 7)), 1u, __ATOMIC_RELAXED, __HIP_MEMORY_SCOPE_AGENT);
      if (t == 31) flag_add(flag_ptr(p, F_Q, 16));
    }
    int budget = 1 << 20;
    while ((unsigned)__builtin_amdgcn_readfirstlane(__hip_atomic_load(flag_ptr(p, F_Q, 16), __ATOMIC_RELAXED, __HIP_MEMORY_SCOPE_AGENT)) < 8u) {
      if (--budget < 0) break;
      __builtin_amdgcn_s_sleep(2);
    }
  }
  wg_barrier();
}

DI void xgemm_wg(const Params& p, LAS char* lds, int nb, int seg0, int step, int w, int l) {
  const int n16 = l & 15, g = l >> 4;
  const int n0 = nb * 256 + w * 32;
  const bool isU = n0 >= NKT;
  const int r0 = isU ? n0 + n16 : n0 + 8 * (n16 >> 2) + (n16 & 3);
  const int r1 = isU ? r0 + 16 : r0 + 4;
  const __amdgpu_buffer_rsrc_t rw = ws_rsrc(p, WS_WT, (size_t)NTOT * DM * 2), rxf = ws_rsrc(p, WS_XF, (size_t)SEQ * DM * 2);
  const unsigned W0 = (unsigned)((r0 >> 4) * 14 * 64 + g * 16 + (r0 & 15)) * 16, W1 = (unsigned)((r1 >> 4) * 14 * 64 + g * 16 + (r1 & 15)) * 16;
  bf16x8 w0[14], w1[14];
#pragma unroll
  for (int ks = 0; ks < 14; ++ks) { w0[ks] = LDF(rw, W0 + ks * 1024); w1[ks] = LDF(rw, W1 + ks * 1024); }
  const int nn = isU ? n0 - NKT : n0;
  const int hh = nn / DM, cc = nn % DM;
  const int tid = w * 64 + l;
  u32x4 pre[4];
  auto gload = [&](int s) {
#pragma unroll
    for (int i = 0; i < 4; ++i) { const int c = i * 512 + tid; pre[i] = LD16(rxf, c < 1792 ? (unsigned)(s * 1792 + c) * 16 : OOB); }
  };
  gload(seg0);
  int buf = 0, prev = -1;
  const __amdgpu_buffer_rsrc_t rout = ws_rsrc(p, isU ? WS_UT : WS_KT, (size_t)SEQ * NKT * 2);
  const __amdgpu_buffer_rsrc_t rsent = ws_rsrc(p, 0, WS_SENT_END);
  const unsigned tx0 = (unsigned)__builtin_amdgcn_s_memtime();
  unsigned txd = 0;
  for (int s = seg0; s < NSEG; s += step) {
    while ((unsigned)__builtin_amdgcn_s_memtime() - tx0 < txd) __builtin_amdgcn_s_sleep(8);
    txd += XP;
    LAS char* L = lds + buf * 28672;
#pragma unroll
    for (int i = 0; i < 4; ++i) { const int c = i * 512 + tid; if (c < 1792) *(LAS u32x4*)(L + c * 16) = pre[i]; }
    asm volatile("s_waitcnt vmcnt(0)" ::: "memory");
    if (s + step < NSEG) gload(s + step);
    wg_barrier();
    if (tid == 0 && prev >= 0) flag_add(flag_ptr(p, F_XG, prev));
    f32x4 a00 = {0, 0, 0, 0}, a01 = a00, a10 = a00, a11 = a00;
#pragma unroll
    for (int ks = 0; ks < 14; ++ks) {
      bf16x8 x0 = *(LAS bf16x8*)(L + ks * 1024 + l * 16), x1 = *(LAS bf16x8*)(L + (14 + ks) * 1024 + l * 16);
      if (isU) { a00 = MFMA16(x0, w0[ks], a00); a01 = MFMA16(x1, w0[ks], a01); a10 = MFMA16(x0, w1[ks], a10); a11 = MFMA16(x1, w1[ks], a11); }
      else     { a00 = MFMA16(w0[ks], x0, a00); a01 = MFMA16(w1[ks], x0, a01); a10 = MFMA16(w0[ks], x1, a10); a11 = MFMA16(w1[ks], x1, a11); }
    }
    u32x4 o0 = {cvt_pk(a00[0], a00[1]), cvt_pk(a00[2], a00[3]), cvt_pk(a01[0], a01[1]), cvt_pk(a01[2], a01[3])};
    u32x4 o1 = {cvt_pk(a10[0], a10[1]), cvt_pk(a10[2], a10[3]), cvt_pk(a11[0], a11[1]), cvt_pk(a11[2], a11[3])};
    {
      const unsigned off = (unsigned)((((s * NH + hh) * 28 + (isU ? (cc >> 4) : (cc >> 5))) * 64 + l) * 16);
      ST16(o0, rout, off); ST16(o1, rout, off + (isU ? 1024 : 14 * 1024));
    }
    if (s + 1 < NSEG) {
      const int i = nb * 147 + tid;
      if (tid < 147 && i < (XCH_B + MEMIMG_B) / 16) {
        const unsigned off = i < XCH_B / 16 ? (unsigned)(WS_XCH + (size_t)(s + 1) * XCH_B) + i * 16 : (unsigned)((s + 2) * MEMIMG_B) + (i - XCH_B / 16) * 16;
        ST16(((u32x4){~0u, ~0u, ~0u, ~0u}), rsent, off);
      }
    }
    if (s == seg0) { drain_barrier(); if (tid == 0) flag_add(flag_ptr(p, F_XG, s)); prev = -1; }
    else prev = s;
    buf ^= 1;
  }
  drain_barrier();
  if (tid == 0 && prev >= 0) flag_add(flag_ptr(p, F_XG, prev));
}
DI bf16x16 load_wfrag(__amdgpu_buffer_rsrc_t rw, unsigned base, int k) {
  u32x2 alo = LD8(rw, base + k * 1024), ahi = LD8(rw, base + k * 1024 + 512);
  u32x2 blo_ = LD8(rw, base + (k + 7) * 1024), bhi_ = LD8(rw, base + (k + 7) * 1024 + 512);
  u32x8 r = {alo.x, blo_.x, alo.y, blo_.y, ahi.x, bhi_.x, ahi.y, bhi_.y};
  return __builtin_bit_cast(bf16x16, r);
}
DI f32x4 comb32(f32x4 a, f32x4 b) {
  f32x4 r;
#pragma unroll
  for (int i = 0; i < 4; ++i) { float fa = a[i], fb = b[i]; auto t = __builtin_amdgcn_permlane32_swap(f2u(fa), f2u(fb), false, false); r[i] = u2f(t[0]) + u2f(t[1]); }
  return r;
}
DI bf16x16 lds_wfrag(LAS char* d) { u32x4 t0 = *(LAS u32x4*)d, t1 = *(LAS u32x4*)(d + 1024);
  u32x8 t = {t0.x, t0.y, t0.z, t0.w, t1.x, t1.y, t1.z, t1.w}; return __builtin_bit_cast(bf16x16, t); }
DI unsigned now32() { return (unsigned)__builtin_amdgcn_s_memtime(); }
DI void wait_until(unsigned t0, unsigned d) { while (now32() - t0 < d) { } }
DI void wait_until_asm(unsigned t0, unsigned d) {
  asm volatile("1:\n\ts_memtime vcc\n\ts_waitcnt lgkmcnt(0)\n\ts_sub_u32 vcc_lo, vcc_lo, %0\n\ts_cmp_lt_u32 vcc_lo, %1\n\ts_cbranch_scc1 1b"
               :: "s"(t0), "s"(d) : "vcc", "scc", "memory");
}
DI unsigned mx4(u32x4 v) { return max(umax3(v.x, v.y, v.z), v.w); }
DI bool isbad(unsigned m) { return __builtin_amdgcn_ballot_w64(m == 0xffffffffu) != 0; }

template<bool W7, bool OUT>
DI void chain_wave(const Params& p, const int cw, LAS char* lds, const int w, const int l) {
  constexpr int KRW = W7 ? 14 : KR;
  constexpr int NL = 14 - KRW;
  constexpr int PRE = W7 ? 0 : PRE0;
  const int n16 = l & 15, g = l >> 4, tid = w * 64 + l;
  const int h = cw >> 1, c = cw & 1, o = cw;
  const int idx = n16 < 8 ? 0x4444 : 0xEEEE;
  const __amdgpu_buffer_rsrc_t rw = ws_rsrc(p, WS_WT, (size_t)NTOT * DM * 2);
  const unsigned WX = ((g >> 1) * 16 + n16) * 16 + (g & 1) * 8;
  const __amdgpu_buffer_rsrc_t rmem = __builtin_amdgcn_make_buffer_rsrc(p.ws + WS_MEMIMG, 0, (NSEG + 1) * MEMIMG_B, 0x00020000);
  const __amdgpu_buffer_rsrc_t rx = __builtin_amdgcn_make_buffer_rsrc(p.ws + WS_XCH, 0, NSEG * XCH_B, 0x00020000);
  const __amdgpu_buffer_rsrc_t rktm = __builtin_amdgcn_make_buffer_rsrc(p.ws + WS_KTM, 0, NSEG * KVM_B, 0x00020000);
  const __amdgpu_buffer_rsrc_t rvtm = __builtin_amdgcn_make_buffer_rsrc(p.ws + WS_VTM, 0, NSEG * KVM_B, 0x00020000);
  bf16x16 wa[W7 ? 1 : 14];
  bf16x16 wb[OUT ? KRW : 1];
  if (!W7) {
    const unsigned ra = WX + (unsigned)(h * 28 + 14 * c + 2 * w) * 14336;
#pragma unroll
    for (int u = 0; u < 14; ++u) wa[u] = load_wfrag(rw, ra + (u & 1) * 14336, u >> 1);
  }
  LAS char* wl = lds + L_WB_AL + w * (14 - KR) * 2048 + l * 16;
  if (OUT) {
    const unsigned ra = WX + (unsigned)(448 + (2 * w) * 28 + o) * 14336;
#pragma unroll
    for (int u = 0; u < 14; ++u) {
      bf16x16 f = load_wfrag(rw, ra + (u & 1) * 28 * 14336, u >> 1);
      if (u < KRW) wb[u] = f;
      else { u32x8 t = __builtin_bit_cast(u32x8, f); LAS char* d = wl + (u - KRW) * 2048;
        *(LAS u32x4*)d = (u32x4){t[0], t[1], t[2], t[3]}; *(LAS u32x4*)(d + 1024) = (u32x4){t[4], t[5], t[6], t[7]}; }
    }
  }
  if (W7) { if (l < T_STR / 16) *(LAS u32x4*)(lds + L_T + 7 * T_STR + l * 16) = (u32x4){0, 0, 0, 0}; }
  unsigned off_mem = tid * 16;
  const int hb = n16 >> 3, i8 = n16 & 7;
  unsigned off_mm = XM_OFF + w * 1024 + (hb * 16 + (g & 1) * 8 + i8) * 16;
  unsigned off_ms = XS_OFF + w * 128 + n16 * 8;
  unsigned off_ax = XA_OFF + (o * 8 + w) * 512 + hb * 256 + g * 64 + i8 * 8;
  const bool own_m = hb == (g >> 1);
  const bool st_m = n16 < 8 && g < 2;
  unsigned st_mm = st_m ? XM_OFF + (h >> 1) * 1024 + c * 512 + ((h & 1) * 16 + g * 8 + i8) * 16 : OOB;
  const unsigned inc_mm = st_m ? XCH_B : 0;
  const int gq = (l >> 1) & 3;
  unsigned st_mem = ((l & 3) == 0) ? MEMIMG_B + o * 256 + (gq >> 1) * 128 + (l >> 3) * 16 : OOB;
  const unsigned inc_mem = ((l & 3) == 0) ? MEMIMG_B : 0;
  unsigned st_vtm = ((w * 28 + o) * 64 + l) * 8;
  unsigned st_ktm = ((((h >> 1) * 14 + 7 * c + (g >> 1)) * 4 + 2 * hb + (g & 1)) * 16 + (h & 1) * 8 + i8) * 16;
  LAS char* a_rd = lds + L_M + hb * 7 * 512 + g * 128 + i8 * 16;
  int budget = 1 << 20;
  unsigned tb1 = 0, adj = 0, clean = 0;
  wg_barrier();
  for (int s = 0; s < NSEG; ++s) {
    bf16x16 tw[NL > 0 ? NL : 1];
    if (OUT) {
#pragma unroll
      for (int j = 0; j < PRE; ++j) tw[j] = lds_wfrag(wl + j * 2048);
      SBAR();
    }
    if (!W7) {
      if (s) wait_until(tb1, T2 + 2 * adj);
      CBAR();
      u32x4 v = LD16(rmem, off_mem);
      if (__builtin_expect(isbad(mx4(v)), 0)) { do { if (RS) __builtin_amdgcn_s_sleep(RS); CBAR(); v = LD16(rmem, off_mem); } while (isbad(mx4(v)) && --budget > 0); }
      *(LAS u32x4*)(lds + L_M + tid * 16) = v;
      off_mem += MEMIMG_B;
    }
    if (W7) asm volatile("s_waitcnt vmcnt(0)" ::: "memory");
    wg_barrier();
    const unsigned tprev = tb1;
    tb1 = now32();
    if (W7 && l == 0 && s > 0) flag_add(flag_ptr(p, F_CM, s - 1));
    unsigned pt = 0;
    if (PT && !W7) pt = LD4(rmem, off_mem);
    bf16x8 af[7];
#pragma unroll
    for (int k = 0; k < 7; ++k) af[k] = *(LAS bf16x8*)(a_rd + k * 512);
    f32x4 vt1 = {0, 0, 0, 0}, vt2 = {0, 0, 0, 0};
    if (!W7) {
      f32x4 acc1 = {0, 0, 0, 0}, acc2 = {0, 0, 0, 0};
#pragma unroll
      for (int k = 0; k < 7; ++k) { acc1 = SMFMAC(af[k], wa[2 * k], acc1, idx); acc2 = SMFMAC(af[k], wa[2 * k + 1], acc2, idx); }
      f32x4 acc = comb32(acc1, acc2);
      u32x2 pk = {cvt_pk(acc[0], acc[1]), cvt_pk(acc[2], acc[3])};
      *(LAS u32x2*)(lds + L_T + w * T_STR + n16 * 40 + g * 8) = pk;
      wg_barrier();
    } else {
      bf16x8 mb[4];
#pragma unroll
      for (int q = 0; q < 4; ++q) { int ks = min(7 * c + 2 * q + (g >> 1), 13); mb[q] = *(LAS bf16x8*)(lds + L_M + ks * 512 + (2 * hb + (g & 1)) * 128 + i8 * 16); }
      if (OUT) {
#pragma unroll
        for (int k = 0; k < 7; ++k) { vt1 = SMFMAC(af[k], wb[2 * k], vt1, idx); vt2 = SMFMAC(af[k], wb[2 * k + 1], vt2, idx); }
      }
      asm volatile("" : "+v"(vt1), "+v"(vt2));
      SBAR();
      __builtin_amdgcn_s_setprio(3);
      wg_barrier();
      f32x4 d2 = {0, 0, 0, 0}, d3 = {0, 0, 0, 0};
      bf16x8 kt[4];
#pragma unroll
      for (int q = 0; q < 4; ++q) {
        LAS char* b = lds + L_T + (2 * q + (g >> 1)) * T_STR + (8 * (g & 1) + (n16 >> 2)) * 40 + (n16 & 3) * 8;
        s16x4 lo = __builtin_amdgcn_ds_read_tr16_b64_v4i16((LAS s16x4*)b);
        s16x4 hi = __builtin_amdgcn_ds_read_tr16_b64_v4i16((LAS s16x4*)(b + 160));
        u32x2 l2 = __builtin_bit_cast(u32x2, lo), h2 = __builtin_bit_cast(u32x2, hi);
        kt[q] = __builtin_bit_cast(bf16x8, (u32x4){l2.x, l2.y, h2.x, h2.y});
      }
      d2 = MFMA16(kt[0], mb[0], d2); d3 = MFMA16(kt[1], mb[1], d3); d2 = MFMA16(kt[2], mb[2], d2); d3 = MFMA16(kt[3], mb[3], d3);
      u32x4 s2;
#pragma unroll
      for (int r = 0; r < 4; ++r) { float f = d2[r] + d3[r]; auto t = __builtin_amdgcn_permlane32_swap(f2u(f), f2u(f), false, false);
        s2[r] = min(f2u(f + dpp_f<0x128>(u2f(t[1]))), 0xfffffffeu); }
      __builtin_amdgcn_raw_buffer_store_b128(s2, rx, st_mm, 0, 16);
      __builtin_amdgcn_s_setprio(0);
      CBAR();
#pragma unroll
      for (int q = 0; q < 4; ++q) __builtin_amdgcn_raw_buffer_store_b128(__builtin_bit_cast(u32x4, kt[q]), rktm, (q == 3 && g >= 2) ? OOB : st_ktm + q * 2048, 0, 16);
      st_mm += inc_mm; st_ktm += KVM_B;
    }
    if (ADAPT) {
      if (s > 1 && tb1 - tprev > (unsigned)(T2 + 1300) + 2 * adj) { adj = min(adj + 64u, 256u); clean = 0; }
      else if (++clean >= 4 && adj) { adj -= 32; clean = 0; }
    }
    if (OUT) {
      u32x4 m0, m1; u32x2 axu, ms;
#define XLOADS() { SBAR(); wait_until_asm(tb1, TM + adj); CBAR(); m0 = LD16(rx, off_mm); m1 = LD16(rx, off_mm + 512); axu = LD8(rx, off_ax); ms = LD8(rx, off_ms); SBAR(); }
      if (!W7) {
        __builtin_amdgcn_s_sleep(DB);
#pragma unroll
        for (int u = 0; u < 14; ++u) {
          if (u < KRW) { if (u & 1) vt2 = SMFMAC(af[u >> 1], wb[u], vt2, idx); else vt1 = SMFMAC(af[u >> 1], wb[u], vt1, idx);
            if (u >= KRW - XW && PRE + (u - (KRW - XW)) < NL) tw[PRE + (u - (KRW - XW))] = lds_wfrag(wl + (PRE + (u - (KRW - XW))) * 2048); }
          else { if (u & 1) vt2 = SMFMAC(af[u >> 1], tw[u - KRW], vt2, idx); else vt1 = SMFMAC(af[u >> 1], tw[u - KRW], vt1, idx);
            if (u - KRW + PRE + XW < NL) tw[u - KRW + PRE + XW] = lds_wfrag(wl + (u - KRW + PRE + XW) * 2048); }
          SBAR();
          if (u == MP) XLOADS()
        }
      }
      if (PT && !W7) asm volatile("" :: "v"(pt));
      f32x4 vt = comb32(vt1, vt2);
      u32x2 vpk = {cvt_pk(vt[0], vt[1]), cvt_pk(vt[2], vt[3])};
      if (__builtin_expect(s + 1 < NSEG, 1)) {
        bf16x8 va = __builtin_bit_cast(bf16x8, (u32x4){vpk.x, vpk.y, 0u, 0u});
        if (VLATE == 2) __builtin_amdgcn_raw_buffer_store_b64(vpk, rvtm, st_vtm, 0, 16);
        if (W7 || MP < 0) XLOADS()
        if (__builtin_expect(isbad(max(mx4(m0), mx4(m1))), 0)) { do { if (RS) __builtin_amdgcn_s_sleep(RS); CBAR(); m0 = LD16(rx, off_mm); m1 = LD16(rx, off_mm + 512); } while (isbad(max(mx4(m0), mx4(m1))) && --budget > 0); }
        f32x4 mv = __builtin_bit_cast(f32x4, m0) + __builtin_bit_cast(f32x4, m1);
        float mm = fmaxf(fmax3(mv[0], mv[1], mv[2]), mv[3]);
        mm = x16_max(mm);
        float em[4];
#pragma unroll
        for (int r = 0; r < 4; ++r) em[r] = __builtin_amdgcn_exp2f(mv[r] - mm);
        unsigned pm0 = cvt_pk(em[0], em[1]), pm1 = cvt_pk(em[2], em[3]);
        bf16x8 pm = __builtin_bit_cast(bf16x8, (u32x4){own_m ? pm0 : 0u, own_m ? pm1 : 0u, 0u, 0u});
        f32x4 am = MFMA16(va, pm, ((f32x4){0, 0, 0, 0}));
        float summ = x16_sum((em[0] + em[1]) + (em[2] + em[3]));
        asm volatile("" : "+v"(am), "+v"(summ), "+v"(mm));
        SBAR();
        if (__builtin_expect(isbad(max(umax3(axu.x, axu.y, ms.x), ms.y)), 0)) { do { __builtin_amdgcn_s_sleep(1); CBAR(); axu = LD8(rx, off_ax); ms = LD8(rx, off_ms); } while (isbad(max(umax3(axu.x, axu.y, ms.x), ms.y)) && --budget > 0); }
        off_mm += XCH_B; off_ms += XCH_B; off_ax += XCH_B;
        const float mx = u2f(ms.x), sumx = u2f(ms.y);
        const float mt = fmaxf(mx, mm);
        const float fx = __builtin_amdgcn_exp2f(mx - mt), fm = __builtin_amdgcn_exp2f(mm - mt);
        const float rinv = __builtin_amdgcn_rcpf(fx * sumx + fm * summ);
        const float cx = fx * rinv, cm = fm * rinv;
        const f32x4 axv = {blo(axu.x), bhi(axu.x), blo(axu.y), bhi(axu.y)};
        f32x4 r4;
#pragma unroll
        for (int r = 0; r < 4; ++r) { float t = axv[r] * cx + am[r] * cm; r4[r] = t + dpp_f<0x128>(t); }
        *(LAS f32x4*)(lds + L_P + w * P_STR + n16 * 64 + g * 16) = r4;
        if (VLATE == 0) __builtin_amdgcn_raw_buffer_store_b64(vpk, rvtm, st_vtm, 0, 16);
        wg_barrier();
        if (w == 0) {
          LAS char* b = lds + L_P + (l & 1) * 4 * P_STR + (l >> 3) * 64 + gq * 16;
          f32x4 t0 = *(LAS f32x4*)b, t1 = *(LAS f32x4*)(b + P_STR), t2 = *(LAS f32x4*)(b + 2 * P_STR), t3 = *(LAS f32x4*)(b + 3 * P_STR);
          f32x4 t = (t0 + t1) + (t2 + t3);
#pragma unroll
          for (int r = 0; r < 4; ++r) t[r] += dpp_f<0xB1>(t[r]);
          unsigned p0 = min(cvt_pk(t[0], t[1]), 0xfffffffeu), p1 = min(cvt_pk(t[2], t[3]), 0xfffffffeu);
          unsigned q0 = dpp_u<0x4E>(p0), q1 = dpp_u<0x4E>(p1);
          __builtin_amdgcn_raw_buffer_store_b128((u32x4){p0, p1, q0, q1}, rmem, st_mem, 0, 16);
          st_mem += inc_mem;
        }
        if (VLATE == 1) { CBAR(); __builtin_amdgcn_raw_buffer_store_b64(vpk, rvtm, st_vtm, 0, 16); }
        if (PT) { CBAR(); unsigned a = LD4(rx, off_mm), b = LD4(rx, off_mm + 512), c2 = LD4(rx, off_ax), d = LD4(rx, off_ms); asm volatile("" :: "v"(a), "v"(b), "v"(c2), "v"(d)); }
      } else {
        if (!W7 && MP >= 0) asm volatile("" :: "v"(m0), "v"(m1), "v"(axu), "v"(ms));
        __builtin_amdgcn_raw_buffer_store_b64(vpk, rvtm, st_vtm, 0, 16);
      }
      st_vtm += KVM_B;
    } else {
      if (PT && !W7) asm volatile("" :: "v"(pt));
      wg_barrier();
    }
  }
  drain_barrier();
  if (W7 && l == 0) flag_add(flag_ptr(p, F_CM, NSEG - 1));
}

constexpr int H_P = 0, HP_STR = 1056, H_E = 7 * HP_STR + 32;
DI void helper_wave(const Params& p, const int hid, LAS char* lds, const int w, const int l) {
  const int n16 = l & 15, g = l >> 4, hb = n16 >> 3, i8 = n16 & 7;
  const int h = hid >> 1, hf = hid & 1;
  const __amdgpu_buffer_rsrc_t rmem = __builtin_amdgcn_make_buffer_rsrc(p.ws + WS_MEMIMG, 0, (NSEG + 1) * MEMIMG_B, 0x00020000);
  const __amdgpu_buffer_rsrc_t rx = __builtin_amdgcn_make_buffer_rsrc(p.ws + WS_XCH, 0, NSEG * XCH_B, 0x00020000);
  const __amdgpu_buffer_rsrc_t rk = ws_rsrc(p, WS_KT, (size_t)SEQ * NKT * 2), ru = ws_rsrc(p, WS_UT, (size_t)SEQ * NKT * 2);
  const int wk = w < 7 ? w : 0;
  unsigned off_mem = (wk + 7 * hb) * 512 + g * 128 + i8 * 16;
  const int o0 = hf * 14 + 2 * wk;
  const unsigned kp = (unsigned)((h * 28 + wk) * 64 + l) * 16, up = (unsigned)((h * 28 + o0) * 64 + l) * 16;
  unsigned st_ax = n16 < 8 ? XA_OFF + (o0 * 8 + (h >> 1)) * 512 + (h & 1) * 256 + g * 64 + n16 * 8 : OOB;
  const unsigned inc_ax = n16 < 8 ? XCH_B : 0;
  unsigned st_ms = (l < 8 && hf == 0) ? XS_OFF + (h >> 1) * 128 + ((h & 1) * 8 + l) * 8 : OOB;
  const unsigned inc_ms = (l < 8 && hf == 0) ? XCH_B : 0;
  bf16x8 k00, k01, k10, k11, u[2];
  auto prefetch = [&](int s) {
    flag_wait(flag_ptr(p, F_XG, s), 56);
    const unsigned k = kp + (unsigned)s * (SEG * NKT * 2), q = up + (unsigned)s * (SEG * NKT * 2);
    k00 = LDF(rk, k); k01 = LDF(rk, k + 7 * 1024); k10 = LDF(rk, k + 14 * 1024); k11 = LDF(rk, k + 21 * 1024);
    u[0] = LDF(ru, q); u[1] = LDF(ru, q + 1024);
  };
  if (w < 7) prefetch(0);
  int budget = 1 << 20;
  unsigned tb1 = 0;
  for (int s = 0; s + 1 < NSEG; ++s) {
    if (w < 7) {
      if (s) wait_until(tb1, TH);
      CBAR();
      u32x4 v = LD16(rmem, off_mem);
      if (__builtin_expect(isbad(mx4(v)), 0)) { do { __builtin_amdgcn_s_sleep(1); CBAR(); v = LD16(rmem, off_mem); } while (isbad(mx4(v)) && --budget > 0); }
      off_mem += MEMIMG_B;
      bf16x8 f = __builtin_bit_cast(bf16x8, v);
      asm volatile("" : "+v"(u[0]), "+v"(u[1]));
      f32x4 z = {0, 0, 0, 0};
      f32x4 a0 = MFMA16(k00, f, z), a1 = MFMA16(k01, f, z), b0 = MFMA16(k10, f, z), b1 = MFMA16(k11, f, z);
      f32x4 r;
#pragma unroll
      for (int q = 0; q < 4; ++q) { float own = hb ? b1[q] : a0[q]; float oth = hb ? a1[q] : b0[q]; r[q] = own + dpp_f<0x128>(oth); }
      *(LAS f32x4*)(lds + H_P + w * HP_STR + l * 16) = r;
    }
    wg_barrier();
    tb1 = now32();
    if (w == 0 || w == 7) {
      f32x4 t[7];
#pragma unroll
      for (int k = 0; k < 7; ++k) t[k] = *(LAS f32x4*)(lds + H_P + k * HP_STR + l * 16);
      f32x4 sc = ((t[0] + t[1]) + (t[2] + t[3])) + ((t[4] + t[5]) + t[6]);
      float mx = fmaxf(fmax3(sc[0], sc[1], sc[2]), sc[3]);
      mx = fmaxf(mx, dpp_f<0x128>(mx)); mx = x32_max(mx); mx = x16_max(mx);
      float e0 = __builtin_amdgcn_exp2f(sc[0] - mx), e1 = __builtin_amdgcn_exp2f(sc[1] - mx), e2 = __builtin_amdgcn_exp2f(sc[2] - mx), e3 = __builtin_amdgcn_exp2f(sc[3] - mx);
      if (w == 0) {
        *(LAS u32x2*)(lds + H_E + (g * 16 + i8) * 16 + hb * 8) = (u32x2){cvt_pk(e0, e1), cvt_pk(e2, e3)};
        wg_barrier();
      } else {
        wg_barrier();
        float sm = (e0 + e1) + (e2 + e3);
        sm += dpp_f<0x128>(sm); sm = x32_sum(sm); sm = x16_sum(sm);
        ST8(((u32x2){min(f2u(mx), 0xfffffffeu), min(f2u(sm), 0xfffffffeu)}), rx, st_ms);
        st_ms += inc_ms;
      }
    } else {
      wg_barrier();
    }
    if (w < 7) {
      bf16x8 pe = *(LAS bf16x8*)(lds + H_E + (g * 16 + i8) * 16);
      f32x4 z = {0, 0, 0, 0};
      f32x4 d0 = MFMA16(u[0], pe, z), d1 = MFMA16(u[1], pe, z);
      u32x2 s0 = {min(cvt_pk(d0[0], d0[1]), 0xfffffffeu), min(cvt_pk(d0[2], d0[3]), 0xfffffffeu)};
      u32x2 s1 = {min(cvt_pk(d1[0], d1[1]), 0xfffffffeu), min(cvt_pk(d1[2], d1[3]), 0xfffffffeu)};
      ST8(s0, rx, st_ax);
      ST8(s1, rx, n16 < 8 ? st_ax + 4096 : OOB);
    }
    st_ax += inc_ax;
    CBAR();
    if (w < 7) prefetch(s + 1);
  }
}

constexpr int HS_STR = 452 * 4, L_HS = 12288, L_ST = L_HS + 8 * HS_STR;
DI void comb_wg(const Params& p, LAS char* lds, int s, int hg, int w, int l) {
  const int tid = w * 64 + l, rt = hg >> 1, half = hg & 1;
  const __amdgpu_buffer_rsrc_t rpo = ws_rsrc(p, WS_PO, (size_t)4 * SEQ * DM * 4), rh = ws_rsrc(p, WS_H, (size_t)SEQ * DM * 4), rhn = ws_rsrc(p, WS_HN, (size_t)SEQ * DM * 2);
  u32x4 pv[2][4]; u32x2 xv[2]; unsigned off[2];
#pragma unroll
  for (int j = 0; j < 2; ++j) {
    const int idx = j * 512 + tid, o = idx >> 5, g = (idx >> 3) & 3, i8 = idx & 7;
    const bool ok = idx < 896;
    off[j] = ok ? (unsigned)((((s * 2 + rt) * 28 + o) * 64 + g * 16 + half * 8 + i8) * 16) : OOB;
#pragma unroll
    for (int k = 0; k < 4; ++k) pv[j][k] = LD16(rpo, ok ? off[j] + k * (SEQ * DM * 4) : OOB);
    xv[j] = ok ? *(const u32x2*)(p.x + (size_t)(s * SEG + hg * 8 + i8) * DM + o * 16 + g * 4) : (u32x2){0, 0};
  }
#pragma unroll
  for (int j = 0; j < 2; ++j) {
    const int idx = j * 512 + tid, o = idx >> 5, g = (idx >> 3) & 3, i8 = idx & 7;
    f32x4 a = (__builtin_bit_cast(f32x4, pv[j][0]) + __builtin_bit_cast(f32x4, pv[j][1])) + (__builtin_bit_cast(f32x4, pv[j][2]) + __builtin_bit_cast(f32x4, pv[j][3]));
    a += (f32x4){blo(xv[j].x), bhi(xv[j].x), blo(xv[j].y), bhi(xv[j].y)};
    ST16(__builtin_bit_cast(u32x4, a), rh, off[j]);
    if (idx < 896) *(LAS f32x4*)(lds + L_HS + i8 * HS_STR + (o * 16 + g * 4) * 4) = a;
  }
  wg_barrier();
  {
    float v[7], sm = 0.f;
#pragma unroll
    for (int j = 0; j < 7; ++j) { v[j] = *(LAS float*)(lds + L_HS + w * HS_STR + (j * 64 + l) * 4); sm += v[j]; }
    const float mean = wave_sum(sm) * (1.0f / DM);
    float q = 0.f;
#pragma unroll
    for (int j = 0; j < 7; ++j) { float d = v[j] - mean; q += d * d; }
    const float rstd = 1.0f / sqrtf(wave_sum(q) * (1.0f / DM) + 1e-5f);
    if (l == 0) { *(LAS float*)(lds + L_ST + w * 8) = mean; *(LAS float*)(lds + L_ST + w * 8 + 4) = rstd; }
  }
  wg_barrier();
  if (tid < 448) {
    const int c8 = tid >> 3, i8 = tid & 7;
    const float mean = *(LAS float*)(lds + L_ST + i8 * 8), rstd = *(LAS float*)(lds + L_ST + i8 * 8 + 4);
    const f32x4 a = *(LAS f32x4*)(lds + L_HS + i8 * HS_STR + c8 * 32), b = *(LAS f32x4*)(lds + L_HS + i8 * HS_STR + c8 * 32 + 16);
    ST16(((u32x4){cvt_pk((a[0] - mean) * rstd, (a[1] - mean) * rstd), cvt_pk((a[2] - mean) * rstd, (a[3] - mean) * rstd),
                  cvt_pk((b[0] - mean) * rstd, (b[1] - mean) * rstd), cvt_pk((b[2] - mean) * rstd, (b[3] - mean) * rstd)}), rhn, tiled_off(s * SEG + hg * 8 + i8, c8));
  }
}

DI void att_wg(const Params& p, LAS char* lds, int s, int hg, int w, int l) {
  const int n16 = l & 15, g = l >> 4;
  const int hl = w >> 1, qt = w & 1, h = hg * 4 + hl;
  const __amdgpu_buffer_rsrc_t rk = ws_rsrc(p, WS_KT, (size_t)SEQ * NKT * 2), ru = ws_rsrc(p, WS_UT, (size_t)SEQ * NKT * 2);
  const __amdgpu_buffer_rsrc_t rkm = ws_rsrc(p, WS_KTM, (size_t)NSEG * KVM_B), rvm = ws_rsrc(p, WS_VTM, (size_t)NSEG * KVM_B);
  const __amdgpu_buffer_rsrc_t rpo = ws_rsrc(p, WS_PO, (size_t)4 * SEQ * DM * 4);
  const __amdgpu_buffer_rsrc_t rxf = ws_rsrc(p, WS_XF, (size_t)SEQ * DM * 2);
  bf16x8 q[14];
#pragma unroll
  for (int ks = 0; ks < 14; ++ks) q[ks] = LDF(rxf, (unsigned)(((s * 2 + qt) * 14 + ks) * 64 + l) * 16);
  flag_wait(flag_ptr(p, F_XG, s), 56);
  const unsigned ko = (unsigned)(((s * NH + h) * 28) * 64 + l) * 16;
  f32x4 s0 = {0, 0, 0, 0}, s1 = s0, sm = s0;
  {
    bf16x8 k0[14], k1[14];
#pragma unroll
    for (int ks = 0; ks < 14; ++ks) { k0[ks] = LDF(rk, ko + ks * 1024); k1[ks] = LDF(rk, qt ? ko + (14 + ks) * 1024 : OOB); }
    SBAR();
#pragma unroll
    for (int ks = 0; ks < 14; ++ks) { s0 = MFMA16(k0[ks], q[ks], s0); s1 = MFMA16(k1[ks], q[ks], s1); }
  }
  bf16x8 uv[4][4];
  const unsigned uo = (unsigned)(((s * NH + hg * 4) * 28 + w) * 64 + l) * 16;
#pragma unroll
  for (int j = 0; j < 4; ++j)
#pragma unroll
    for (int k = 0; k < 4; ++k) uv[j][k] = LDF(ru, uo + (k * 28 + ((j < 3 || w < 4) ? j * 8 : 0)) * 1024);
  flag_wait(flag_ptr(p, F_CM, s), 32);
  const unsigned kmo = (unsigned)s * KVM_B + (unsigned)(((h >> 1) * 14) * 64 + l) * 16;
  bf16x8 vm[4];
#pragma unroll
  for (int j = 0; j < 4; ++j) {
    const unsigned vo = (unsigned)s * KVM_B + (unsigned)((2 * hg * 28 + w + ((j < 3 || w < 4) ? j * 8 : 0)) * 64 + l) * 8;
    const u32x2 a = LD8(rvm, vo), b = LD8(rvm, vo + 28 * 512);
    vm[j] = __builtin_bit_cast(bf16x8, (u32x4){a.x, a.y, b.x, b.y});
  }
  {
    bf16x8 km[14];
#pragma unroll
    for (int ks = 0; ks < 14; ++ks) km[ks] = LDF(rkm, kmo + ks * 1024);
    SBAR();
#pragma unroll
    for (int ks = 0; ks < 14; ++ks) sm = MFMA16(km[ks], q[ks], sm);
  }
  const float NEG = -3.0e38f;
  const bool ownm = (g >> 1) == (h & 1);
  float v[12];
#pragma unroll
  for (int r = 0; r < 4; ++r) {
    v[r] = (qt || (4 * g + r) <= n16) ? s0[r] : NEG;
    v[4 + r] = (qt && (4 * g + r) <= n16) ? s1[r] : NEG;
    v[8 + r] = ownm ? sm[r] : NEG;
  }
  float mx = NEG;
#pragma unroll
  for (int r = 0; r < 12; ++r) mx = fmaxf(mx, v[r]);
  mx = x16_max(mx); mx = x32_max(mx);
  float sum = 0.f;
#pragma unroll
  for (int r = 0; r < 12; ++r) { v[r] = v[r] > -1.0e38f ? __builtin_amdgcn_exp2f(v[r] - mx) : 0.f; sum += v[r]; }
  sum = x16_sum(sum); sum = x32_sum(sum);
  const float rinv = 1.0f / sum;
  u32x4 px = {cvt_pk(v[0] * rinv, v[1] * rinv), cvt_pk(v[2] * rinv, v[3] * rinv), cvt_pk(v[4] * rinv, v[5] * rinv), cvt_pk(v[6] * rinv, v[7] * rinv)};
  *(LAS u32x4*)(lds + (hl * 2 + qt) * 1024 + l * 16) = px;
  if (ownm) *(LAS u32x2*)(lds + 8192 + qt * 1024 + l * 16 + (hl >> 1) * 8) = (u32x2){cvt_pk(v[8] * rinv, v[9] * rinv), cvt_pk(v[10] * rinv, v[11] * rinv)};
  wg_barrier();
  bf16x8 P[2][5];
#pragma unroll
  for (int t = 0; t < 2; ++t) {
#pragma unroll
    for (int k = 0; k < 4; ++k) P[t][k] = *(LAS bf16x8*)(lds + (k * 2 + t) * 1024 + l * 16);
    P[t][4] = *(LAS bf16x8*)(lds + 8192 + t * 1024 + l * 16);
  }
  const unsigned po = (unsigned)((((hg * NSEG + s) * 2) * 28 + w) * 64 + l) * 16;
#pragma unroll
  for (int j = 0; j < 4; ++j) {
    f32x4 d0 = {0, 0, 0, 0}, d1 = d0;
#pragma unroll
    for (int k = 0; k < 4; ++k) { d0 = MFMA16(uv[j][k], P[0][k], d0); d1 = MFMA16(uv[j][k], P[1][k], d1); }
    d0 = MFMA16(vm[j], P[0][4], d0); d1 = MFMA16(vm[j], P[1][4], d1);
    if (j < 3 || w < 4) { ST16(__builtin_bit_cast(u32x4, d0), rpo, po + j * 8192); ST16(__builtin_bit_cast(u32x4, d1), rpo, po + j * 8192 + 28 * 1024); }
  }
  drain_barrier();
  if (w == 0) {
    if (l == 0) flag_add(flag_ptr(p, F_AT, s));
    flag_wait(flag_ptr(p, F_AT, s), 4);
  }
  wg_barrier();
  comb_wg(p, lds, s, hg, w, l);
  drain_barrier();
  if (w == 0 && l == 0) flag_add(flag_ptr(p, F_HN, s));
}

constexpr int L_GW = 57344, G1_STR = 72, G2_STR = 40;
DI void glu1_stage(const Params& p, LAS char* lds, int fb, int tid) {
#pragma unroll
  for (int i = 0; i < 7; ++i) {
    const int c = i * 512 + tid, row = c >> 2, gu = row >= 448, d = row - 448 * gu;
    u32x4 v = *(const u32x4*)((gu ? p.wu : p.wg) + (size_t)d * FF + fb * 32 + (c & 3) * 8);
    LAS char* dst = lds + L_GW + row * G1_STR + (c & 3) * 16;
    *(LAS u32x2*)dst = (u32x2){v.x, v.y}; *(LAS u32x2*)(dst + 8) = (u32x2){v.z, v.w};
  }
}
DI void glu2_stage(const Params& p, LAS char* lds, int o, int tid) {
#pragma unroll
  for (int i = 0; i < 7; ++i) {
    const int c = i * 512 + tid, row = c >> 1;
    u32x4 v = *(const u32x4*)(p.wd + (size_t)row * DM + o * 16 + (c & 1) * 8);
    LAS char* dst = lds + L_GW + row * G2_STR + (c & 1) * 16;
    *(LAS u32x2*)dst = (u32x2){v.x, v.y}; *(LAS u32x2*)(dst + 8) = (u32x2){v.z, v.w};
  }
}
DI bf16x8 tr_frag(LAS char* b, int str) {
  s16x4 lo = __builtin_amdgcn_ds_read_tr16_b64_v4i16((LAS s16x4*)b);
  s16x4 hi = __builtin_amdgcn_ds_read_tr16_b64_v4i16((LAS s16x4*)(b + 4 * str));
  u32x2 l2 = __builtin_bit_cast(u32x2, lo), h2 = __builtin_bit_cast(u32x2, hi);
  return __builtin_bit_cast(bf16x8, (u32x4){l2.x, l2.y, h2.x, h2.y});
}
DI void glu1_wg(const Params& p, LAS char* lds, int fb, int w, int l) {
  const int ftl = w & 1, rt = (w >> 1) & 1, sp = w >> 2;
  const __amdgpu_buffer_rsrc_t rhn = ws_rsrc(p, WS_HN, (size_t)SEQ * DM * 2), rhg = ws_rsrc(p, WS_HG, (size_t)SEQ * FF * 2);
  bf16x8 wgt[14], wup[14];
#pragma unroll
  for (int ks = 0; ks < 14; ++ks) {
    LAS char* b = lds + L_GW + (32 * ks + 8 * (l >> 4) + ((l & 15) >> 2)) * G1_STR + ftl * 32 + (l & 3) * 8;
    wgt[ks] = tr_frag(b, G1_STR); wup[ks] = tr_frag(b + 448 * G1_STR, G1_STR);
  }
  const int n16 = l & 15, g = l >> 4;
  if (w == 0) *(LAS unsigned*)(lds + l * 4) = 0u;
  wg_barrier();
  for (int s = sp; s < NSEG; s += 2) {
    flag_wait(flag_ptr(p, F_HN, s), 4);
    const unsigned ho = (unsigned)(((s * 2 + rt) * 14) * 64 + l) * 16;
    bf16x8 x[14];
#pragma unroll
    for (int ks = 0; ks < 14; ++ks) x[ks] = LDF(rhn, ho + ks * 1024);
    SBAR();
    f32x4 ag = {0, 0, 0, 0}, au = ag;
#pragma unroll
    for (int ks = 0; ks < 14; ++ks) { ag = MFMA16(wgt[ks], x[ks], ag); au = MFMA16(wup[ks], x[ks], au); }
    u32x2 o = {cvt_pk(ag[0] * au[0], ag[1] * au[1]), cvt_pk(ag[2] * au[2], ag[3] * au[3])};
    ST8(o, rhg, (unsigned)(((((s * 2 + rt) * 56 + fb) * 64 + (2 * ftl + (g >> 1)) * 16 + n16) * 8 + 4 * (g & 1)) * 2));
    asm volatile("s_waitcnt vmcnt(0)" ::: "memory");
    if (l == 0) {
      const unsigned t = __hip_atomic_fetch_add((LAS unsigned*)(lds + s * 4), 1u, __ATOMIC_RELAXED, __HIP_MEMORY_SCOPE_WORKGROUP);
      if (t == 3) flag_add(flag_ptr(p, F_G1, s));
    }
  }
}

DI void glu2_wg(const Params& p, LAS char* lds, int o, int s0, int w, int l) {
  const __amdgpu_buffer_rsrc_t rhg = ws_rsrc(p, WS_HG, (size_t)SEQ * FF * 2), rh = ws_rsrc(p, WS_H, (size_t)SEQ * DM * 4);
  bf16x8 wd[7];
#pragma unroll
  for (int k = 0; k < 7; ++k) wd[k] = tr_frag(lds + L_GW + (32 * (7 * w + k) + 8 * (l >> 4) + ((l & 15) >> 2)) * G2_STR + (l & 3) * 8, G2_STR);
  const int tid = w * 64 + l;
  int buf = 0;
  for (int s = s0; s < NSEG; s += 2) {
    if (w == 0) flag_wait(flag_ptr(p, F_G1, s), 56);
    wg_barrier();
    const unsigned ho = (unsigned)(((s * 2) * 56 + 7 * w) * 64 + l) * 16;
    bf16x8 a[7], b[7];
#pragma unroll
    for (int k = 0; k < 7; ++k) { a[k] = LDF(rhg, ho + k * 1024); b[k] = LDF(rhg, ho + (56 + k) * 1024); }
    const int row = s * SEG + ((tid >> 6) & 1) * 16 + (tid & 15), col = o * 16 + 4 * ((tid >> 4) & 3);
    SBAR();
    u32x4 hv = {0, 0, 0, 0};
    if (tid < 128) hv = LD16(rh, (unsigned)(((s * 2 + (tid >> 6)) * 28 + o) * 64 + (tid & 63)) * 16);
    f32x4 d0 = {0, 0, 0, 0}, d1 = d0;
#pragma unroll
    for (int k = 0; k < 7; ++k) { d0 = MFMA16(wd[k], a[k], d0); d1 = MFMA16(wd[k], b[k], d1); }
    LAS char* L = lds + buf * 16384;
    *(LAS f32x4*)(L + w * 2048 + l * 16) = d0;
    *(LAS f32x4*)(L + w * 2048 + 1024 + l * 16) = d1;
    wg_barrier();
    if (tid < 128) {
      f32x4 acc = __builtin_bit_cast(f32x4, hv);
#pragma unroll
      for (int k = 0; k < 8; ++k) acc += *(LAS f32x4*)(L + k * 2048 + tid * 16);
      *(u32x2*)(p.out + (size_t)row * DM + col) = (u32x2){cvt_pk(acc[0], acc[1]), cvt_pk(acc[2], acc[3])};
    }
    buf ^= 1;
  }
}

__global__ void __launch_bounds__(512, 2) k_main(Params p) {
  extern __shared__ __attribute__((aligned(16))) char smem[];
  LAS char* lds = (LAS char*)smem;
  const int xg = blockIdx.x & 7, r = blockIdx.x >> 3;
  const int w = __builtin_amdgcn_readfirstlane(threadIdx.x >> 6), l = threadIdx.x & 63;
  pre_phase(p, lds, blockIdx.x, w, l);
  if (xg == 0) {
    const int cw = r;
    if (cw < 28) { if (w == 7) chain_wave<true, true>(p, cw, lds, w, l); else chain_wave<false, true>(p, cw, lds, w, l); }
    else { if (w == 7) chain_wave<true, false>(p, cw, lds, w, l); else chain_wave<false, false>(p, cw, lds, w, l); }
    return;
  }
  if (xg == 1) { helper_wave(p, r, lds, w, l); return; }
  const int wi = (xg - 2) * 32 + r;
  if (wi < 168) { const unsigned t0 = now32(); wait_until(t0, (wi / 56) * XSTAG); xgemm_wg(p, lds, wi % 56, wi / 56, 3, w, l); }
  if (wi < 56) glu1_stage(p, lds, wi, threadIdx.x); else if (wi < 112) glu2_stage(p, lds, (wi - 56) % 28, threadIdx.x);
  wg_barrier();
  if (wi < 56) { glu1_wg(p, lds, wi, w, l); return; }
  if (wi < 112) {
    glu2_wg(p, lds, (wi - 56) % 28, (wi - 56) / 28, w, l);
    return;
  }
  for (;;) {
    if (threadIdx.x == 0) *(LAS unsigned*)(lds + 10244) = __hip_atomic_fetch_add(flag_ptr(p, F_Q, 0), 1u, __ATOMIC_RELAXED, __HIP_MEMORY_SCOPE_AGENT);
    wg_barrier();
    const unsigned t = __builtin_amdgcn_readfirstlane(*(LAS unsigned*)(lds + 10244));
    if (t >= NSEG * 4) break;
    att_wg(p, lds, t >> 2, t & 3, w, l);
  }
}

extern "C" void kernel_launch(void* const* d_in, const int* in_sizes, int n_in,
                              void* d_out, int out_size, void* d_ws, size_t ws_size,
                              hipStream_t stream) {
  Params p;
  p.x = (const uint16_t*)d_in[0]; p.mem0 = (const uint16_t*)d_in[1];
  p.Wq = (const uint16_t*)d_in[2]; p.Wk = (const uint16_t*)d_in[3]; p.Wv = (const uint16_t*)d_in[4]; p.Wo = (const uint16_t*)d_in[5];
  p.wg = (const uint16_t*)d_in[8]; p.wu = (const uint16_t*)d_in[9]; p.wd = (const uint16_t*)d_in[10];
  p.out = (uint16_t*)d_out; p.ws = (char*)d_ws;
  (void)hipFuncSetAttribute((const void*)k_main, hipFuncAttributeMaxDynamicSharedMemorySize, CHAIN_LDS + 1024);
  (void)hipMemsetAsync(p.ws + WS_FLG, 0, WS_END - WS_FLG, stream);
  k_main<<<256, 512, CHAIN_LDS + 1024, stream>>>(p);
}
```
